```python
import torch

def _softmax(x, axis=-1):
    return torch.softmax(x, dim=axis)

def _gelu(x):
    return torch.nn.functional.gelu(x, approximate='tanh')

def _silu(x):
    return torch.nn.functional.silu(x)

def _sigmoid(x):
    return torch.sigmoid(x)

def _layernorm(x, axis=-1, eps=1e-05):
    m = x.mean(dim=axis, keepdim=True)
    v = x.var(dim=axis, keepdim=True, unbiased=False)
    return (x - m) / torch.sqrt(v + eps)

def _scatter_add(tgt, idx, src, axis=0):
    return tgt.clone().index_add_(axis, idx, src)

def _segment_sum(src, seg, n):
    out = torch.zeros((n,) + src.shape[1:], dtype=src.dtype)
    return out.index_add_(0, seg, src)

def _pad(x, pw):
    flat = tuple((v for pair in reversed(pw) for v in pair))
    return torch.nn.functional.pad(x, flat)
kernel_name = 'concat_heads__memory_aug_recurrent_mem_tr__linear_attn_block__prompt'

def setup_inputs():

    _w = lambda a: (lambda t: t.to(torch.bfloat16) if t.is_floating_point() else t)(torch.as_tensor(a))

    def _A_setup():
        B, S, Dm = (1, 2048, 448)
        H, Dh = (16, 256)
        M, seg = (8, 32)
        seg = min(seg, S)
        S = S // seg * seg
        return {'x': _w((lambda *s: torch.randn(*s, dtype=torch.bfloat16) * 0.05)(B, S, Dm)), 'mem0': _w((lambda *s: torch.randn(*s, dtype=torch.bfloat16) * 0.8)(B, M, Dm)), 'Wq': _w((lambda *s: torch.randn(*s, dtype=torch.bfloat16) * 1.6)(Dm, H, Dh) * Dm ** (-0.5)), 'Wk': _w((lambda *s: torch.randn(*s, dtype=torch.bfloat16) * 1.6)(Dm, H, Dh) * Dm ** (-0.5)), 'Wv': _w((lambda *s: torch.randn(*s, dtype=torch.bfloat16) * 0.8)(Dm, H, Dh) * Dm ** (-0.5)), 'Wo': _w((lambda *s: torch.randn(*s, dtype=torch.bfloat16) * 0.8)(H, Dh, Dm) * (H * Dh) ** (-0.5)), 'seg_len': seg}
    _A_in = _A_setup()

    def _B_setup():
        B, S, D = (1, 2048, 448)
        return {'x': _w((lambda *s: torch.randn(*s, dtype=torch.bfloat16) * 0.05)(B, S, D)), 'wqkv': _w((lambda *s: torch.randn(*s, dtype=torch.bfloat16) * 1.6)(D, 3 * D)), 'wo': _w((lambda *s: torch.randn(*s, dtype=torch.bfloat16) * 0.1)(D, D))}
    _B_in = _B_setup()
    return {'A_x': _A_in['x'], 'A_mem0': _A_in['mem0'], 'A_Wq': _A_in['Wq'], 'A_Wk': _A_in['Wk'], 'A_Wv': _A_in['Wv'], 'A_Wo': _A_in['Wo'], 'A_seg_len': _A_in['seg_len'], 'B_x': _B_in['x'], 'B_wqkv': _B_in['wqkv'], 'B_wo': _B_in['wo']}

def reference(A_x, A_mem0, A_Wq, A_Wk, A_Wv, A_Wo, A_seg_len, B_x, B_wqkv, B_wo):

    def _A_ref(x, mem0, Wq, Wk, Wv, Wo, seg_len):
        B, S, Dm = x.shape
        H, Dh = (Wq.shape[1], Wq.shape[2])
        M = mem0.shape[1]
        scale = Dh ** (-0.5)
        n_seg = S // seg_len
        L = M + seg_len
        idx = torch.arange(L)
        mask = (idx[:, None] < M) | (idx[:, None] >= idx[None, :])
        mem = mem0
        outs = []
        for s in range(n_seg):
            xs = x[:, s * seg_len:(s + 1) * seg_len, :].clone()
            h = torch.concatenate([mem, xs], axis=1)
            q = torch.einsum('btd,dhe->bhte', h, Wq)
            k = torch.einsum('btd,dhe->bhte', h, Wk)
            v = torch.einsum('btd,dhe->bhte', h, Wv)
            scores = torch.einsum('bhqe,bhke->bhqk', q, k) * scale
            scores = torch.where(mask[None, None], scores, -1000000000.0)
            p = _softmax(scores, axis=-1)
            o = torch.einsum('bhqk,bhke->bhqe', p, v)
            o = torch.einsum('bhte,hed->btd', o, Wo)
            mem = o[:, :M, :].clone()
            outs.append(o[:, M:, :])
        return torch.concatenate(outs, axis=1)

    def _B_ref(x, wqkv, wo):
        B, S, D = x.shape
        H = 16
        Dh = D // H
        qkv = (lambda x, *d: x.permute(*d))((x @ wqkv).reshape(B, S, 3, H, Dh), 2, 0, 3, 1, 4)
        q, k, v = (qkv[0], qkv[1], qkv[2])
        phi = lambda t: torch.where(t > 0, t + 1, torch.exp(t)) + 1e-06
        pq, pk = (phi(q), phi(k))
        kv = torch.einsum('bhsd,bhse->bhde', pk, v)
        z = torch.einsum('bhsd->bhd', pk)
        num = torch.einsum('bhqd,bhde->bhqe', pq, kv)
        den = torch.einsum('bhqd,bhd->bhq', pq, z)[..., None] + 1e-06
        o = num / den
        return (lambda x, *d: x.permute(*d))(o, 0, 2, 1, 3).reshape(B, S, D) @ wo
    a = _A_ref(A_x, A_mem0, A_Wq, A_Wk, A_Wv, A_Wo, A_seg_len)
    b = _B_ref(B_x, B_wqkv, B_wo)
    return torch.concatenate([a, b], axis=-1)
```

```cpp
#include <hip/hip_runtime.h>
#include <hip/hip_bf16.h>
#include <stdint.h>

typedef __attribute__((ext_vector_type(8))) __bf16 bf16x8;
typedef __attribute__((ext_vector_type(4))) short s16x4;
typedef __attribute__((ext_vector_type(2))) __bf16 bf16x2;
typedef __attribute__((ext_vector_type(2))) float f32x2;
typedef __attribute__((ext_vector_type(4))) float f32x4;
typedef __attribute__((ext_vector_type(4))) unsigned u32x4;
typedef __attribute__((ext_vector_type(2))) unsigned u32x2;
typedef unsigned short u16;
#define LAS __attribute__((address_space(3)))
#define DEVI __device__ __forceinline__

constexpr int DM = 448, NH = 16, NSEG = 64, SEGL = 32, OUTW = 896;
constexpr size_t FR = 1024;
constexpr size_t OFF_FLAG = 0;
constexpr size_t SZ_FLAG = 65536;
constexpr size_t OFF_AF = OFF_FLAG + SZ_FLAG;
constexpr size_t SZ_AF = (size_t)16 * 28 * 14 * FR;
constexpr size_t OFF_BF = OFF_AF + SZ_AF;
constexpr size_t OFF_XF = OFF_BF + SZ_AF;
constexpr size_t SZ_XF = (size_t)64 * 28 * FR;
constexpr size_t OFF_GX = OFF_XF + SZ_XF;
constexpr size_t SZ_GX = (size_t)64 * 16 * 28 * FR;
constexpr size_t OFF_UF = OFF_GX + SZ_GX;
constexpr size_t SZ_UF = (size_t)64 * 56 * 8 * FR;
constexpr size_t OFF_XM = OFF_UF + SZ_UF;
constexpr size_t SZ_XM = (size_t)64 * 16 * 7 * 1024;
constexpr size_t OFF_WC = OFF_XM + SZ_XM;
constexpr size_t SZ_WC = (size_t)64 * 56 * 8 * 256;
constexpr size_t OFF_PX = OFF_WC + SZ_WC;
constexpr size_t SZ_PX = (size_t)64 * 16 * 2 * FR;
constexpr size_t OFF_PM = OFF_PX + SZ_PX;
constexpr size_t SZ_PM = (size_t)64 * 8 * 2 * 512;
constexpr size_t OFF_BQ = OFF_PM + SZ_PM;
constexpr size_t SZ_BQ = (size_t)128 * 16 * FR;
constexpr size_t OFF_BW = OFF_BQ + SZ_BQ;
constexpr size_t SZ_BW = (size_t)3 * 16 * 2 * 14 * FR;
constexpr size_t OFF_BX = OFF_BW + SZ_BW;
constexpr size_t SZ_BX = (size_t)128 * 14 * FR;
constexpr size_t OFF_BKV = OFF_BX + SZ_BX;
constexpr size_t SZ_BKV = (size_t)16 * 16 * 4 * 1024;
constexpr size_t OFF_BM = OFF_BKV + SZ_BKV;
constexpr size_t SZ_BM = (size_t)16 * 28 * FR;
constexpr size_t OFF_BZ = OFF_BM + SZ_BM;
constexpr size_t SZ_BZ = 16 * 32 * 4;
constexpr size_t OFF_COMM_RAW = OFF_BZ + SZ_BZ;
constexpr size_t COMM_BYTES = (size_t)4 << 20;
constexpr unsigned POISON = 0xFFFFFFFFu;
constexpr int FLAG_CFG = 0;
constexpr int FLAG_B1 = 32;
constexpr int FLAG_B2 = 64;
constexpr int FLAG_XM = 1024;
constexpr int FLAG_GX = 2048;
constexpr int FLAG_U = 3072;
constexpr int FLAG_P = 4096;
constexpr int FLAG_W = 5120;
constexpr int STATS_OFF = 7168;

struct Params {
    const u16 *x, *mem0, *Wq, *Wk, *Wv, *Wo, *bx, *wqkv, *bwo;
    u16* out;
    char* ws;
    unsigned long long comm_off;
    unsigned long long mode;
    unsigned long long mode2;
};

DEVI unsigned pk2(float a, float b) { f32x2 v = {a, b}; bf16x2 r = __builtin_convertvector(v, bf16x2); return __builtin_bit_cast(unsigned, r); }
DEVI f32x4 mfma32(u32x4 a, u32x4 b, f32x4 c) { return __builtin_amdgcn_mfma_f32_16x16x32_bf16(__builtin_bit_cast(bf16x8, a), __builtin_bit_cast(bf16x8, b), c, 0, 0, 0); }
DEVI f32x4 mfma16(u32x2 a, u32x2 b, f32x4 c) { return __builtin_amdgcn_mfma_f32_16x16x16bf16_1k(__builtin_bit_cast(s16x4, a), __builtin_bit_cast(s16x4, b), c, 0, 0, 0); }
DEVI u32x4 pack8(f32x4 a, f32x4 b) { u32x4 r; r.x = pk2(a.x, a.y); r.y = pk2(a.z, a.w); r.z = pk2(b.x, b.y); r.w = pk2(b.z, b.w); return r; }
template <int CTRL> DEVI float dppf(float x) { return __uint_as_float((unsigned)__builtin_amdgcn_update_dpp(0, (int)__float_as_uint(x), CTRL, 0xf, 0xf, false)); }
constexpr int DPP_ROR8 = 0x128, DPP_ROR4 = 0x124, DPP_QP_1032 = 0xB1, DPP_QP_2301 = 0x4E;
DEVI void swap16(float x, float& a, float& b) { auto s = __builtin_amdgcn_permlane16_swap(__float_as_uint(x), __float_as_uint(x), false, false); a = __uint_as_float(s[0]); b = __uint_as_float(s[1]); }
DEVI void swap32(float x, float& a, float& b) { auto s = __builtin_amdgcn_permlane32_swap(__float_as_uint(x), __float_as_uint(x), false, false); a = __uint_as_float(s[0]); b = __uint_as_float(s[1]); }
DEVI void uswap16(unsigned x, unsigned& a, unsigned& b) { auto s = __builtin_amdgcn_permlane16_swap(x, x, false, false); a = s[0]; b = s[1]; }
DEVI void uswap32(unsigned x, unsigned& a, unsigned& b) { auto s = __builtin_amdgcn_permlane32_swap(x, x, false, false); a = s[0]; b = s[1]; }
template <int CTRL> DEVI unsigned dppu(unsigned x) { return (unsigned)__builtin_amdgcn_update_dpp(0, (int)x, CTRL, 0xf, 0xf, false); }
constexpr int DPP_SHL4 = 0x104, DPP_SHL8 = 0x108;
DEVI float bf_lo(unsigned v) { return __uint_as_float(v << 16); }
DEVI float bf_hi(unsigned v) { return __uint_as_float(v & 0xFFFF0000u); }
DEVI int kcol(int t, int q4) { return 64 * (t >> 1) + 16 * q4 + 8 * (t & 1); }
DEVI unsigned nopoison(unsigned v) { return v < 0xFFFFFFFEu ? v : 0xFFFFFFFEu; }
DEVI unsigned max4(u32x4 v) { unsigned a = v.x > v.y ? v.x : v.y, b = v.z > v.w ? v.z : v.w; return a > b ? a : b; }

typedef __amdgpu_buffer_rsrc_t rsrc_t;
DEVI rsrc_t mkrsrc(const void* p, unsigned bytes) { return __builtin_amdgcn_make_buffer_rsrc((void*)p, 0, bytes, 0x00020000); }
DEVI u32x4 bld4(rsrc_t r, unsigned v, unsigned s) { return __builtin_amdgcn_raw_buffer_load_b128(r, v, s, 16); }
DEVI u32x2 bld2(rsrc_t r, unsigned v, unsigned s) { return __builtin_amdgcn_raw_buffer_load_b64(r, v, s, 16); }
DEVI unsigned bld1(rsrc_t r, unsigned v, unsigned s) { return __builtin_amdgcn_raw_buffer_load_b32(r, v, s, 16); }
DEVI u32x4 bld4p(rsrc_t r, unsigned v, unsigned s) { return __builtin_amdgcn_raw_buffer_load_b128(r, v, s, 0); }
DEVI void bst4(rsrc_t r, u32x4 d, unsigned v, unsigned s) { __builtin_amdgcn_raw_buffer_store_b128(d, r, v, s, 16); }
DEVI void bst2(rsrc_t r, u32x2 d, unsigned v, unsigned s) { __builtin_amdgcn_raw_buffer_store_b64(d, r, v, s, 16); }
DEVI void bst1(rsrc_t r, unsigned d, unsigned v, unsigned s) { __builtin_amdgcn_raw_buffer_store_b32(d, r, v, s, 16); }
#define MEMFENCE() asm volatile("" ::: "memory")
#define WAITVM0() do { asm volatile("s_waitcnt vmcnt(0)" ::: "memory"); __builtin_amdgcn_s_waitcnt(0x0F70); } while (0)
DEVI void bar() { asm volatile("s_waitcnt lgkmcnt(0)\n\ts_barrier" ::: "memory"); }
constexpr unsigned OOB = 0xF0000000u;
DEVI u32x4 mkdesc(const void* p, unsigned bytes) {
    const unsigned long long a = (unsigned long long)p;
    u32x4 d; d.x = __builtin_amdgcn_readfirstlane((unsigned)a); d.y = __builtin_amdgcn_readfirstlane((unsigned)(a >> 32)) & 0xFFFFu; d.z = bytes; d.w = 0x00020000u; return d;
}
#define ALD4(dst, vo, ds, so) asm volatile("buffer_load_dwordx4 %0, %1, %2, %3 offen sc1" : "=v"(dst) : "v"(vo), "s"(ds), "s"(so) : "memory")
#define ALD4R(dst, vo, ds, so) asm volatile("buffer_load_dwordx4 %0, %1, %2, %3 offen sc1" : "+v"(dst) : "v"(vo), "s"(ds), "s"(so) : "memory")
#define ALD4PR(dst, vo, ds, so) asm volatile("buffer_load_dwordx4 %0, %1, %2, %3 offen" : "+v"(dst) : "v"(vo), "s"(ds), "s"(so) : "memory")
#define ALD1(dst, vo, ds, so) asm volatile("buffer_load_dword %0, %1, %2, %3 offen sc1" : "=v"(dst) : "v"(vo), "s"(ds), "s"(so) : "memory")
#define ALD1R(dst, vo, ds, so) asm volatile("buffer_load_dword %0, %1, %2, %3 offen sc1" : "+v"(dst) : "v"(vo), "s"(ds), "s"(so) : "memory")
#define AST4(dat, vo, ds, so) asm volatile("buffer_store_dwordx4 %0, %1, %2, %3 offen sc1\n\ts_nop 1" :: "v"(dat), "v"(vo), "s"(ds), "s"(so) : "memory")
#define AST2(dat, vo, ds, so) asm volatile("buffer_store_dwordx2 %0, %1, %2, %3 offen sc1\n\ts_nop 1" :: "v"(dat), "v"(vo), "s"(ds), "s"(so) : "memory")
#define AST1(dat, vo, ds, so) asm volatile("buffer_store_dword %0, %1, %2, %3 offen sc1" :: "v"(dat), "v"(vo), "s"(ds), "s"(so) : "memory")
#define AADD1(vo, ds) asm volatile("buffer_atomic_add %0, %1, %2, 0 offen" :: "v"(1u), "v"(vo), "s"(ds) : "memory")
#define SCHEDB() __builtin_amdgcn_sched_barrier(0)
#define AWAIT(n) asm volatile("s_waitcnt vmcnt(" #n ")" ::: "memory")

__global__ void __launch_bounds__(512, 2) k0_prep(Params p) {
    extern __shared__ __attribute__((aligned(16))) char lds[];
    const int b = blockIdx.x, tid = threadIdx.x, lane = tid & 63, w = __builtin_amdgcn_readfirstlane(tid >> 6);
    const int l15 = lane & 15, q4 = lane >> 4;
    const int mat = b & 1, h = (b >> 1) & 15, sub = b >> 5, th = sub & 1, cg = sub >> 1;
    if (mat == 0) {
        for (int f = w; f < 56; f += 8) {
            const int i = f >> 3, k = f & 7, ct = 7 * cg + i;
            const int c = 64 * (ct >> 2) + 16 * (l15 >> 2) + 4 * (ct & 3) + (l15 & 3);
            const u32x4 v = *(const u32x4*)(p.Wq + ((size_t)c * 16 + h) * 256 + 32 * k + 8 * q4);
            *(u32x4*)(lds + f * 1024 + lane * 16) = v;
        }
    } else {
        for (int n = tid; n < 256 * 14; n += 512) {
            const int e = n / 14, ch = n % 14;
            const u32x4 v = *(const u32x4*)(p.Wo + ((size_t)h * 256 + e) * 448 + 112 * cg + 8 * ch);
            *(u32x4*)(lds + e * 240 + ch * 16) = v;
        }
    }
    u32x4 rw[2][8];
    const int t = 7 * th + (w < 7 ? w : 0);
    {
        const u16* Wr = mat ? p.Wv : p.Wk;
#pragma unroll
        for (int bb = 0; bb < 2; ++bb) {
            const int d = 64 * (t >> 1) + 16 * (l15 >> 2) + 8 * (t & 1) + 4 * bb + (l15 & 3);
#pragma unroll
            for (int k = 0; k < 8; ++k) rw[bb][k] = *(const u32x4*)(Wr + ((size_t)d * 16 + h) * 256 + 32 * k + 8 * q4);
        }
    }
    __syncthreads();
    if (w < 7) {
        for (int i = 0; i < 7; ++i) {
            f32x4 a0 = {0, 0, 0, 0}, a1 = {0, 0, 0, 0};
#pragma unroll
            for (int k = 0; k < 8; ++k) {
                u32x4 cf;
                if (mat == 0) cf = *(const u32x4*)(lds + (i * 8 + k) * 1024 + lane * 16);
                else {
                    const int qq = l15 >> 2, pp = l15 & 3;
                    const int row0 = 32 * k + 8 * q4 + qq;
                    typedef __attribute__((ext_vector_type(4))) short v4s;
                    v4s lo = __builtin_amdgcn_ds_read_tr16_b64_v4i16((LAS v4s*)(lds + row0 * 240 + (16 * i + 4 * pp) * 2));
                    v4s hi = __builtin_amdgcn_ds_read_tr16_b64_v4i16((LAS v4s*)(lds + (row0 + 4) * 240 + (16 * i + 4 * pp) * 2));
                    u32x2 l2 = __builtin_bit_cast(u32x2, lo), h2 = __builtin_bit_cast(u32x2, hi);
                    cf = (u32x4){l2.x, l2.y, h2.x, h2.y};
                }
                a0 = mfma32(rw[0][k], cf, a0);
                a1 = mfma32(rw[1][k], cf, a1);
            }
            const u32x4 o = pack8(a0, a1);
            if (mat == 0) {
                const int ct = 7 * cg + i;
                *(u32x4*)(p.ws + OFF_AF + ((size_t)(h * 28 + ct) * 14 + t) * FR + lane * 16) = o;
            } else {
                const int beta = 14 * cg + 2 * i + (l15 >> 3);
                *(u32x4*)(p.ws + OFF_BF + ((size_t)((h >> 1) * 56 + beta) * 14 + t) * FR + (16 * q4 + 8 * (h & 1) + (l15 & 7)) * 16) = o;
            }
        }
    }
    if (w < 7) {
        const int f = b * 7 + w, s = f / 28, kt = (f / 14) & 1, tt = f % 14;
        *(u32x4*)(p.ws + OFF_XF + (size_t)f * FR + lane * 16) = *(const u32x4*)(p.x + (size_t)(32 * s + 16 * kt + l15) * DM + kcol(tt, q4));
        *(u32x4*)(p.ws + OFF_BX + (size_t)f * FR + lane * 16) = *(const u32x4*)(p.bx + (size_t)(16 * (f / 14) + l15) * DM + kcol(f % 14, q4));
    }
    {
        char* cb = p.ws + p.comm_off + (size_t)b * 16384;
        const u32x4 pz = {POISON, POISON, POISON, POISON};
        u32x4 v0 = pz, v1 = pz;
        *(u32x4*)(cb + tid * 16) = v0;
        *(u32x4*)(cb + 8192 + tid * 16) = v1;
        if (b == 0 && tid < 448) {
            const int beta = tid >> 3, i = tid & 7;
            const u32x4 m = *(const u32x4*)(p.mem0 + i * DM + 8 * beta);
            *(u32x4*)(cb + tid * 16) = m;
            *(u32x4*)(cb + 8192 + tid * 16) = m;
        }
    }
    if (b < 32) *(u32x4*)(p.ws + OFF_FLAG + (size_t)b * 2048 + (tid & 127) * 16) = (u32x4){0, 0, 0, 0};
    {
        const int nf = 3 * 16 * 2 * 14;
        for (int f = b * 8 + w; f < nf; f += 2048) {
            const int tt = f % 14, tile = (f / 14) & 1, hh = (f / 28) & 15, t3 = f / 448;
            const int col = (t3 == 0) ? (8 * (l15 >> 2) + 4 * tile + (l15 & 3)) : (16 * tile + l15);
            u16 e[8];
#pragma unroll
            for (int j = 0; j < 8; ++j) e[j] = (col < 28) ? p.wqkv[(size_t)(kcol(tt, q4) + j) * 1344 + t3 * 448 + hh * 28 + col] : (u16)0;
            u32x4 o = {e[0] | ((unsigned)e[1] << 16), e[2] | ((unsigned)e[3] << 16), e[4] | ((unsigned)e[5] << 16), e[6] | ((unsigned)e[7] << 16)};
            *(u32x4*)(p.ws + OFF_BW + (size_t)f * FR + lane * 16) = o;
        }
    }
}

constexpr int SPIN_MAX = 200000;
DEVI unsigned* flagp(const Params& p, int idx) { return (unsigned*)(p.ws + OFF_FLAG) + idx; }
DEVI void signal(const Params& p, int idx) { __hip_atomic_fetch_add(flagp(p, idx), 1u, __ATOMIC_RELAXED, __HIP_MEMORY_SCOPE_AGENT); }
DEVI unsigned ldflag(const Params& p, int idx) { return __hip_atomic_load(flagp(p, idx), __ATOMIC_RELAXED, __HIP_MEMORY_SCOPE_AGENT); }
DEVI void waitflag(const Params& p, int idx, unsigned target, bool& dead, int sleep = 4) {
    int k = dead ? SPIN_MAX : 0;
    _Pragma("nounroll") for (; k < SPIN_MAX; ++k) {
        const unsigned v = __builtin_amdgcn_readfirstlane(ldflag(p, idx));
        if (v >= target) break;
        __builtin_amdgcn_s_sleep(4);
    }
    if (k >= SPIN_MAX) dead = true;
    MEMFENCE();
}

DEVI void gemm_worker(const Params& p, int u, char* lds) {
    const int tid = threadIdx.x, lane = tid & 63, w = __builtin_amdgcn_readfirstlane(tid >> 6);
    const rsrc_t rs = mkrsrc(p.ws, 0x10000000u);
    const int isU = u >> 6, v = u & 63, par = v & 1;
    u32x4 wt[2][14];
    unsigned vo_out[2];
    int flag_idx, flag_stride;
    {
        const int ww = (w < 7) ? w : 0;
        const char* base;
        if (!isU) {
            const int h = v >> 2, tp = 7 * ((v >> 1) & 1) + ww; base = p.ws + OFF_AF + (size_t)(h * 28 + 2 * tp) * 14 * FR;
            vo_out[0] = (unsigned)(OFF_GX + ((size_t)(h * 2 + 0) * 14 + tp) * FR + lane * 16);
            vo_out[1] = (unsigned)(OFF_GX + ((size_t)(h * 2 + 1) * 14 + tp) * FR + lane * 16);
            flag_idx = FLAG_GX + h; flag_stride = 16;
        } else {
            const int hp = v >> 3, b0 = 14 * ((v >> 1) & 3) + 2 * ww; base = p.ws + OFF_BF + (size_t)(hp * 56 + b0) * 14 * FR;
            vo_out[0] = (unsigned)(OFF_UF + ((size_t)(b0 + 0) * 8 + hp) * FR + lane * 16);
            vo_out[1] = (unsigned)(OFF_UF + ((size_t)(b0 + 1) * 8 + hp) * FR + lane * 16);
            flag_idx = FLAG_U + ((v >> 1) & 3); flag_stride = 16;
        }
#pragma unroll
        for (int a = 0; a < 2; ++a)
#pragma unroll
            for (int t = 0; t < 14; ++t) wt[a][t] = *(const u32x4*)(base + (size_t)(a * 14 + t) * FR + lane * 16);
    }
    WAITVM0();
    constexpr unsigned SEG_STRIDE = 16 * 28 * 1024;
    int buf = 0;
    for (int f = w; f < 28; f += 8) *(u32x4*)(lds + f * 1024 + lane * 16) = *(const u32x4*)(p.ws + OFF_XF + ((size_t)par * 28 + f) * FR + lane * 16);
    bar();
    for (int s = par; s < NSEG; s += 2) {
        u32x4 nx[4];
        const bool more = (s + 2 < NSEG);
        if (more) {
#pragma unroll
            for (int j = 0; j < 4; ++j) { const int f = (w + 8 * j < 28) ? w + 8 * j : w; nx[j] = *(const u32x4*)(p.ws + OFF_XF + ((size_t)(s + 2) * 28 + f) * FR + lane * 16); }
        }
        MEMFENCE();
        if (w < 7) {
            f32x4 acc[2][2];
#pragma unroll
            for (int a = 0; a < 2; ++a)
#pragma unroll
                for (int kt = 0; kt < 2; ++kt) acc[a][kt] = (f32x4){0, 0, 0, 0};
            const char* xb = lds + buf * 28672 + lane * 16;
            if (!isU) {
#pragma unroll
                for (int t = 0; t < 14; ++t)
#pragma unroll
                    for (int kt = 0; kt < 2; ++kt) {
                        const u32x4 xf = *(const u32x4*)(xb + (kt * 14 + t) * 1024);
                        acc[0][kt] = mfma32(wt[0][t], xf, acc[0][kt]); acc[1][kt] = mfma32(wt[1][t], xf, acc[1][kt]);
                    }
            } else {
#pragma unroll
                for (int t = 0; t < 14; ++t)
#pragma unroll
                    for (int kt = 0; kt < 2; ++kt) {
                        const u32x4 xf = *(const u32x4*)(xb + (kt * 14 + t) * 1024);
                        acc[0][kt] = mfma32(xf, wt[0][t], acc[0][kt]); acc[1][kt] = mfma32(xf, wt[1][t], acc[1][kt]);
                    }
            }
            WAITVM0();
            u32x4 o0, o1;
            if (!isU) { o0 = pack8(acc[0][0], acc[1][0]); o1 = pack8(acc[0][1], acc[1][1]); }
            else { o0 = pack8(acc[0][0], acc[0][1]); o1 = pack8(acc[1][0], acc[1][1]); }
            bst4(rs, o0, vo_out[0], (unsigned)s * SEG_STRIDE);
            bst4(rs, o1, vo_out[1], (unsigned)s * SEG_STRIDE);
        }
        if (more) {
#pragma unroll
            for (int j = 0; j < 4; ++j) { const int f = w + 8 * j; if (f < 28) *(u32x4*)(lds + (buf ^ 1) * 28672 + f * 1024 + lane * 16) = nx[j]; }
        }
        bar();
        if (s >= 2 && tid == 448) signal(p, flag_idx + (s - 2) * flag_stride);
        buf ^= 1;
    }
    WAITVM0();
    bar();
    if (tid == 448) signal(p, flag_idx + (NSEG - 2 + par) * flag_stride);
}

constexpr float SC2 = 0.0625f * 1.4426950408889634f;
constexpr int L_R = 0;
constexpr int L_RED1 = 7168;
constexpr int L_RED2 = L_RED1 + 4352;
constexpr int L_REDX = L_RED2 + 2176;
constexpr int L_XX = L_REDX + 8192;
constexpr int L_CFG = L_XX + 73728;

DEVI unsigned chunk_off(const Params& p, unsigned cfg, int s, int j) {
    const int pg = s >> 5;
    return (unsigned)p.comm_off + (unsigned)pg * (2u << 20) + (unsigned)(2 * (4 * (s & 31) + j) + ((cfg >> pg) & 1)) * 8192u;
}

DEVI unsigned get_cfg(const Params& p, rsrc_t rs, int r, char* lds) {
    const int tid = threadIdx.x, lane = tid & 63, w = __builtin_amdgcn_readfirstlane(tid >> 6);
    if (w == 7) {
        if (r == 0) {
            unsigned cfg = 0x100;
            for (int pg = 0; pg < 2; ++pg) {
                unsigned best[2] = {1u << 30, 1u << 30};
                for (int k = 0; k < 4; ++k) {
                    for (int par = 0; par < 2; ++par) {
                        const unsigned off = (unsigned)p.comm_off + pg * (2u << 20) + (unsigned)(2 * (3 + 4 * k) + par) * 8192u + 4096u + 128u * k;
                        const unsigned long long c0 = __builtin_amdgcn_s_memtime();
                        unsigned vv = bld1(rs, off, 0);
                        asm volatile("s_waitcnt vmcnt(0)" : "+v"(vv) :: "memory");
                        const unsigned long long c1 = __builtin_amdgcn_s_memtime();
                        const unsigned dt = (unsigned)(c1 - c0);
                        if (k > 0 && dt < best[par]) best[par] = dt;
                    }
                }
                if (best[1] < best[0]) cfg |= 1u << pg;
            }
            if (lane == 0) bst1(rs, cfg, OFF_FLAG + FLAG_CFG * 4, 0);
            if (lane == 0) *(unsigned*)(lds + L_CFG) = cfg;
        } else {
            unsigned cfg = 0;
            for (int it = 0; it < SPIN_MAX; ++it) { cfg = bld1(rs, OFF_FLAG + FLAG_CFG * 4, 0); MEMFENCE(); cfg = __builtin_amdgcn_readfirstlane(cfg); if (cfg) break; __builtin_amdgcn_s_sleep(2); }
            if (lane == 0) *(unsigned*)(lds + L_CFG) = cfg;
        }
    }
    __syncthreads();
    return __builtin_amdgcn_readfirstlane(*(unsigned*)(lds + L_CFG));
}

#define D1 14
#define D2 16
template <int DELAY> DEVI void pause() { if (DELAY > 0) __builtin_amdgcn_s_sleep(DELAY); }

#define EV(k)
#define STAMP(k)

constexpr int L_PU = L_CFG + 256;
constexpr int L_PX = L_PU + 8192;
constexpr int L_GX = L_CFG + 256;
#define DX 48
#define WSPLIT 3

DEVI void await_flag(u32x4 ds, unsigned vo, unsigned target, bool& dead) {
    int spins = 0; const int lim = dead ? 1 : SPIN_MAX; bool bad;
    _Pragma("nounroll") do {
        unsigned v; ALD1(v, vo, ds, 0u);
        asm volatile("s_waitcnt vmcnt(0)" : "+v"(v) :: "memory");
        bad = (unsigned)__builtin_amdgcn_readfirstlane(v) < target;
        if (bad) __builtin_amdgcn_s_sleep(2);
    } while (bad && ++spins < lim);
    if (bad) dead = true;
}

template <bool ISG>
DEVI void chain_g_loop(const Params& p, int r, char* lds, unsigned cfg, u32x4 ds) {
    const int tid = threadIdx.x, lane = tid & 63, w = __builtin_amdgcn_readfirstlane(tid >> 6);
    const int col = lane & 15, q4 = lane >> 4;
    const int h = r / 7, c7 = r % 7, beta = r >> 1, rh = r & 1;
    u32x4 aw[2][2][4];
    u32x4 bw[14];
    const int nq = (w < 3) ? 2 : 1;
    if (ISG) {
#pragma unroll
        for (int qi = 0; qi < 2; ++qi)
#pragma unroll
            for (int e = 0; e < 2; ++e)
#pragma unroll
                for (int tau = 0; tau < 4; ++tau) {
                    const int q = (w < 3) ? 2 * w + qi : 2 * w;
                    aw[qi][e][tau] = *(const u32x4*)(p.ws + OFF_AF + ((size_t)(h * 28 + 4 * c7 + tau) * 14 + 2 * q + e) * FR + lane * 16);
                }
    }
#pragma unroll
    for (int t = 0; t < 14; ++t) bw[t] = *(const u32x4*)(p.ws + OFF_BF + ((size_t)(w * 56 + beta) * 14 + t) * FR + lane * 16);
    const unsigned mE = (col < 8) ? 0xFFFFFFFFu : 0u;
    u32x4 sel;
    { const int j = col & 7; const unsigned one = (j & 1) ? 0x3F800000u : 0x00003F80u; sel.x = (j >> 1) == 0 ? one : 0; sel.y = (j >> 1) == 1 ? one : 0; sel.z = (j >> 1) == 2 ? one : 0; sel.w = (j >> 1) == 3 ? one : 0; }
    WAITVM0();
    bool dead = false;
    const int chi = col >> 3, cb = (col >> 2) & 1, i2 = col & 3;
    const int hB = 2 * w + cb;
    const int slot = 4 * chi + q4;
    const unsigned vo_part = (slot == 7) ? OOB : (unsigned)((hB >> 3) * 16384 + (hB & 7) * 1024 + rh * 512 + slot * 64 + i2 * 16);
    const unsigned vo_stat = (unsigned)(STATS_OFF + (hB * 8 + 4 * rh + i2) * 8 + (q4 & 1) * 4);
    const unsigned vo_ex = (unsigned)(hB * 512 + rh * 256 + q4 * 64 + i2 * 16);
    const unsigned vo_m0 = (unsigned)(2 * (w & 3) * 1024 + lane * 16), vo_m1 = (unsigned)((2 * (w & 3) + nq - 1) * 1024 + lane * 16);
    const int hw = w & 3;
    const unsigned vo_u0 = (unsigned)(OFF_UF + ((size_t)beta * 8 + 2 * hw) * FR + lane * 16), vo_u1 = vo_u0 + 1024;
    const unsigned vo_x = (unsigned)(OFF_XF + (size_t)((hw >> 1) * 14 + 2 * c7 + (hw & 1)) * FR + lane * 16);
    const unsigned vo_fu = (unsigned)(OFF_FLAG + (FLAG_U + beta / 14) * 4);
    const unsigned vo_pp = (unsigned)((h >> 3) * 16384 + (h & 7) * 1024 + ((lane >> 2) & 1) * 512 + c7 * 64 + (lane & 3) * 16);
    const unsigned vo_wc = (unsigned)(OFF_WC + ((size_t)beta * 8 + w) * 256 + col * 16);
    const unsigned vo_mp = (unsigned)(beta * 128 + (4 * rh + (lane & 3)) * 16);
    const unsigned vo_xm = (unsigned)(OFF_XM + ((size_t)h * 7 + c7) * 1024 + lane * 16);
    const unsigned vo_fxm = (unsigned)(OFF_FLAG + (FLAG_XM + h) * 4), vo_fw = (unsigned)(OFF_FLAG + (FLAG_W + (beta >> 3)) * 4);
    const bool diag = (col >> 3) == (q4 >> 1);
    u32x4 pu0 = {0,0,0,0}, pu1 = {0,0,0,0}, px = {0,0,0,0};
    unsigned fu = 0;
    u32x4 pl = {0,0,0,0}, ex = {0,0,0,0}; unsigned st = 0;
    u32x4 R0 = {0,0,0,0}, R1 = {0,0,0,0};
    u32x4 wp_prev = {0,0,0,0}; u32x4 xm_prev = {0,0,0,0};
    if (!ISG) {
        await_flag(ds, vo_fu, 8, dead);
        ALD4R(pu0, vo_u0, ds, 0u); ALD4R(pu1, vo_u1, ds, 0u); ALD4PR(px, vo_x, ds, 0u);
        ALD1R(fu, vo_fu, ds, 64u);
    }
    for (int s = 0; s < NSEG; ++s) {
        const unsigned oM = chunk_off(p, cfg, s, 0), oP = oM + 16384u, oE = oM + 3u * 16384u;
        u32x4 gE, gO;
        f32x4 acc[4];
        STAMP(0);
        if (ISG) {
            if (s > 0) pause<D2>();
            STAMP(1);
            if (w == 0) EV(4);
            { int spins = 0; const int lim = dead ? 1 : SPIN_MAX; bool bad;
              _Pragma("nounroll") do {
                ALD4R(R0, vo_m0, ds, oM);
                ALD4R(R1, vo_m1, ds, oM);
                asm volatile("s_waitcnt vmcnt(0)" : "+v"(R0), "+v"(R1) :: "memory");
                bad = __any(max(max4(R0), max4(R1)) == POISON);
              } while (bad && ++spins < lim);
              if (bad) dead = true; }
            STAMP(2);
            if (w == 0) EV(1);
            const u32x4 Re = R0 & mE;
#pragma unroll
            for (int tau = 0; tau < 4; ++tau) acc[tau] = mfma32(aw[0][0][tau], Re, (f32x4){0, 0, 0, 0});
            *(u32x4*)(lds + L_R + (2 * w) * 1024 + lane * 16) = R0;
            if (nq == 2) *(u32x4*)(lds + L_R + (2 * w + 1) * 1024 + lane * 16) = R1;
        } else {
            asm volatile("s_waitcnt vmcnt(0)" : "+v"(pu0), "+v"(pu1), "+v"(px), "+v"(fu) :: "memory");
            *(u32x4*)(lds + L_PU + (2 * hw) * 1024 + lane * 16) = pu0;
            *(u32x4*)(lds + L_PU + (2 * hw + 1) * 1024 + lane * 16) = pu1;
            *(u32x4*)(lds + L_PX + hw * 1024 + lane * 16) = px;
            if (s + 1 < NSEG - 1 && (unsigned)__builtin_amdgcn_readfirstlane(fu) < 8u) await_flag(ds, vo_fu + (unsigned)(s + 1) * 64u, 8, dead);
        }
        bar();
        STAMP(3);
        if (!ISG) {
            if (w == 6 && rh == 0 && lane == 0 && s >= 2) AADD1(vo_fw + (unsigned)(s - 2) * 64u, ds);
            if (w == 4 && lane == 0 && s >= 2) AADD1(vo_fxm + (unsigned)(s - 2) * 64u, ds);
        }
        if (ISG) {
            const u32x4 Rc = *(const u32x4*)(lds + L_R + c7 * 1024 + lane * 16);
            {
                const u32x4 Ro = R0 & ~mE;
#pragma unroll
                for (int tau = 0; tau < 4; ++tau) acc[tau] = mfma32(aw[0][1][tau], Ro, acc[tau]);
            }
            if (nq == 2) {
                const u32x4 Re = R1 & mE, Ro = R1 & ~mE;
#pragma unroll
                for (int tau = 0; tau < 4; ++tau) acc[tau] = mfma32(aw[1][0][tau], Re, acc[tau]);
#pragma unroll
                for (int tau = 0; tau < 4; ++tau) acc[tau] = mfma32(aw[1][1][tau], Ro, acc[tau]);
            }
            const u32x4 Rce = Rc & mE, Rco = Rc & ~mE;
            gE = pack8(acc[0], acc[1]);
            f32x4 d2 = mfma32(gE, Rce, (f32x4){0, 0, 0, 0});
            gO = pack8(acc[2], acc[3]);
            d2 = mfma32(gO, Rco, d2);
            float* red = (float*)(lds + L_RED1);
            const int i = col & 7, cp = col >> 3, jp = q4 >> 1, jh = q4 & 1;
#pragma unroll
            for (int rr = 0; rr < 4; ++rr) red[i * 132 + (4 * jh + rr) * 16 + 4 * w + 2 * cp + jp] = d2[rr];
        }
        bar();
        STAMP(4);
        if (ISG && w == 0 && s < NSEG - 1) {
            const float* red = (const float*)(lds + L_RED1);
            const int i = lane & 7, j = lane >> 3;
            const f32x4 a = *(const f32x4*)(red + i * 132 + j * 16), b2 = *(const f32x4*)(red + i * 132 + j * 16 + 4),
                        c = *(const f32x4*)(red + i * 132 + j * 16 + 8), d = *(const f32x4*)(red + i * 132 + j * 16 + 12);
            const f32x4 t4 = (a + b2) + (c + d);
            const float v = (t4.x + t4.y) + (t4.z + t4.w);
            const float vn = dppf<DPP_SHL8>(v);
            const unsigned o = nopoison(pk2(v, vn));
            unsigned e02, e13, d0, d1, d2, d3;
            uswap16(o, e02, e13);
            uswap32(e02, d0, d2);
            uswap32(e13, d1, d3);
            const u32x4 ov = {d0, d1, d2, d3};
            if (lane < 8) AST4(ov, vo_pp, ds, oP);
            EV(2);
        }
        MEMFENCE();
        f32x4 ae = {0, 0, 0, 0}, ao = {0, 0, 0, 0};
#pragma unroll
        for (int q = 0; q < WSPLIT; ++q) {
            const u32x4 R = *(const u32x4*)(lds + L_R + q * 1024 + lane * 16);
            ae = mfma32(R, bw[2 * q], ae);
            ao = mfma32(R, bw[2 * q + 1], ao);
        }
        MEMFENCE();
        SCHEDB();
        if (s >= 1) {
            if (rh == 0 && lane < 16) AST4(wp_prev, vo_wc, ds, (unsigned)(s - 1) * (56u * 8u * 256u));
            if (!ISG && w == 5) AST4(xm_prev, vo_xm, ds, (unsigned)(s - 1) * (16u * 7u * 1024u));
        }
        pause<D1>();
        if (w == 6) EV(6);
        ALD4R(pl, vo_part, ds, oP);
        ALD1R(st, vo_stat, ds, oM);
        ALD4R(ex, vo_ex, ds, oE);
        STAMP(5);
#pragma unroll
        for (int q = WSPLIT; q < 7; ++q) {
            const u32x4 R = *(const u32x4*)(lds + L_R + q * 1024 + lane * 16);
            ae = mfma32(R, bw[2 * q], ae);
            ao = mfma32(R, bw[2 * q + 1], ao);
        }
        const u32x4 uf = *(const u32x4*)(lds + L_PU + w * 1024 + lane * 16);
        u32x2 wop, wp;
        {
            float wv[4];
#pragma unroll
            for (int rr = 0; rr < 4; ++rr) { const float x = (lane < 32) ? ae[rr] : ao[rr]; float a, b2; swap32(x, a, b2); wv[rr] = a + b2; }
            wp = (u32x2){pk2(wv[0], wv[1]), pk2(wv[2], wv[3])};
            wop.x = diag ? wp.x : 0u; wop.y = diag ? wp.y : 0u;
        }
        if (ISG) {
#pragma unroll
            for (int it = 0; it < 2; ++it) {
                const u32x4 x0 = *(const u32x4*)(lds + L_PX + (it * 2 + 0) * 1024 + lane * 16), x1 = *(const u32x4*)(lds + L_PX + (it * 2 + 1) * 1024 + lane * 16);
                f32x4 dx = mfma32(gE, x0, (f32x4){0, 0, 0, 0});
                dx = mfma32(gO, x1, dx);
                *(f32x4*)(lds + L_REDX + (2 * w + (q4 >> 1)) * 1024 + ((it * 2 + (q4 & 1)) * 16 + col) * 16) = dx;
            }
            asm volatile("" : "+v"(wop));
            SCHEDB();
            asm volatile("s_waitcnt vmcnt(0)" : "+v"(pl), "+v"(st), "+v"(ex) :: "memory");
            SCHEDB();
        } else {
            const unsigned sn = (s + 1 < NSEG) ? (unsigned)(s + 1) : (unsigned)s;
            ALD4R(pu0, vo_u0, ds, sn * (56u * 8u * 1024u));
            ALD4R(pu1, vo_u1, ds, sn * (56u * 8u * 1024u));
            ALD4PR(px, vo_x, ds, sn * 28672u);
            ALD1R(fu, vo_fu, ds, (sn + 1u) * 64u);
            asm volatile("" : "+v"(wop));
            SCHEDB();
            asm volatile("s_waitcnt vmcnt(4)" : "+v"(pl), "+v"(st), "+v"(ex) :: "memory");
            SCHEDB();
        }
        STAMP(6);
        if (s < NSEG - 1) {
            { int spins = 0; const int lim = dead ? 1 : SPIN_MAX;
              bool bad = __any(max(max(max4(pl), max4(ex)), st) == POISON);
              _Pragma("nounroll") while (bad && ++spins < lim) {
                ALD4R(pl, vo_part, ds, oP);
                ALD1R(st, vo_stat, ds, oM);
                ALD4R(ex, vo_ex, ds, oE);
                asm volatile("s_waitcnt vmcnt(0)" : "+v"(pl), "+v"(st), "+v"(ex) :: "memory");
                bad = __any(max(max(max4(pl), max4(ex)), st) == POISON);
              }
              if (bad) dead = true; }
            STAMP(7);
            if (w == 7) EV(3);
            f32x4 dx = mfma32(uf, ex, (f32x4){0, 0, 0, 0});
            f32x4 sm = mfma32(sel, pl, (f32x4){0, 0, 0, 0});
            float sv[4];
#pragma unroll
            for (int rr = 0; rr < 4; ++rr) sv[rr] = sm[rr] + dppf<DPP_ROR8>(sm[rr]);
            float mx, sx; swap16(__uint_as_float(st), mx, sx);
            float ml = fmaxf(fmaxf(sv[0], sv[1]), fmaxf(sv[2], sv[3]));
            { float a, b2; swap16(ml, a, b2); ml = fmaxf(a, b2); }
            const float M = fmaxf(ml, mx);
            const float nM = -M * SC2;
            float e[4];
#pragma unroll
            for (int rr = 0; rr < 4; ++rr) e[rr] = __builtin_amdgcn_exp2f(__builtin_fmaf(sv[rr], SC2, nM));
            const float al = __builtin_amdgcn_exp2f(__builtin_fmaf(mx, SC2, nM));
            float ls = (e[0] + e[1]) + (e[2] + e[3]);
            { float a, b2; swap16(ls, a, b2); ls = a + b2; }
            const float l = __builtin_fmaf(al, sx, ls);
            const float inv = __builtin_amdgcn_rcpf(l);
            const u32x2 pp = {pk2(e[0] * inv, e[1] * inv), pk2(e[2] * inv, e[3] * inv)};
            const float ai = al * inv;
            f32x4 acc2 = dx * ai;
            acc2 = mfma16(wop, pp, acc2);
            if (chi == 0 && (q4 >> 1) == cb) {
                float* red = (float*)(lds + L_RED2);
#pragma unroll
                for (int rr = 0; rr < 4; ++rr) red[i2 * 132 + (4 * (q4 & 1) + rr) * 16 + 2 * w + cb] = acc2[rr];
            }
        }
        bar();
        STAMP(8);
        if (w == 6) EV(5);
        if (!ISG && w == 7 && s < NSEG - 1) {
            const float* red = (const float*)(lds + L_RED2);
            const int ii = lane & 3, cc = (lane >> 2) & 7, half = lane >> 5;
            const f32x4 a = *(const f32x4*)(red + ii * 132 + cc * 16 + half * 8), b2 = *(const f32x4*)(red + ii * 132 + cc * 16 + half * 8 + 4);
            const f32x4 t4 = a + b2;
            float v = (t4.x + t4.y) + (t4.z + t4.w);
            { float x0, x1; swap32(v, x0, x1); v = x0 + x1; }
            const float vn = dppf<DPP_SHL4>(v);
            const unsigned o = nopoison(pk2(v, vn));
            const unsigned o8 = dppu<DPP_SHL8>(o);
            unsigned r0a, r1a, r0b, r1b;
            uswap16(o, r0a, r1a);
            uswap16(o8, r0b, r1b);
            const u32x4 ov = {r0a, r0b, r1a, r1b};
            if (lane < 4) AST4(ov, vo_mp, ds, chunk_off(p, cfg, s + 1, 0));
            EV(0);
        }
        { unsigned a0, b0, a1, b1; uswap16(wp.x, a0, b0); uswap16(wp.y, a1, b1); wp_prev = (u32x4){a0, a1, b0, b1}; }
        if (!ISG && w == 5) {
            f32x4 t4 = *(const f32x4*)(lds + L_REDX + lane * 16);
#pragma unroll
            for (int pp = 1; pp < 8; ++pp) t4 += *(const f32x4*)(lds + L_REDX + pp * 1024 + lane * 16);
            xm_prev = __builtin_bit_cast(u32x4, t4);
        }
        STAMP(9);
    }
    if (rh == 0 && lane < 16) AST4(wp_prev, vo_wc, ds, (unsigned)(NSEG - 1) * (56u * 8u * 256u));
    if (!ISG && w == 5) AST4(xm_prev, vo_xm, ds, (unsigned)(NSEG - 1) * (16u * 7u * 1024u));
    AWAIT(0);
    bar();
    if (!ISG) {
        if (w == 6 && rh == 0 && lane == 0) { AADD1(vo_fw + (unsigned)(NSEG - 2) * 64u, ds); AADD1(vo_fw + (unsigned)(NSEG - 1) * 64u, ds); }
        if (w == 4 && lane == 0) { AADD1(vo_fxm + (unsigned)(NSEG - 2) * 64u, ds); AADD1(vo_fxm + (unsigned)(NSEG - 1) * 64u, ds); }
    }
}

DEVI void chain_g(const Params& p, int r, char* lds) {
    const int w = __builtin_amdgcn_readfirstlane(threadIdx.x >> 6);
    const rsrc_t rs = mkrsrc(p.ws, 0x10000000u);
    const u32x4 ds = mkdesc(p.ws, 0x10000000u);
    const unsigned cfg = get_cfg(p, rs, r, lds);
    if (w < 4) chain_g_loop<true>(p, r, lds, cfg, ds); else chain_g_loop<false>(p, r, lds, cfg, ds);
}

struct XBuf { u32x4 g[2][2][2]; u32x4 x[2][2][2]; };

DEVI void x_issue(u32x4 ds, XBuf& B, int s, int h, int w, int lane) {
#pragma unroll
    for (int qi = 0; qi < 2; ++qi)
#pragma unroll
        for (int e = 0; e < 2; ++e) {
            const int t = 2 * ((w < 3) ? 2 * w + qi : 2 * w) + e;
#pragma unroll
            for (int k = 0; k < 2; ++k) {
                const unsigned vg = (unsigned)(OFF_GX + ((size_t)(h * 2 + k) * 14 + t) * FR + lane * 16), vx = (unsigned)(OFF_XF + ((size_t)k * 14 + t) * FR + lane * 16);
                ALD4R(B.g[qi][e][k], vg, ds, (unsigned)s * (16u * 28u * 1024u));
                ALD4PR(B.x[qi][e][k], vx, ds, (unsigned)s * 28672u);
            }
        }
}
DEVI void x_pin(XBuf& B) {
#pragma unroll
    for (int qi = 0; qi < 2; ++qi)
#pragma unroll
        for (int e = 0; e < 2; ++e) asm volatile("" : "+v"(B.g[qi][e][0]), "+v"(B.g[qi][e][1]), "+v"(B.x[qi][e][0]), "+v"(B.x[qi][e][1]));
}
DEVI void x_consume(const XBuf& B, int s, int w, int lane, char* lds) {
    f32x4 xx[3] = {{0,0,0,0},{0,0,0,0},{0,0,0,0}};
    const int nq = (w < 3) ? 2 : 1;
#pragma unroll
    for (int qi = 0; qi < 2; ++qi) {
        if (qi < nq) {
#pragma unroll
            for (int e = 0; e < 2; ++e) {
                xx[0] = mfma32(B.g[qi][e][0], B.x[qi][e][0], xx[0]);
                xx[1] = mfma32(B.g[qi][e][0], B.x[qi][e][1], xx[1]);
                xx[2] = mfma32(B.g[qi][e][1], B.x[qi][e][1], xx[2]);
#pragma unroll
                for (int k = 0; k < 2; ++k) *(u32x4*)(lds + L_GX + (s & 1) * 28672 + (k * 14 + 2 * (2 * w + qi) + e) * 1024 + lane * 16) = B.g[qi][e][k];
            }
        }
    }
#pragma unroll
    for (int k = 0; k < 3; ++k) *(f32x4*)(lds + L_XX + (s % 6) * 12288 + (w * 3 + k) * 1024 + lane * 16) = xx[k];
}
DEVI void x_xm_issue(u32x4 ds, u32x4 (&xm)[7], int s, int h, int it, int lane) {
    const int col = lane & 15, q4 = lane >> 4;
#pragma unroll
    for (int c = 0; c < 7; ++c) { const unsigned vo = (q4 < 2) ? (unsigned)(OFF_XM + ((size_t)h * 7 + c) * 1024 + ((it * 2 + q4) * 16 + col) * 16) : OOB; ALD4R(xm[c], vo, ds, (unsigned)s * (16u * 7u * 1024u)); }
}
DEVI void x_finalize(u32x4 ds, const u32x4 (&xm)[7], int s, int h, int it, int lane, char* lds) {
    const int col = lane & 15, q4 = lane >> 4;
    f32x4 sm = {0, 0, 0, 0};
#pragma unroll
    for (int c = 0; c < 7; ++c) sm += __builtin_bit_cast(f32x4, xm[c]);
    f32x4 s0 = {0,0,0,0}, s1 = {0,0,0,0};
    const char* xx = lds + L_XX + (s % 6) * 12288 + lane * 16;
#pragma unroll
    for (int ww = 0; ww < 4; ++ww) {
        s0 += *(const f32x4*)(xx + (ww * 3 + it) * 1024);
        s1 += *(const f32x4*)(xx + (ww * 3 + 2) * 1024);
    }
    const float NEG = -3.0e38f;
    float v0[4], v1[4], vm[4];
#pragma unroll
    for (int rr = 0; rr < 4; ++rr) {
        const bool causal = (4 * q4 + rr) <= col;
        v0[rr] = (it == 1 || causal) ? s0[rr] : NEG;
        v1[rr] = (it == 1 && causal) ? s1[rr] : NEG;
        vm[rr] = (q4 < 2) ? sm[rr] : NEG;
    }
    float m = NEG;
#pragma unroll
    for (int rr = 0; rr < 4; ++rr) m = fmaxf(m, fmaxf(fmaxf(v0[rr], v1[rr]), vm[rr]));
    { float a, b2; swap16(m, a, b2); m = fmaxf(a, b2); swap32(m, a, b2); m = fmaxf(a, b2); }
    const float nM = -m * SC2;
    float l = 0;
#pragma unroll
    for (int rr = 0; rr < 4; ++rr) {
        v0[rr] = (v0[rr] == NEG) ? 0.f : __builtin_amdgcn_exp2f(__builtin_fmaf(v0[rr], SC2, nM));
        v1[rr] = (v1[rr] == NEG) ? 0.f : __builtin_amdgcn_exp2f(__builtin_fmaf(v1[rr], SC2, nM));
        vm[rr] = (vm[rr] == NEG) ? 0.f : __builtin_amdgcn_exp2f(__builtin_fmaf(vm[rr], SC2, nM));
        l += v0[rr] + v1[rr] + vm[rr];
    }
    { float a, b2; swap16(l, a, b2); l = a + b2; swap32(l, a, b2); l = a + b2; }
    const float inv = __builtin_amdgcn_rcpf(l);
    const u32x4 px = {pk2(v0[0] * inv, v0[1] * inv), pk2(v0[2] * inv, v0[3] * inv), pk2(v1[0] * inv, v1[1] * inv), pk2(v1[2] * inv, v1[3] * inv)};
    const unsigned vpx = (unsigned)(OFF_PX + ((size_t)h * 2 + it) * FR + lane * 16);
    AST4(px, vpx, ds, (unsigned)s * 32768u);
    if (q4 < 2) { const u32x2 pm = {pk2(vm[0] * inv, vm[1] * inv), pk2(vm[2] * inv, vm[3] * inv)};
        const unsigned vpm = (unsigned)(OFF_PM + ((size_t)(h >> 1) * 2 + it) * 512 + (col + 16 * (2 * (h & 1) + q4)) * 8);
        AST2(pm, vpm, ds, (unsigned)s * 8192u); }
}

DEVI void chain_x(const Params& p, int h, char* lds) {
    const int tid = threadIdx.x, lane = tid & 63, w = __builtin_amdgcn_readfirstlane(tid >> 6);
    const int col = lane & 15, q4 = lane >> 4;
    const rsrc_t rs = mkrsrc(p.ws, 0x10000000u);
    const u32x4 ds = mkdesc(p.ws, 0x10000000u);
    const unsigned cfg = get_cfg(p, rs, 112 + h, lds);
    WAITVM0();
    bool dead = false;
    if (w < 4) {
        const unsigned mE = (col < 8) ? 0xFFFFFFFFu : 0u;
        const int nq = (w < 3) ? 2 : 1;
        const unsigned vo_m0 = (unsigned)(2 * w * 1024 + lane * 16), vo_m1 = (unsigned)((2 * w + nq - 1) * 1024 + lane * 16);
        const unsigned vo_e = (unsigned)(h * 512 + ((col >> 2) & 1) * 256 + q4 * 64 + (col & 3) * 16), vo_s = (unsigned)(STATS_OFF + (h * 8 + (lane & 7)) * 8);
        u32x4 R0 = {0,0,0,0}, R1 = {0,0,0,0};
        bar();
        for (int s = 0; s < NSEG; ++s) {
            const unsigned oM = chunk_off(p, cfg, s, 0), oE = oM + 3u * 16384u;
            if (s < NSEG - 1) {
                u32x4 g[2][2][2];
#pragma unroll
                for (int qi = 0; qi < 2; ++qi)
#pragma unroll
                    for (int e = 0; e < 2; ++e)
#pragma unroll
                        for (int k = 0; k < 2; ++k) g[qi][e][k] = *(const u32x4*)(lds + L_GX + (s & 1) * 28672 + (k * 14 + 2 * ((w < 3) ? 2 * w + qi : 2 * w) + e) * 1024 + lane * 16);
                if (s > 0) pause<DX>();
                { int spins = 0; const int lim = dead ? 1 : SPIN_MAX; bool bad;
                  _Pragma("nounroll") do {
                    ALD4R(R0, vo_m0, ds, oM);
                    ALD4R(R1, vo_m1, ds, oM);
                    asm volatile("s_waitcnt vmcnt(0)" : "+v"(R0), "+v"(R1) :: "memory");
                    bad = __any(max(max4(R0), max4(R1)) == POISON);
                  } while (bad && ++spins < lim);
                  if (bad) dead = true; }
                f32x4 d[2] = {{0,0,0,0},{0,0,0,0}};
#pragma unroll
                for (int kt = 0; kt < 2; ++kt) { d[kt] = mfma32(g[0][0][kt], R0 & mE, d[kt]); d[kt] = mfma32(g[0][1][kt], R0 & ~mE, d[kt]); }
                if (nq == 2) {
#pragma unroll
                    for (int kt = 0; kt < 2; ++kt) { d[kt] = mfma32(g[1][0][kt], R1 & mE, d[kt]); d[kt] = mfma32(g[1][1][kt], R1 & ~mE, d[kt]); }
                }
                f32x4 o;
#pragma unroll
                for (int rr = 0; rr < 4; ++rr) {
                    const float a = d[0][rr] + dppf<DPP_ROR8>(d[0][rr]), b2 = d[1][rr] + dppf<DPP_ROR8>(d[1][rr]);
                    o[rr] = (col < 8) ? a : b2;
                }
                *(f32x4*)(lds + L_REDX + w * 1024 + lane * 16) = o;
            }
            bar();
            if (w == 0 && s < NSEG - 1) {
                f32x4 t4 = *(const f32x4*)(lds + L_REDX + lane * 16);
#pragma unroll
                for (int pp = 1; pp < 4; ++pp) t4 += *(const f32x4*)(lds + L_REDX + pp * 1024 + lane * 16);
                float m = fmaxf(fmaxf(t4.x, t4.y), fmaxf(t4.z, t4.w));
                m = fmaxf(m, dppf<DPP_ROR8>(m));
                { float a, b2; swap16(m, a, b2); m = fmaxf(a, b2); swap32(m, a, b2); m = fmaxf(a, b2); }
                const float nM = -m * SC2;
                float e[4];
#pragma unroll
                for (int rr = 0; rr < 4; ++rr) e[rr] = __builtin_amdgcn_exp2f(__builtin_fmaf(t4[rr], SC2, nM));
                const unsigned o0 = nopoison(pk2(e[0], e[1])), o1 = nopoison(pk2(e[2], e[3]));
                const u32x4 o = {o0, o1, dppu<DPP_ROR8>(o0), dppu<DPP_ROR8>(o1)};
                if (col < 8) AST4(o, vo_e, ds, oE);
                float l = (e[0] + e[1]) + (e[2] + e[3]);
                l += dppf<DPP_ROR8>(l);
                { float a, b2; swap16(l, a, b2); l = a + b2; swap32(l, a, b2); l = a + b2; }
                if (lane < 8) { const u32x2 st = {nopoison(__float_as_uint(m)), nopoison(__float_as_uint(l))}; AST2(st, vo_s, ds, oM); }
            }
        }
        bar();
    } else {
        const int hw = w - 4;
        XBuf A, B;
#pragma unroll
        for (int qi = 0; qi < 2; ++qi)
#pragma unroll
            for (int e = 0; e < 2; ++e)
#pragma unroll
                for (int k = 0; k < 2; ++k) { A.g[qi][e][k] = (u32x4){0,0,0,0}; A.x[qi][e][k] = (u32x4){0,0,0,0}; B.g[qi][e][k] = (u32x4){0,0,0,0}; B.x[qi][e][k] = (u32x4){0,0,0,0}; }
        u32x4 xm[7];
#pragma unroll
        for (int c = 0; c < 7; ++c) xm[c] = (u32x4){0,0,0,0};
        const unsigned vo_fg = (unsigned)(OFF_FLAG + (FLAG_GX + h) * 4), vo_fx = (unsigned)(OFF_FLAG + (FLAG_XM + h) * 4), vo_fp = (unsigned)(OFF_FLAG + FLAG_P * 4);
        await_flag(ds, vo_fg, 2, dead);
        x_issue(ds, A, 0, h, hw, lane);
        await_flag(ds, vo_fg + 64u, 2, dead);
        x_issue(ds, B, 1, h, hw, lane);
        x_pin(A);
        x_consume(A, 0, hw, lane, lds);
        unsigned fg = 0, fx = 0;
        ALD1R(fg, vo_fg, ds, 128u);
        bar();
        auto interval = [&](XBuf& useB, XBuf& loadB, int s) {
            asm volatile("s_waitcnt vmcnt(0)" : "+v"(fg), "+v"(fx) :: "memory");
            x_pin(useB);
            if (hw == 2 && s >= 6 && lane == 0) AADD1(vo_fp + (unsigned)(s - 6) * 64u, ds);
            if (hw < 2 && s >= 4) {
                if ((unsigned)__builtin_amdgcn_readfirstlane(fx) < 7u) await_flag(ds, vo_fx + (unsigned)(s - 4) * 64u, 7, dead);
                x_xm_issue(ds, xm, s - 4, h, hw, lane);
            }
            const bool doload = (s + 2 < NSEG);
            if (doload) {
                if ((unsigned)__builtin_amdgcn_readfirstlane(fg) < 2u) await_flag(ds, vo_fg + (unsigned)(s + 2) * 64u, 2, dead);
                x_issue(ds, loadB, s + 2, h, hw, lane);
            }
            if (s + 1 < NSEG) x_consume(useB, s + 1, hw, lane, lds);
            if (hw < 2 && s >= 4) {
                if (doload) asm volatile("s_waitcnt vmcnt(16)" : "+v"(xm[0]), "+v"(xm[1]), "+v"(xm[2]), "+v"(xm[3]), "+v"(xm[4]), "+v"(xm[5]), "+v"(xm[6]) :: "memory");
                else asm volatile("s_waitcnt vmcnt(0)" : "+v"(xm[0]), "+v"(xm[1]), "+v"(xm[2]), "+v"(xm[3]), "+v"(xm[4]), "+v"(xm[5]), "+v"(xm[6]) :: "memory");
                x_finalize(ds, xm, s - 4, h, hw, lane, lds);
            }
            ALD1R(fg, vo_fg, ds, (unsigned)(s + 3) * 64u);
            ALD1R(fx, vo_fx, ds, (unsigned)(s >= 3 ? s - 3 : 0) * 64u);
            bar();
        };
        for (int s = 0; s < NSEG; s += 2) {
            interval(B, A, s);
            interval(A, B, s + 1);
        }
        for (int round = 0; round < 2; ++round) {
            const int sg = NSEG - 4 + 2 * round + (hw >> 1);
            {
                AWAIT(0);
                await_flag(ds, vo_fx + (unsigned)sg * 64u, 7, dead);
                x_xm_issue(ds, xm, sg, h, hw & 1, lane);
                asm volatile("s_waitcnt vmcnt(0)" : "+v"(xm[0]), "+v"(xm[1]), "+v"(xm[2]), "+v"(xm[3]), "+v"(xm[4]), "+v"(xm[5]), "+v"(xm[6]) :: "memory");
                x_finalize(ds, xm, sg, h, hw & 1, lane, lds);
            }
        }
        AWAIT(0);
        bar();
        if (lane == 0) AADD1(vo_fp + (unsigned)(NSEG - 4 + hw) * 64u, ds);
        if (lane == 0 && hw < 2) AADD1(vo_fp + (unsigned)(NSEG - 6 + hw) * 64u, ds);
    }
}

DEVI void out_unit(const Params& p, int s, int bg, bool& dead) {
    const int tid = threadIdx.x, lane = tid & 63, w = __builtin_amdgcn_readfirstlane(tid >> 6);
    const int col = lane & 15, q4 = lane >> 4, it = w & 1;
    const rsrc_t rs = mkrsrc(p.ws, 0x10000000u);
    const int b0 = 8 * bg + 2 * (w >> 1);
    waitflag(p, FLAG_U + s * 16 + b0 / 14, 8, dead);
    waitflag(p, FLAG_U + s * 16 + (b0 + 1) / 14, 8, dead);
    u32x4 uf[2][8];
#pragma unroll
    for (int bi = 0; bi < 2; ++bi)
#pragma unroll
        for (int hp = 0; hp < 8; ++hp) uf[bi][hp] = bld4(rs, (unsigned)(OFF_UF + (((size_t)s * 56 + b0 + bi) * 8 + hp) * FR + lane * 16), 0);
    waitflag(p, FLAG_W + s * 16 + bg, 8, dead);
    const bool diag = (col >> 3) == (q4 >> 1);
    u32x2 wv[2][8];
#pragma unroll
    for (int bi = 0; bi < 2; ++bi)
#pragma unroll
        for (int hp = 0; hp < 8; ++hp) wv[bi][hp] = bld2(rs, diag ? (unsigned)(OFF_WC + (((size_t)s * 56 + b0 + bi) * 8 + hp) * 256 + col * 16 + (q4 & 1) * 8) : OOB, 0);
    waitflag(p, FLAG_P + s * 16, 16, dead);
    f32x4 aa[2], ab[2], am[2];
#pragma unroll
    for (int bi = 0; bi < 2; ++bi) { aa[bi] = (f32x4){0,0,0,0}; ab[bi] = (f32x4){0,0,0,0}; am[bi] = (f32x4){0,0,0,0}; }
#pragma unroll
    for (int hp = 0; hp < 8; ++hp) {
        const u32x4 pa = bld4(rs, (unsigned)(OFF_PX + (((size_t)s * 16 + 2 * hp) * 2 + it) * FR + lane * 16), 0);
        const u32x4 pb = bld4(rs, (unsigned)(OFF_PX + (((size_t)s * 16 + 2 * hp + 1) * 2 + it) * FR + lane * 16), 0);
        const unsigned pmo = (unsigned)(OFF_PM + (((size_t)s * 8 + hp) * 2 + it) * 512 + lane * 8);
        const u32x2 pm = {bld1(rs, pmo, 0), bld1(rs, pmo + 4, 0)};
#pragma unroll
        for (int bi = 0; bi < 2; ++bi) {
            aa[bi] = mfma32(uf[bi][hp], pa, aa[bi]);
            ab[bi] = mfma32(uf[bi][hp], pb, ab[bi]);
            am[bi] = mfma16(wv[bi][hp], pm, am[bi]);
        }
    }
#pragma unroll
    for (int bi = 0; bi < 2; ++bi) {
        float v[4];
#pragma unroll
        for (int rr = 0; rr < 4; ++rr) { const float x = ((q4 >> 1) == 0 ? aa[bi][rr] : ab[bi][rr]) + am[bi][rr]; float a, b2; swap32(x, a, b2); v[rr] = a + b2; }
        if (lane < 32) { const u32x2 o = {pk2(v[0], v[1]), pk2(v[2], v[3])};
            *(u32x2*)(p.out + (size_t)(32 * s + 16 * it + col) * OUTW + 8 * (b0 + bi) + 4 * q4) = o; }
    }
}

DEVI float phi(float t) { return (t > 0.f ? t + 1.f : __builtin_amdgcn_exp2f(t * 1.4426950408889634f)) + 1e-6f; }
DEVI float rbf(float x) { return bf_lo(pk2(x, 0.f)); }

DEVI void b1_unit(const Params& p, int rb, int hp, char* lds) {
    const int tid = threadIdx.x, lane = tid & 63, w = __builtin_amdgcn_readfirstlane(tid >> 6);
    const int col = lane & 15, q4 = lane >> 4;
    const rsrc_t rs = mkrsrc(p.ws, 0x10000000u);
    const int h = 2 * hp + (w & 1), rt0 = rb * 8 + (w >> 1) * 2;
    f32x4 aq[2][2], ak[2][2], av[2][2];
#pragma unroll
    for (int a = 0; a < 2; ++a)
#pragma unroll
        for (int c = 0; c < 2; ++c) { aq[a][c] = (f32x4){0,0,0,0}; ak[a][c] = (f32x4){0,0,0,0}; av[a][c] = (f32x4){0,0,0,0}; }
#pragma unroll 2
    for (int t = 0; t < 14; ++t) {
        u32x4 xf[2], wq[2], wk[2], wv[2];
#pragma unroll
        for (int a = 0; a < 2; ++a) xf[a] = *(const u32x4*)(p.ws + OFF_BX + ((size_t)(rt0 + a) * 14 + t) * FR + lane * 16);
#pragma unroll
        for (int c = 0; c < 2; ++c) {
            wq[c] = *(const u32x4*)(p.ws + OFF_BW + ((size_t)((0 * 16 + h) * 2 + c) * 14 + t) * FR + lane * 16);
            wk[c] = *(const u32x4*)(p.ws + OFF_BW + ((size_t)((1 * 16 + h) * 2 + c) * 14 + t) * FR + lane * 16);
            wv[c] = *(const u32x4*)(p.ws + OFF_BW + ((size_t)((2 * 16 + h) * 2 + c) * 14 + t) * FR + lane * 16);
        }
#pragma unroll
        for (int a = 0; a < 2; ++a)
#pragma unroll
            for (int c = 0; c < 2; ++c) {
                aq[a][c] = mfma32(wq[c], xf[a], aq[a][c]);
                ak[a][c] = mfma32(xf[a], wk[c], ak[a][c]);
                av[a][c] = mfma32(xf[a], wv[c], av[a][c]);
            }
    }
#pragma unroll
    for (int a = 0; a < 2; ++a) {
        f32x4 p0, p1;
#pragma unroll
        for (int rr = 0; rr < 4; ++rr) {
            p0[rr] = phi(rbf(aq[a][0][rr]));
            p1[rr] = (8 * q4 + 4 + rr < 28) ? phi(rbf(aq[a][1][rr])) : 0.f;
        }
        bst4(rs, pack8(p0, p1), (unsigned)(OFF_BQ + ((size_t)(rt0 + a) * 16 + h) * FR + lane * 16), 0);
    }
    u32x4 pk[2], vv[2];
#pragma unroll
    for (int c = 0; c < 2; ++c) {
        f32x4 k0, k1, v0, v1;
        const int d = 16 * c + col;
#pragma unroll
        for (int rr = 0; rr < 4; ++rr) {
            k0[rr] = (d < 28) ? phi(rbf(ak[0][c][rr])) : 0.f; k1[rr] = (d < 28) ? phi(rbf(ak[1][c][rr])) : 0.f;
            v0[rr] = (d == 28) ? 1.f : av[0][c][rr]; v1[rr] = (d == 28) ? 1.f : av[1][c][rr];
        }
        pk[c] = pack8(k0, k1); vv[c] = pack8(v0, v1);
    }
    float* red = (float*)lds;
#pragma unroll
    for (int dc = 0; dc < 2; ++dc)
#pragma unroll
        for (int ec = 0; ec < 2; ++ec) {
            const f32x4 d = mfma32(pk[dc], vv[ec], (f32x4){0,0,0,0});
            *(f32x4*)(red + ((w * 4 + dc * 2 + ec) * 64 + lane) * 4) = d;
        }
    bar();
    {
        const int hh = w & 1, tile = w >> 1;
        f32x4 t4 = {0,0,0,0};
#pragma unroll
        for (int g = 0; g < 4; ++g) t4 += *(const f32x4*)(red + (((g * 2 + hh) * 4 + tile) * 64 + lane) * 4);
        bst4(rs, __builtin_bit_cast(u32x4, t4), (unsigned)(OFF_BKV + (((size_t)rb * 16 + 2 * hp + hh) * 4 + tile) * 1024 + lane * 16), 0);
    }
    WAITVM0();
    bar();
    if (tid == 0) signal(p, FLAG_B1);
}

DEVI void b2_unit(const Params& p, int h, char* lds, bool& dead) {
    const int tid = threadIdx.x, lane = tid & 63, w = __builtin_amdgcn_readfirstlane(tid >> 6);
    const int col = lane & 15, q4 = lane >> 4;
    const rsrc_t rs = mkrsrc(p.ws, 0x10000000u);
    float* kvs = (float*)lds;
    waitflag(p, FLAG_B1, 128, dead);
    if (w < 4) {
        f32x4 t4 = {0,0,0,0};
#pragma unroll
        for (int rb = 0; rb < 16; ++rb) t4 += __builtin_bit_cast(f32x4, bld4(rs, (unsigned)(OFF_BKV + (((size_t)rb * 16 + h) * 4 + w) * 1024 + lane * 16), 0));
        const int dc = w >> 1, ec = w & 1;
#pragma unroll
        for (int rr = 0; rr < 4; ++rr) kvs[(16 * dc + 4 * q4 + rr) * 33 + 16 * ec + col] = t4[rr];
    }
    bar();
    if (tid < 32) bst1(rs, __float_as_uint(kvs[tid * 33 + 28]), (unsigned)(OFF_BZ + (h * 32 + tid) * 4), 0);
    u32x4 ka[2];
#pragma unroll
    for (int bb = 0; bb < 2; ++bb) {
        const int d = 8 * (col >> 2) + 4 * bb + (col & 3);
        float e[8];
#pragma unroll
        for (int j = 0; j < 8; ++j) e[j] = (8 * q4 + j < 28) ? kvs[d * 33 + 8 * q4 + j] : 0.f;
        ka[bb] = (u32x4){pk2(e[0], e[1]), pk2(e[2], e[3]), pk2(e[4], e[5]), pk2(e[6], e[7])};
    }
    for (int ct = w; ct < 28; ct += 8) {
        u16 e[8];
#pragma unroll
        for (int j = 0; j < 8; ++j) e[j] = (8 * q4 + j < 28) ? p.bwo[(size_t)(28 * h + 8 * q4 + j) * DM + 16 * ct + col] : (u16)0;
        const u32x4 wf = {e[0] | ((unsigned)e[1] << 16), e[2] | ((unsigned)e[3] << 16), e[4] | ((unsigned)e[5] << 16), e[6] | ((unsigned)e[7] << 16)};
        const f32x4 d0 = mfma32(ka[0], wf, (f32x4){0,0,0,0}), d1 = mfma32(ka[1], wf, (f32x4){0,0,0,0});
        bst4(rs, pack8(d0, d1), (unsigned)(OFF_BM + ((size_t)h * 28 + ct) * FR + lane * 16), 0);
    }
    WAITVM0();
    bar();
    if (tid == 0) signal(p, FLAG_B2);
}

DEVI void b3_unit(const Params& p, int u, bool& dead) {
    const int tid = threadIdx.x, lane = tid & 63, w = __builtin_amdgcn_readfirstlane(tid >> 6);
    const int col = lane & 15, q4 = lane >> 4;
    const rsrc_t rs = mkrsrc(p.ws, 0x10000000u);
    const int rt = 2 * u + (w & 1), ct0 = 7 * (w >> 1);
    waitflag(p, FLAG_B2, 16, dead);
    u32x4 pn[16];
#pragma unroll
    for (int h = 0; h < 16; ++h) {
        const u32x4 pq = bld4(rs, (unsigned)(OFF_BQ + ((size_t)rt * 16 + h) * FR + lane * 16), 0);
        const f32x4 z0 = __builtin_bit_cast(f32x4, bld4(rs, (unsigned)(OFF_BZ + (h * 32 + 8 * q4) * 4), 0)), z1 = __builtin_bit_cast(f32x4, bld4(rs, (unsigned)(OFF_BZ + (h * 32 + 8 * q4 + 4) * 4), 0));
        float f[8] = {bf_lo(pq.x), bf_hi(pq.x), bf_lo(pq.y), bf_hi(pq.y), bf_lo(pq.z), bf_hi(pq.z), bf_lo(pq.w), bf_hi(pq.w)};
        float den = f[0] * z0.x + f[1] * z0.y + f[2] * z0.z + f[3] * z0.w + f[4] * z1.x + f[5] * z1.y + f[6] * z1.z + f[7] * z1.w;
        { float a, b2; swap16(den, a, b2); den = a + b2; swap32(den, a, b2); den = a + b2; }
        const float inv = 1.0f / (den + 1e-6f);
        pn[h] = (u32x4){pk2(f[0] * inv, f[1] * inv), pk2(f[2] * inv, f[3] * inv), pk2(f[4] * inv, f[5] * inv), pk2(f[6] * inv, f[7] * inv)};
    }
    for (int i = 0; i < 7; ++i) {
        const int ct = ct0 + i;
        f32x4 acc = {0,0,0,0};
#pragma unroll
        for (int h = 0; h < 16; ++h) acc = mfma32(bld4(rs, (unsigned)(OFF_BM + ((size_t)h * 28 + ct) * FR + lane * 16), 0), pn[h], acc);
        const u32x2 o = {pk2(acc[0], acc[1]), pk2(acc[2], acc[3])};
        *(u32x2*)(p.out + (size_t)(16 * rt + col) * OUTW + 448 + 16 * ct + 4 * q4) = o;
    }
}

DEVI void worker(const Params& p, int u, char* lds) {
    bool dead = false;
    if (p.mode != 2) gemm_worker(p, u, lds);
    if (p.mode == 1) return;
    if (p.mode == 2 && (p.mode2 & 1)) return;
    b1_unit(p, u >> 3, u & 7, lds);
    if (u < 16) b2_unit(p, u, lds, dead);
    for (int j = 0; j < 4; ++j) {
        const int idx = u + 128 * j;
        if (idx >= 224 && idx < 288) b3_unit(p, idx - 224, dead);
        else { const int o = (idx < 224) ? idx : idx - 64; out_unit(p, o / 7, o % 7, dead); }
    }
}

__global__ void __launch_bounds__(512, 2) k_main(Params p) {
    extern __shared__ __attribute__((aligned(16))) char lds[];
    const int b = blockIdx.x;
    const int idx = (b >> 3) * 4 + (b & 3);
    if ((b & 7) < 4) {
        if (p.mode == 1) return;
        if (idx < 112) chain_g(p, idx, lds); else chain_x(p, idx - 112, lds);
    } else {
        worker(p, idx, lds);
    }
}

extern "C" void kernel_launch(void* const* d_in, const int* in_sizes, int n_in, void* d_out, int out_size, void* d_ws, size_t ws_size, hipStream_t stream) {
    Params p;
    p.x = (const u16*)d_in[0]; p.mem0 = (const u16*)d_in[1]; p.Wq = (const u16*)d_in[2]; p.Wk = (const u16*)d_in[3];
    p.Wv = (const u16*)d_in[4]; p.Wo = (const u16*)d_in[5]; p.bx = (const u16*)d_in[7]; p.wqkv = (const u16*)d_in[8]; p.bwo = (const u16*)d_in[9];
    p.out = (u16*)d_out; p.ws = (char*)d_ws;
    const uintptr_t base = (uintptr_t)d_ws;
    const uintptr_t al = (base + OFF_COMM_RAW + ((2u << 20) - 1)) & ~(uintptr_t)((2u << 20) - 1);
    p.comm_off = (unsigned long long)(al - base);
    (void)hipFuncSetAttribute((const void*)k0_prep, hipFuncAttributeMaxDynamicSharedMemorySize, 65536);
    (void)hipFuncSetAttribute((const void*)k_main, hipFuncAttributeMaxDynamicSharedMemorySize, 160000);
    k0_prep<<<256, 512, 65536, stream>>>(p);
    p.mode = 0; p.mode2 = 0;
    k_main<<<256, 512, 160000, stream>>>(p);
}
```

```cpp
#include <hip/hip_runtime.h>
#include <hip/hip_bf16.h>
#include <stdint.h>

typedef __attribute__((ext_vector_type(8))) __bf16 bf16x8;
typedef __attribute__((ext_vector_type(4))) short s16x4;
typedef __attribute__((ext_vector_type(2))) __bf16 bf16x2;
typedef __attribute__((ext_vector_type(2))) float f32x2;
typedef __attribute__((ext_vector_type(4))) float f32x4;
typedef __attribute__((ext_vector_type(4))) unsigned u32x4;
typedef __attribute__((ext_vector_type(2))) unsigned u32x2;
typedef unsigned short u16;
#define LAS __attribute__((address_space(3)))
#define DEVI __device__ __forceinline__

constexpr int DM = 448, NH = 16, NSEG = 64, SEGL = 32, OUTW = 896;
constexpr size_t FR = 1024;
constexpr size_t OFF_FLAG = 0;
constexpr size_t SZ_FLAG = 131072;
constexpr size_t OFF_AF = OFF_FLAG + SZ_FLAG;
constexpr size_t SZ_AF = (size_t)16 * 28 * 14 * FR;
constexpr size_t OFF_BF = OFF_AF + SZ_AF;
constexpr size_t OFF_XF = OFF_BF + SZ_AF;
constexpr size_t SZ_XF = (size_t)64 * 28 * FR;
constexpr size_t OFF_GX = OFF_XF + SZ_XF;
constexpr size_t SZ_GX = (size_t)64 * 16 * 28 * FR;
constexpr size_t OFF_UF = OFF_GX + SZ_GX;
constexpr size_t SZ_UF = (size_t)64 * 56 * 8 * FR;
constexpr size_t OFF_XM = OFF_UF + SZ_UF;
constexpr size_t SZ_XM = (size_t)64 * 16 * 7 * 1024;
constexpr size_t OFF_WC = OFF_XM + SZ_XM;
constexpr size_t SZ_WC = (size_t)64 * 56 * 8 * 256;
constexpr size_t OFF_PX = OFF_WC + SZ_WC;
constexpr size_t SZ_PX = (size_t)64 * 16 * 2 * FR;
constexpr size_t OFF_PM = OFF_PX + SZ_PX;
constexpr size_t SZ_PM = (size_t)64 * 8 * 2 * 512;
constexpr size_t OFF_BQ = OFF_PM + SZ_PM;
constexpr size_t SZ_BQ = (size_t)128 * 16 * FR;
constexpr size_t OFF_BW = OFF_BQ + SZ_BQ;
constexpr size_t SZ_BW = (size_t)3 * 16 * 2 * 14 * FR;
constexpr size_t OFF_BX = OFF_BW + SZ_BW;
constexpr size_t SZ_BX = (size_t)128 * 14 * FR;
constexpr size_t OFF_BKV = OFF_BX + SZ_BX;
constexpr size_t SZ_BKV = (size_t)16 * 16 * 4 * 1024;
constexpr size_t OFF_BM = OFF_BKV + SZ_BKV;
constexpr size_t SZ_BM = (size_t)16 * 28 * FR;
constexpr size_t OFF_BZ = OFF_BM + SZ_BM;
constexpr size_t SZ_BZ = 16 * 32 * 4;
constexpr size_t OFF_COMM_RAW = OFF_BZ + SZ_BZ;
constexpr size_t COMM_BYTES = (size_t)6 << 20;
constexpr unsigned POISON = 0xFFFFFFFFu;
constexpr int FLAG_CFG = 0;
constexpr int FLAG_RDY = 256;
constexpr int FLAG_B1 = 32;
constexpr int FLAG_B2 = 64;
constexpr int FLAG_XM = 16384;
constexpr int FLAG_GX = 2048;
constexpr int FLAG_U = 3072;
constexpr int FLAG_P = 4096;
constexpr int FLAG_W = 8192;
constexpr int STATS_OFF = 7168;
constexpr int TS_OFF = 32768 + 448;

struct Params {
    const u16 *x, *mem0, *Wq, *Wk, *Wv, *Wo, *bx, *wqkv, *bwo;
    u16* out;
    char* ws;
    unsigned long long comm_off;
    unsigned long long mode;
    unsigned long long mode2;
};

DEVI unsigned pk2(float a, float b) { f32x2 v = {a, b}; bf16x2 r = __builtin_convertvector(v, bf16x2); return __builtin_bit_cast(unsigned, r); }
DEVI f32x4 mfma32(u32x4 a, u32x4 b, f32x4 c) { return __builtin_amdgcn_mfma_f32_16x16x32_bf16(__builtin_bit_cast(bf16x8, a), __builtin_bit_cast(bf16x8, b), c, 0, 0, 0); }
DEVI f32x4 mfma16(u32x2 a, u32x2 b, f32x4 c) { return __builtin_amdgcn_mfma_f32_16x16x16bf16_1k(__builtin_bit_cast(s16x4, a), __builtin_bit_cast(s16x4, b), c, 0, 0, 0); }
DEVI u32x4 pack8(f32x4 a, f32x4 b) { u32x4 r; r.x = pk2(a.x, a.y); r.y = pk2(a.z, a.w); r.z = pk2(b.x, b.y); r.w = pk2(b.z, b.w); return r; }
DEVI float fmx(float a, float b) { return __builtin_amdgcn_fmed3f(a, b, 3.4028235e38f); }
template <int CTRL> DEVI float dppf(float x) { return __uint_as_float((unsigned)__builtin_amdgcn_update_dpp(0, (int)__float_as_uint(x), CTRL, 0xf, 0xf, true)); }
constexpr int DPP_ROR8 = 0x128, DPP_ROR4 = 0x124, DPP_QP_1032 = 0xB1, DPP_QP_2301 = 0x4E;
DEVI void swap16(float x, float& a, float& b) { auto s = __builtin_amdgcn_permlane16_swap(__float_as_uint(x), __float_as_uint(x), false, false); a = __uint_as_float(s[0]); b = __uint_as_float(s[1]); }
DEVI void swap32(float x, float& a, float& b) { auto s = __builtin_amdgcn_permlane32_swap(__float_as_uint(x), __float_as_uint(x), false, false); a = __uint_as_float(s[0]); b = __uint_as_float(s[1]); }
DEVI void uswap16(unsigned x, unsigned& a, unsigned& b) { auto s = __builtin_amdgcn_permlane16_swap(x, x, false, false); a = s[0]; b = s[1]; }
DEVI void uswap32(unsigned x, unsigned& a, unsigned& b) { auto s = __builtin_amdgcn_permlane32_swap(x, x, false, false); a = s[0]; b = s[1]; }
template <int CTRL> DEVI unsigned dppu(unsigned x) { return (unsigned)__builtin_amdgcn_update_dpp(0, (int)x, CTRL, 0xf, 0xf, true); }
constexpr int DPP_SHL4 = 0x104, DPP_SHL8 = 0x108;
DEVI float bf_lo(unsigned v) { return __uint_as_float(v << 16); }
DEVI float bf_hi(unsigned v) { return __uint_as_float(v & 0xFFFF0000u); }
DEVI int kcol(int t, int q4) { return 64 * (t >> 1) + 16 * q4 + 8 * (t & 1); }
DEVI unsigned nopoison(unsigned v) { return v < 0xFFFFFFFEu ? v : 0xFFFFFFFEu; }
DEVI unsigned max4(u32x4 v) { unsigned a = v.x > v.y ? v.x : v.y, b = v.z > v.w ? v.z : v.w; return a > b ? a : b; }

typedef __amdgpu_buffer_rsrc_t rsrc_t;
DEVI rsrc_t mkrsrc(const void* p, unsigned bytes) { return __builtin_amdgcn_make_buffer_rsrc((void*)p, 0, bytes, 0x00020000); }
DEVI u32x4 bld4(rsrc_t r, unsigned v, unsigned s) { return __builtin_amdgcn_raw_buffer_load_b128(r, v, s, 16); }
DEVI u32x2 bld2(rsrc_t r, unsigned v, unsigned s) { return __builtin_amdgcn_raw_buffer_load_b64(r, v, s, 16); }
DEVI unsigned bld1(rsrc_t r, unsigned v, unsigned s) { return __builtin_amdgcn_raw_buffer_load_b32(r, v, s, 16); }
DEVI u32x4 bld4p(rsrc_t r, unsigned v, unsigned s) { return __builtin_amdgcn_raw_buffer_load_b128(r, v, s, 0); }
DEVI void bst4(rsrc_t r, u32x4 d, unsigned v, unsigned s) { __builtin_amdgcn_raw_buffer_store_b128(d, r, v, s, 16); }
DEVI void bst2(rsrc_t r, u32x2 d, unsigned v, unsigned s) { __builtin_amdgcn_raw_buffer_store_b64(d, r, v, s, 16); }
DEVI void bst1(rsrc_t r, unsigned d, unsigned v, unsigned s) { __builtin_amdgcn_raw_buffer_store_b32(d, r, v, s, 16); }
#define MEMFENCE() asm volatile("" ::: "memory")
#define WAITVM0() do { asm volatile("s_waitcnt vmcnt(0)" ::: "memory"); __builtin_amdgcn_s_waitcnt(0x0F70); } while (0)
DEVI void bar() { asm volatile("s_waitcnt lgkmcnt(0)\n\ts_barrier" ::: "memory"); }
constexpr unsigned OOB = 0xF0000000u;
DEVI u32x4 mkdesc(const void* p, unsigned bytes) {
    const unsigned long long a = (unsigned long long)p;
    u32x4 d; d.x = __builtin_amdgcn_readfirstlane((unsigned)a); d.y = __builtin_amdgcn_readfirstlane((unsigned)(a >> 32)) & 0xFFFFu; d.z = bytes; d.w = 0x00020000u; return d;
}
#define ALD4(dst, vo, ds, so) asm volatile("buffer_load_dwordx4 %0, %1, %2, %3 offen sc1" : "=v"(dst) : "v"(vo), "s"(ds), "s"(so) : "memory")
#define ALD4R(dst, vo, ds, so) asm volatile("buffer_load_dwordx4 %0, %1, %2, %3 offen sc1" : "+v"(dst) : "v"(vo), "s"(ds), "s"(so) : "memory")
#define ALD4PR(dst, vo, ds, so) asm volatile("buffer_load_dwordx4 %0, %1, %2, %3 offen" : "+v"(dst) : "v"(vo), "s"(ds), "s"(so) : "memory")
#define ALD1(dst, vo, ds, so) asm volatile("buffer_load_dword %0, %1, %2, %3 offen sc1" : "=v"(dst) : "v"(vo), "s"(ds), "s"(so) : "memory")
#define ALD1R(dst, vo, ds, so) asm volatile("buffer_load_dword %0, %1, %2, %3 offen sc1" : "+v"(dst) : "v"(vo), "s"(ds), "s"(so) : "memory")
#define AST4(dat, vo, ds, so) asm volatile("buffer_store_dwordx4 %0, %1, %2, %3 offen sc1\n\ts_nop 1" :: "v"(dat), "v"(vo), "s"(ds), "s"(so) : "memory")
#define AST2(dat, vo, ds, so) asm volatile("buffer_store_dwordx2 %0, %1, %2, %3 offen sc1\n\ts_nop 1" :: "v"(dat), "v"(vo), "s"(ds), "s"(so) : "memory")
#define AST1(dat, vo, ds, so) asm volatile("buffer_store_dword %0, %1, %2, %3 offen sc1" :: "v"(dat), "v"(vo), "s"(ds), "s"(so) : "memory")
#define AADD1(vo, ds) asm volatile("buffer_atomic_add %0, %1, %2, 0 offen" :: "v"(1u), "v"(vo), "s"(ds) : "memory")
#define SCHEDB() __builtin_amdgcn_sched_barrier(0)
#define PIN(x) asm volatile("" : "+v"(x))
#define AWAIT(n) asm volatile("s_waitcnt vmcnt(" #n ")" ::: "memory")

__global__ void __launch_bounds__(512, 2) k0_prep(Params p) {
    extern __shared__ __attribute__((aligned(16))) char lds[];
    const int b = blockIdx.x, tid = threadIdx.x, lane = tid & 63, w = __builtin_amdgcn_readfirstlane(tid >> 6);
    const int l15 = lane & 15, q4 = lane >> 4;
    const int mat = b & 1, h = (b >> 1) & 15, sub = b >> 5, th = sub & 1, cg = sub >> 1;
    constexpr int K_SCR = 61440, K_XT = K_SCR + 7 * 8448;
    const int lr = lane >> 2, lp = lane & 3;
    u32x4 stg[7];
    if (mat == 0) {
#pragma unroll
        for (int i = 0; i < 7; ++i) {
            const int ct = 7 * cg + i;
            const int c = 64 * (ct >> 2) + 16 * (lr >> 2) + 4 * (ct & 3) + (lr & 3);
            stg[i] = *(const u32x4*)(p.Wq + ((size_t)c * 16 + h) * 256 + 32 * w + 8 * lp);
        }
    } else {
#pragma unroll
        for (int i = 0; i < 7; ++i) {
            const int n = tid + 512 * i, e = n / 14, ch = n % 14;
            stg[i] = *(const u32x4*)(p.Wo + ((size_t)h * 256 + e) * 448 + 112 * cg + 8 * ch);
        }
    }
    u32x4 rw[2][8];
    const int t = 7 * th + (w < 7 ? w : 0);
    {
        const u16* Wr = mat ? p.Wv : p.Wk;
#pragma unroll
        for (int bb = 0; bb < 2; ++bb) {
            const int d = 64 * (t >> 1) + 16 * (lr >> 2) + 8 * (t & 1) + 4 * bb + (lr & 3);
#pragma unroll
            for (int k = 0; k < 8; ++k) rw[bb][k] = *(const u32x4*)(Wr + ((size_t)d * 16 + h) * 256 + 32 * k + 8 * lp);
        }
    }
    const char* xsrc = (const char*)((b & 1) ? p.bx : p.x) + (size_t)(b >> 1) * 14336;
    const u32x4 xt0 = *(const u32x4*)(xsrc + tid * 16);
    const u32x4 xt1 = *(const u32x4*)(xsrc + 8192 + (tid < 384 ? tid : 0) * 16);
    const int fw = b * 8 + w;
    u16 ew[8];
    {
        const int f = (fw < 1344) ? fw : 0;
        const int tt = f % 14, tile = (f / 14) & 1, hh = (f / 28) & 15, t3 = f / 448;
        const int col = (t3 == 0) ? (8 * (l15 >> 2) + 4 * tile + (l15 & 3)) : (16 * tile + l15);
#pragma unroll
        for (int j = 0; j < 8; ++j) ew[j] = (col < 28) ? p.wqkv[(size_t)(kcol(tt, q4) + j) * 1344 + t3 * 448 + hh * 28 + col] : (u16)0;
    }
    if (b & 4) {
        const int q = (b >> 3) * 4 + (b & 3);
        char* cb = p.ws + p.comm_off + (size_t)q * 32768;
        const u32x4 pz = {POISON, POISON, POISON, POISON};
#pragma unroll
        for (int k = 0; k < 4; ++k) *(u32x4*)(cb + k * 8192 + tid * 16) = pz;
        if (q == 0 && tid < 448) {
            const int beta = tid >> 3, i = tid & 7;
            const u32x4 m = *(const u32x4*)(p.mem0 + i * DM + 8 * beta);
            *(u32x4*)(cb + tid * 16) = m;
            *(u32x4*)(cb + 8192 + tid * 16) = m;
        }
    }
    if (b < 64 && tid < 128) *(u32x4*)(p.ws + OFF_FLAG + (size_t)b * 2048 + tid * 16) = (u32x4){0, 0, 0, 0};
    if (mat == 0) {
#pragma unroll
        for (int i = 0; i < 7; ++i) *(u32x4*)(lds + (i * 8 + w) * 1024 + (lp * 16 + lr) * 16) = stg[i];
    } else {
#pragma unroll
        for (int i = 0; i < 7; ++i) { const int n = tid + 512 * i, e = n / 14, ch = n % 14; *(u32x4*)(lds + e * 240 + ch * 16) = stg[i]; }
    }
    { const int o0 = tid * 16, o1 = 8192 + tid * 16;
      *(u32x4*)(lds + K_XT + (o0 / 896) * 912 + (o0 % 896)) = xt0;
      if (tid < 384) *(u32x4*)(lds + K_XT + (o1 / 896) * 912 + (o1 % 896)) = xt1; }
    if (fw < 1344) {
        const u32x4 o = {ew[0] | ((unsigned)ew[1] << 16), ew[2] | ((unsigned)ew[3] << 16), ew[4] | ((unsigned)ew[5] << 16), ew[6] | ((unsigned)ew[7] << 16)};
        *(u32x4*)(p.ws + OFF_BW + (size_t)fw * FR + lane * 16) = o;
    }
    if (w < 7) {
        char* scr = lds + K_SCR + w * 8448;
#pragma unroll
        for (int bb = 0; bb < 2; ++bb) {
#pragma unroll
            for (int k = 0; k < 8; ++k) *(u32x4*)(scr + lr * 528 + 64 * k + 16 * lp) = rw[bb][k];
            asm volatile("s_waitcnt lgkmcnt(0)" ::: "memory");
#pragma unroll
            for (int k = 0; k < 8; ++k) rw[bb][k] = *(const u32x4*)(scr + l15 * 528 + 64 * k + 16 * q4);
            asm volatile("s_waitcnt lgkmcnt(0)" ::: "memory");
        }
    }
    __syncthreads();
#pragma unroll
    for (int j = 0; j < 2; ++j) {
        const int tt = w + 8 * j;
        if (tt < 14) {
            const u32x4 v = *(const u32x4*)(lds + K_XT + l15 * 912 + kcol(tt, q4) * 2);
            *(u32x4*)(p.ws + ((b & 1) ? OFF_BX : OFF_XF) + ((size_t)(b >> 1) * 14 + tt) * FR + lane * 16) = v;
        }
    }
    if (w < 7) {
        auto ldcf = [&](int i, u32x4 (&cf)[8]) {
#pragma unroll
            for (int k = 0; k < 8; ++k) {
                if (mat == 0) cf[k] = *(const u32x4*)(lds + (i * 8 + k) * 1024 + lane * 16);
                else {
                    const int qq = l15 >> 2, pp = l15 & 3;
                    const int row0 = 32 * k + 8 * q4 + qq;
                    typedef __attribute__((ext_vector_type(4))) short v4s;
                    v4s lo = __builtin_amdgcn_ds_read_tr16_b64_v4i16((LAS v4s*)(lds + row0 * 240 + (16 * i + 4 * pp) * 2));
                    v4s hi = __builtin_amdgcn_ds_read_tr16_b64_v4i16((LAS v4s*)(lds + (row0 + 4) * 240 + (16 * i + 4 * pp) * 2));
                    u32x2 l2 = __builtin_bit_cast(u32x2, lo), h2 = __builtin_bit_cast(u32x2, hi);
                    cf[k] = (u32x4){l2.x, l2.y, h2.x, h2.y};
                }
            }
        };
        u32x4 cfa[8], cfb[8];
        auto tile = [&](int i, const u32x4 (&cf)[8]) {
            f32x4 a0 = {0, 0, 0, 0}, a1 = {0, 0, 0, 0};
#pragma unroll
            for (int k = 0; k < 8; ++k) { a0 = mfma32(rw[0][k], cf[k], a0); a1 = mfma32(rw[1][k], cf[k], a1); }
            const u32x4 o = pack8(a0, a1);
            if (mat == 0) {
                const int ct = 7 * cg + i;
                *(u32x4*)(p.ws + OFF_AF + ((size_t)(h * 28 + ct) * 14 + t) * FR + lane * 16) = o;
            } else {
                const int beta = 14 * cg + 2 * i + (l15 >> 3);
                *(u32x4*)(p.ws + OFF_BF + ((size_t)((h >> 1) * 56 + beta) * 14 + t) * FR + (16 * q4 + 8 * (h & 1) + (l15 & 7)) * 16) = o;
            }
        };
        ldcf(0, cfa);
#pragma unroll
        for (int i = 0; i < 7; i += 2) {
            if (i + 1 < 7) ldcf(i + 1, cfb);
            tile(i, cfa);
            if (i + 2 < 7) ldcf(i + 2, cfa);
            if (i + 1 < 7) tile(i + 1, cfb);
        }
    }
}

constexpr int SPIN_MAX = 200000;
DEVI unsigned* flagp(const Params& p, int idx) { return (unsigned*)(p.ws + OFF_FLAG) + idx; }
DEVI void signal(const Params& p, int idx) { __hip_atomic_fetch_add(flagp(p, idx), 1u, __ATOMIC_RELAXED, __HIP_MEMORY_SCOPE_AGENT); }
DEVI unsigned ldflag(const Params& p, int idx) { return __hip_atomic_load(flagp(p, idx), __ATOMIC_RELAXED, __HIP_MEMORY_SCOPE_AGENT); }
DEVI void waitflag(const Params& p, int idx, unsigned target, bool& dead, int sleep = 4) {
    int k = dead ? SPIN_MAX : 0;
    _Pragma("nounroll") for (; k < SPIN_MAX; ++k) {
        const unsigned v = __builtin_amdgcn_readfirstlane(ldflag(p, idx));
        if (v >= target) break;
        __builtin_amdgcn_s_sleep(4);
    }
    if (k >= SPIN_MAX) dead = true;
    MEMFENCE();
}

DEVI void waitall(const Params& p, int idx, int n, bool& dead) {
    int k = dead ? SPIN_MAX : 0;
    const int _l = (threadIdx.x & 63) & (n - 1);
    _Pragma("nounroll") for (; k < SPIN_MAX; ++k) {
        const unsigned v = ldflag(p, idx + _l);
        if (!__any(v == 0u)) break;
        __builtin_amdgcn_s_sleep(8);
    }
    if (k >= SPIN_MAX) dead = true;
    MEMFENCE();
}

DEVI void gemm_worker(const Params& p, int u, char* lds) {
    const int tid = threadIdx.x, lane = tid & 63, w = __builtin_amdgcn_readfirstlane(tid >> 6);
    const rsrc_t rs = mkrsrc(p.ws, 0x10000000u);
    const int isU = u >> 6, par = (u >> 2) & 1, v = ((((u & 3) | (((u & 63) >> 3) << 2))) << 1) | par;
    u32x4 wt[2][14];
    unsigned vo_out[2];
    int flag_idx, flag_stride;
    u32x4 x0[4], x1[4];
#pragma unroll
    for (int j = 0; j < 4; ++j) { const int f = (w + 8 * j < 28) ? w + 8 * j : w;
        x0[j] = *(const u32x4*)(p.ws + OFF_XF + ((size_t)par * 28 + f) * FR + lane * 16);
        x1[j] = *(const u32x4*)(p.ws + OFF_XF + ((size_t)(par + 2) * 28 + f) * FR + lane * 16); }
    {
        const int ww = (w < 7) ? w : 0;
        const char* base;
        if (!isU) {
            const int h = v >> 2, tp = 7 * ((v >> 1) & 1) + ww; base = p.ws + OFF_AF + (size_t)(h * 28 + 2 * tp) * 14 * FR;
            vo_out[0] = (unsigned)(OFF_GX + ((size_t)(h * 2 + 0) * 14 + tp) * FR + lane * 16);
            vo_out[1] = (unsigned)(OFF_GX + ((size_t)(h * 2 + 1) * 14 + tp) * FR + lane * 16);
            flag_idx = FLAG_GX + h; flag_stride = 16;
        } else {
            const int hp = v >> 3, b0 = 14 * ((v >> 1) & 3) + 2 * ww; base = p.ws + OFF_BF + (size_t)(hp * 56 + b0) * 14 * FR;
            vo_out[0] = (unsigned)(OFF_UF + ((size_t)(b0 + 0) * 8 + hp) * FR + lane * 16);
            vo_out[1] = (unsigned)(OFF_UF + ((size_t)(b0 + 1) * 8 + hp) * FR + lane * 16);
            flag_idx = FLAG_U + ((v >> 1) & 3); flag_stride = 16;
        }
        if (w < 7) {
#pragma unroll
            for (int a = 0; a < 2; ++a)
#pragma unroll
                for (int t = 0; t < 14; ++t) wt[a][t] = *(const u32x4*)(base + (size_t)(a * 14 + t) * FR + lane * 16);
        } else {
#pragma unroll
            for (int a = 0; a < 2; ++a)
#pragma unroll
                for (int t = 0; t < 14; ++t) wt[a][t] = (u32x4){0, 0, 0, 0};
        }
    }
    WAITVM0();
    constexpr unsigned SEG_STRIDE = 16 * 28 * 1024;
    int buf = 0;
#pragma unroll
    for (int j = 0; j < 4; ++j) { const int f = w + 8 * j; if (f < 28) { *(u32x4*)(lds + f * 1024 + lane * 16) = x0[j]; *(u32x4*)(lds + 28672 + f * 1024 + lane * 16) = x1[j]; } }
    bar();
    for (int s = par; s < NSEG; s += 2) {
        u32x4 nx[4];
        const bool more = (s + 2 < NSEG) && (s >= 2);
        if (more) {
#pragma unroll
            for (int j = 0; j < 4; ++j) { const int f = (w + 8 * j < 28) ? w + 8 * j : w; nx[j] = *(const u32x4*)(p.ws + OFF_XF + ((size_t)(s + 2) * 28 + f) * FR + lane * 16); }
        }
        MEMFENCE();
        if (w < 7) {
            f32x4 acc[2][2];
#pragma unroll
            for (int a = 0; a < 2; ++a)
#pragma unroll
                for (int kt = 0; kt < 2; ++kt) acc[a][kt] = (f32x4){0, 0, 0, 0};
            const char* xb = lds + buf * 28672 + lane * 16;
            if (!isU) {
#pragma unroll
                for (int t = 0; t < 14; ++t)
#pragma unroll
                    for (int kt = 0; kt < 2; ++kt) {
                        const u32x4 xf = *(const u32x4*)(xb + (kt * 14 + t) * 1024);
                        acc[0][kt] = mfma32(wt[0][t], xf, acc[0][kt]); acc[1][kt] = mfma32(wt[1][t], xf, acc[1][kt]);
                    }
            } else {
#pragma unroll
                for (int t = 0; t < 14; ++t)
#pragma unroll
                    for (int kt = 0; kt < 2; ++kt) {
                        const u32x4 xf = *(const u32x4*)(xb + (kt * 14 + t) * 1024);
                        acc[0][kt] = mfma32(xf, wt[0][t], acc[0][kt]); acc[1][kt] = mfma32(xf, wt[1][t], acc[1][kt]);
                    }
            }
            WAITVM0();
            u32x4 o0, o1;
            if (!isU) { o0 = pack8(acc[0][0], acc[1][0]); o1 = pack8(acc[0][1], acc[1][1]); }
            else { o0 = pack8(acc[0][0], acc[0][1]); o1 = pack8(acc[1][0], acc[1][1]); }
            bst4(rs, o0, vo_out[0], (unsigned)s * SEG_STRIDE);
            bst4(rs, o1, vo_out[1], (unsigned)s * SEG_STRIDE);
        }
        if (more) {
#pragma unroll
            for (int j = 0; j < 4; ++j) { const int f = w + 8 * j; if (f < 28) *(u32x4*)(lds + (buf ^ 1) * 28672 + f * 1024 + lane * 16) = nx[j]; }
        }
        if (s < 2) WAITVM0();
        bar();
        if (s < 2) { if (tid == 448) signal(p, flag_idx + s * flag_stride);
        }
        else if (s >= 4 && tid == 448) signal(p, flag_idx + (s - 2) * flag_stride);
        buf ^= 1;
    }
    WAITVM0();
    bar();
    if (tid == 448) signal(p, flag_idx + (NSEG - 2 + par) * flag_stride);
}

constexpr float SC2 = 0.0625f * 1.4426950408889634f;
constexpr int L_R = 0;
constexpr int L_RED1 = 7168;
constexpr int L_RED2 = L_RED1 + 4352;
constexpr int L_REDX = L_RED2 + 2432;
constexpr int L_XX = L_REDX + 8192;
constexpr int L_CFG = L_XX + 73728;

DEVI unsigned chunk_off(const Params& p, unsigned cfg, int s, int j) {
    const int pg = s >> 5;
    return (unsigned)p.comm_off + (unsigned)pg * (2u << 20) + (unsigned)(2 * (4 * (s & 31) + j) + ((cfg >> pg) & 1)) * 8192u;
}

DEVI unsigned ts_off(const Params& p, unsigned cfg, int s) { return (unsigned)p.comm_off + (4u << 20) + (unsigned)(2 * (s >> 6) + ((cfg >> 2) & 1)) * 8192u + (unsigned)(s & 63) * 128u; }
DEVI unsigned rt32() { return (unsigned)__builtin_amdgcn_s_memrealtime(); }
DEVI void wait_until(unsigned target) {
    const unsigned now = rt32();
    const int d = (int)(target - now);
    if (d <= 0) return;
    if (d > 2000) target = now + 2000u;
    _Pragma("nounroll") while ((int)(rt32() - target) < 0) { }
}
#define C2T 110
#define O1T 110
#define C2BOOT 260
#define O1BOOT 200
#define C2H 98
#define GDELAY 100
#define XHD 6
#define BULKD 4
#define CUP 40
#define XOFFT 0
#define DEXTRA 0
#define STARTD 60
DEVI void probe_cfg(const Params& p, rsrc_t rs, int idx) {
    const int pg = idx >> 1, par = idx & 1;
    unsigned best = 1u << 20;
    for (int k = 0; k < 4; ++k) {
        const unsigned off = (unsigned)p.comm_off + pg * (2u << 20) + (unsigned)((pg ? 2 * (4 * 31 + 1) : 0) + par) * 8192u + 512u * k;
        const unsigned long long c0 = __builtin_amdgcn_s_memtime();
        unsigned vv = bld1(rs, off, 0);
        asm volatile("s_waitcnt vmcnt(0)" : "+v"(vv) :: "memory");
        const unsigned long long c1 = __builtin_amdgcn_s_memtime();
        const unsigned dt = (unsigned)(c1 - c0);
        if (k > 0 && dt < best) best = dt;
    }
    if ((threadIdx.x & 63) == 0) bst1(rs, best + 1u, OFF_FLAG + (FLAG_CFG + idx) * 4, 0);
}
DEVI unsigned get_cfg(const Params& p, rsrc_t rs, char* lds) {
    const int tid = threadIdx.x, lane = tid & 63, w = __builtin_amdgcn_readfirstlane(tid >> 6);
    if (w == 7) {
        unsigned v = 0;
        for (int it = 0; it < SPIN_MAX; ++it) { v = bld1(rs, OFF_FLAG + (FLAG_CFG + (lane & 3)) * 4, 0); MEMFENCE(); if (!__any(v == 0u)) break; __builtin_amdgcn_s_sleep(2); }
        const unsigned l0 = __builtin_amdgcn_readlane(v, 0), l1 = __builtin_amdgcn_readlane(v, 1), l2 = __builtin_amdgcn_readlane(v, 2), l3 = __builtin_amdgcn_readlane(v, 3);
        unsigned cfg = (l1 < l0 ? 1u : 0u) | (l3 < l2 ? 2u : 0u);
        if (lane == 0) *(unsigned*)(lds + L_CFG) = cfg;
    }
    __syncthreads();
    return __builtin_amdgcn_readfirstlane(*(unsigned*)(lds + L_CFG));
}
DEVI unsigned start_sync(const Params& p, u32x4 ds, int r, char* lds) {
    const int tid = threadIdx.x, lane = tid & 63, w = __builtin_amdgcn_readfirstlane(tid >> 6);
    bar();
    if (w == 3) {
        const unsigned base = rt32();
        if (lane == 0) AST1(base | 1u, (unsigned)(OFF_FLAG + (FLAG_RDY + r) * 4), ds, 0u);
        unsigned v0 = 0, v1 = 0;
        const unsigned vo = (unsigned)(OFF_FLAG + (FLAG_RDY + lane) * 4);
        for (int it = 0; it < SPIN_MAX; ++it) {
            ALD1R(v0, vo, ds, 0u); ALD1R(v1, vo, ds, 256u);
            asm volatile("s_waitcnt vmcnt(0)" : "+v"(v0), "+v"(v1) :: "memory");
            if (!__any(v0 == 0u || v1 == 0u)) break;
            __builtin_amdgcn_s_sleep(2);
        }
        int d = max((int)(v0 - base), (int)(v1 - base));
        d = max(d, (int)dppu<DPP_QP_1032>((unsigned)d));
        d = max(d, (int)dppu<DPP_QP_2301>((unsigned)d));
        d = max(d, (int)dppu<DPP_ROR4>((unsigned)d));
        d = max(d, (int)dppu<DPP_ROR8>((unsigned)d));
        { unsigned a, b2; uswap16((unsigned)d, a, b2); d = max((int)a, (int)b2); uswap32((unsigned)d, a, b2); d = max((int)a, (int)b2); }
        if (lane == 0) *(unsigned*)(lds + L_CFG + 4) = base + (unsigned)d + STARTD;
    }
    bar();
    return __builtin_amdgcn_readfirstlane(*(unsigned*)(lds + L_CFG + 4));
}

#define D1 0
#define D2 0
template <int DELAY> DEVI void pause() { if (DELAY > 0) __builtin_amdgcn_s_sleep(DELAY); }

#define EV(k)
#define STAMP(k)

constexpr int L_TS = L_CFG + 64;
constexpr int L_RF = L_CFG + 128;
constexpr int L_PU = L_CFG + 256;
constexpr int L_PX = L_PU + 8192;
constexpr int L_GX = L_CFG + 256;
#define WSPLIT 5
#define MINH 30
#define MINM 30
DEVI unsigned tmax(unsigned a, unsigned b) { return ((int)(a - b) > 0) ? a : b; }

DEVI void await_flag(u32x4 ds, unsigned vo, unsigned target, bool& dead) {
    int spins = 0; const int lim = dead ? 1 : SPIN_MAX; bool bad;
    _Pragma("nounroll") do {
        unsigned v; ALD1(v, vo, ds, 0u);
        asm volatile("s_waitcnt vmcnt(0)" : "+v"(v) :: "memory");
        bad = (unsigned)__builtin_amdgcn_readfirstlane(v) < target;
        if (bad) __builtin_amdgcn_s_sleep(2);
    } while (bad && ++spins < lim);
    if (bad) dead = true;
}
DEVI void await_all(u32x4 ds, unsigned vo, bool& dead) {
    int spins = 0; const int lim = dead ? 1 : SPIN_MAX; bool bad;
    _Pragma("nounroll") do {
        unsigned v; ALD1(v, vo, ds, 0u);
        asm volatile("s_waitcnt vmcnt(0)" : "+v"(v) :: "memory");
        bad = __any(v == 0u);
        if (bad) __builtin_amdgcn_s_sleep(2);
    } while (bad && ++spins < lim);
    if (bad) dead = true;
}
#define LAT0()
#define LAT1(k)
#define WT(k)
#define TA0(v)
#define TA1(v, k, cond)

#define PTA()
#define PTB(k)
template <bool V> struct BoolC { static constexpr bool value = V; };

DEVI void chain_gw(const Params& p, int r, char* lds, u32x4 ds, unsigned t_entry) {
    const rsrc_t rs = mkrsrc(p.ws, 0x10000000u);
    const int tid = threadIdx.x, lane = tid & 63, w = __builtin_amdgcn_readfirstlane(tid >> 6);
    const int col = lane & 15, q4 = lane >> 4;
    const int h = r / 7, c7 = r % 7, beta = r >> 1, rh = r & 1;
    u32x4 aw[2][2][4];
    const int nq = (w < 3) ? 2 : 1;
    const int qf0 = (c7 + 2 * w) % 7, qf1 = (c7 + 2 * w + nq - 1) % 7;
#pragma unroll
    for (int qi = 0; qi < 2; ++qi)
#pragma unroll
        for (int e = 0; e < 2; ++e)
#pragma unroll
            for (int tau = 0; tau < 4; ++tau) {
                const int q = qi ? qf1 : qf0;
                aw[qi][e][tau] = *(const u32x4*)(p.ws + OFF_AF + ((size_t)(h * 28 + 4 * c7 + tau) * 14 + 2 * q + e) * FR + lane * 16);
                if (qi == 1 && nq == 1) aw[qi][e][tau] = (u32x4){0, 0, 0, 0};
            }
    unsigned mE = (col < 8) ? 0xFFFFFFFFu : 0u, nE = ~mE;
    asm volatile("" : "+v"(mE), "+v"(nE));
    const unsigned cfg = get_cfg(p, rs, lds);
    WAITVM0();
    bool dead = false;
    const unsigned vo_m0 = (unsigned)(qf0 * 1024 + lane * 16), vo_m1 = (unsigned)(qf1 * 1024 + lane * 16);
    const unsigned vo_pp = (unsigned)((((lane >> 4) & 1) ? 16384 : 32768) + h * 512 + c7 * 64 + (lane & 15) * 4);
    const unsigned vo_mp = (unsigned)(beta * 128 + rh * 64 + (lane & 15) * 4);
    const unsigned vo_xm = (unsigned)(OFF_XM + ((size_t)h * 7 + c7) * 1024 + lane * 16);
    const unsigned vo_fxm = (unsigned)(OFF_FLAG + (FLAG_XM + h * 16 + c7) * 4), vo_fw = (unsigned)(OFF_FLAG + (FLAG_W + (beta >> 3) * 16 + (beta & 7)) * 4);
    const unsigned vo_ts = (unsigned)(TS_OFF + (lane & 1) * 4);
    u32x4 R0 = {0,0,0,0}, R1 = {0,0,0,0};
    unsigned ts1 = 0;
    unsigned tm = 0, th = 0;
    volatile LAS unsigned* ldist = (volatile LAS unsigned*)((LAS char*)lds + L_RF + 4);
    bool dh_prev = false;
    LAS char* lbase = (LAS char*)lds;
    LAS char* lr0 = lbase + L_R + vo_m0; LAS char* lr1 = lbase + L_R + vo_m1; LAS char* lrc = lbase + L_R + c7 * 1024 + lane * 16;
    volatile LAS unsigned* rflag = (volatile LAS unsigned*)(lbase + L_RF + (lane & 0));
    volatile LAS unsigned* rflagw = (volatile LAS unsigned*)(lbase + (w == 0 ? L_RF : L_RF + 16 + 4 * (w & 3)) + (lane & 0));
    asm volatile("" : "+v"(lr0), "+v"(lr1), "+v"(lrc), "+v"(rflag), "+v"(rflagw));
    if (tid == 0) { *rflag = 0u; ldist[0] = 0u; ldist[1] = 0u; }
    tm = start_sync(p, ds, r, lds);
    __builtin_amdgcn_s_setprio(2);
    th = tm + O1T;
    auto step = [&](const int s, auto LASTC) {
        constexpr bool LAST = decltype(LASTC)::value;
        const unsigned oM = chunk_off(p, cfg, s, 0);
        u32x4 gE, gO;
        unsigned sv1 = (unsigned)(s + 1); PIN(sv1);
        wait_until(tm);
        if (w == 0) EV(4);
        f32x4 acc[4];
        u32x4 Re1;
        unsigned mx;
        auto ghalf1 = [&]() {
            ALD4R(R0, vo_m0, ds, oM);
            ALD4R(R1, vo_m1, ds, oM);
            asm volatile("s_waitcnt vmcnt(1)" : "+v"(R0) :: "memory");
            SCHEDB();
            u32x4 Re = R0 & mE;
            PIN(Re);
            SCHEDB();
            acc[0] = mfma32(aw[0][0][0], Re, (f32x4){0, 0, 0, 0});
            *(volatile LAS u32x4*)(lr0) = R0;
            SCHEDB();
            u32x4 Ro;
            acc[1] = mfma32(aw[0][0][1], Re, (f32x4){0, 0, 0, 0});
            Ro.x = R0.x & nE; Ro.y = R0.y & nE;
            SCHEDB();
            acc[2] = mfma32(aw[0][0][2], Re, (f32x4){0, 0, 0, 0});
            Ro.z = R0.z & nE; Ro.w = R0.w & nE;
            PIN(Ro);
            SCHEDB();
            acc[3] = mfma32(aw[0][0][3], Re, (f32x4){0, 0, 0, 0});
            mx = max(max(R0.x, R0.y), R0.z); mx = max(mx, R0.w);
            PIN(mx);
            SCHEDB();
            acc[0] = mfma32(aw[0][1][0], Ro, acc[0]);
            SCHEDB();
            asm volatile("s_waitcnt vmcnt(0)" : "+v"(R1) :: "memory");
            SCHEDB();
            *(volatile LAS u32x4*)(lr1) = R1;
            SCHEDB();
            acc[1] = mfma32(aw[0][1][1], Ro, acc[1]);
            mx = max(max(mx, R1.x), R1.y); mx = max(max(mx, R1.z), R1.w);
            PIN(mx);
            SCHEDB();
            acc[2] = mfma32(aw[0][1][2], Ro, acc[2]);
            Re1.x = R1.x & mE; Re1.y = R1.y & mE;
            SCHEDB();
            acc[3] = mfma32(aw[0][1][3], Ro, acc[3]);
            Re1.z = R1.z & mE; Re1.w = R1.w & mE;
            PIN(Re1);
            SCHEDB();
        };
        ghalf1();
        if (__builtin_expect(__any(mx == POISON), 0)) {
            int spins = 0; const int lim = dead ? 1 : SPIN_MAX; bool bad;
            ldist[0] = sv1;
            _Pragma("nounroll") do { ghalf1(); bad = __any(mx == POISON); } while (bad && ++spins < lim);
            if (bad) dead = true;
        }
        if (w == 0) EV(1);
        *rflagw = sv1;
        unsigned fl = *rflag;
        u32x4 Rc = *(volatile LAS u32x4*)(lrc);
        SCHEDB();
        u32x4 Rce, Rco, Ro1;
        acc[0] = mfma32(aw[1][0][0], Re1, acc[0]);
        Ro1.x = R1.x & nE; Ro1.y = R1.y & nE;
        SCHEDB();
        acc[1] = mfma32(aw[1][0][1], Re1, acc[1]);
        Ro1.z = R1.z & nE; Ro1.w = R1.w & nE;
        PIN(Ro1);
        SCHEDB();
        acc[2] = mfma32(aw[1][0][2], Re1, acc[2]);
        SCHEDB();
        acc[3] = mfma32(aw[1][0][3], Re1, acc[3]);
        SCHEDB();
        if (__builtin_expect((unsigned)__builtin_amdgcn_readfirstlane(fl) != (unsigned)(s + 1), 0)) {
            int g = 0;
            _Pragma("nounroll") do { fl = *rflag; Rc = *(volatile LAS u32x4*)(lrc); }
            while ((unsigned)__builtin_amdgcn_readfirstlane(fl) != (unsigned)(s + 1) && ++g < (1 << 22));
        }
        SCHEDB();
        acc[0] = mfma32(aw[1][1][0], Ro1, acc[0]);
        Rce.x = Rc.x & mE; Rce.y = Rc.y & mE;
        SCHEDB();
        acc[1] = mfma32(aw[1][1][1], Ro1, acc[1]);
        Rce.z = Rc.z & mE; Rce.w = Rc.w & mE;
        PIN(Rce);
        SCHEDB();
        acc[2] = mfma32(aw[1][1][2], Ro1, acc[2]);
        Rco.x = dppu<DPP_ROR8>(Rc.x) & mE; Rco.y = dppu<DPP_ROR8>(Rc.y) & mE;
        SCHEDB();
        acc[3] = mfma32(aw[1][1][3], Ro1, acc[3]);
        Rco.z = dppu<DPP_ROR8>(Rc.z) & mE; Rco.w = dppu<DPP_ROR8>(Rc.w) & mE;
        PIN(Rco);
        SCHEDB();
        gE = pack8(acc[0], acc[1]);
        f32x4 d2 = mfma32(gE, Rce, (f32x4){0, 0, 0, 0});
        gO = pack8(acc[2], acc[3]);
        d2 = mfma32(gO, Rco, d2);
        if (col < 8) {
            float* red = (float*)(lds + L_RED1) + (col * 4 + 2 * (q4 & 1)) * 20 + w * 4 + (q4 >> 1) * 2;
            *(f32x2*)(red) = (f32x2){d2[0], d2[1]};
            *(f32x2*)(red + 20) = (f32x2){d2[2], d2[3]};
        }
        bar();
        if (!LAST && w == 0) {
            const float* red = (const float*)(lds + L_RED1) + (lane & 31) * 20;
            const f32x4 a = *(const f32x4*)(red), b2 = *(const f32x4*)(red + 4), c = *(const f32x4*)(red + 8), d = *(const f32x4*)(red + 12);
            const f32x4 t4 = (a + b2) + (c + d);
            const unsigned o = nopoison(pk2(t4.x + t4.z, t4.y + t4.w));
            if (lane < 32) AST1(o, vo_pp, ds, oM);
            EV(2);
            SCHEDB();
        }
        unsigned tb1 = 0, the = 0;
        if (!LAST && w == 1) {
            tb1 = rt32();
            the = tmax(th, tb1 + MINH);
            if (r == 0) {
                const bool dist0 = dh_prev || (unsigned)__builtin_amdgcn_readfirstlane(ldist[0]) == (unsigned)(s + 1);
                const unsigned tv = nopoison(the + (unsigned)(C2H + (dist0 ? CUP : 0)) + ((lane & 1) ? (unsigned)O1T : 0u));
                if (lane < 2) AST1(tv, vo_ts, ds, oM);
            }
            SCHEDB();
        }
#pragma unroll
        for (int it = 0; it < 2; ++it) {
            const char* pxb = lds + L_PX + lane * 16;
            const u32x4 x0 = *(const u32x4*)(pxb + (it * 2 + 0) * 1024), x1 = *(const u32x4*)(pxb + (it * 2 + 1) * 1024);
            f32x4 dx = mfma32(gE, x0, (f32x4){0, 0, 0, 0});
            dx = mfma32(gO, x1, dx);
            *(f32x4*)(lds + L_REDX + (2 * w + (q4 >> 1)) * 1024 + ((it * 2 + (q4 & 1)) * 16 + col) * 16) = dx;
        }
        if (!LAST && w == 1) {
            wait_until(the);
            ALD1R(ts1, vo_ts, ds, oM);
            asm volatile("s_waitcnt vmcnt(0)" : "+v"(ts1) :: "memory");
            if (__builtin_expect(__any(ts1 == POISON), 0)) {
                int spins = 0; const int lim = dead ? 1 : SPIN_MAX; bool bad;
                ldist[1] = sv1;
                _Pragma("nounroll") do {
                    ALD1R(ts1, vo_ts, ds, oM);
                    asm volatile("s_waitcnt vmcnt(0)" : "+v"(ts1) :: "memory");
                    bad = __any(ts1 == POISON);
                } while (bad && ++spins < lim);
                if (bad) dead = true;
            }
            if (lane < 2) *(unsigned*)(lds + L_TS + lane * 4) = ts1;
        }
        bar();
        if (!LAST && w == 3) {
            const float* red = (const float*)(lds + L_RED2) + (lane & 15) * 36 + ((lane >> 4) & 1) * 16;
            const f32x4 a = *(const f32x4*)(red), b2 = *(const f32x4*)(red + 4), c = *(const f32x4*)(red + 8), d = *(const f32x4*)(red + 12);
            const f32x4 t4 = (a + b2) + (c + d);
            float v0 = t4.x + t4.z, v1 = t4.y + t4.w;
            { float x0, x1; swap16(v0, x0, x1); v0 = x0 + x1; swap16(v1, x0, x1); v1 = x0 + x1; }
            const unsigned o = nopoison(pk2(v0, v1));
            if (lane < 16) AST1(o, vo_mp, ds, chunk_off(p, cfg, s + 1, 0));
            EV(0);
            SCHEDB();
        }
        const unsigned tb2 = rt32() - (w == 3 ? 9u : 0u);
        if (w == 1) EV(5);
        if (w == 2) {
            f32x4 t4 = *(const f32x4*)(lds + L_REDX + lane * 16);
#pragma unroll
            for (int pp = 1; pp < 8; ++pp) t4 += *(const f32x4*)(lds + L_REDX + pp * 1024 + lane * 16);
            const u32x4 xm = __builtin_bit_cast(u32x4, t4);
            AST4(xm, vo_xm, ds, (unsigned)s * (16u * 7u * 1024u));
            if (s >= 1) {
                if (lane == 0) AST1(1u, vo_fxm, ds, (unsigned)(s - 1) * 1024u);
                if (rh == 0 && lane == 0) AST1(1u, vo_fw, ds, (unsigned)(s - 1) * 512u);
            }
        }
        if (!LAST) {
          const u32x2 t2 = *(const u32x2*)(lds + L_TS);
          dh_prev = (unsigned)__builtin_amdgcn_readfirstlane(ldist[1]) == (unsigned)(s + 1);
          tm = tmax((unsigned)__builtin_amdgcn_readfirstlane(t2.x), tb2 + MINM); th = (unsigned)__builtin_amdgcn_readfirstlane(t2.y);
        }
    };
    _Pragma("nounroll") for (int s = 0; s < NSEG - 1; ++s) step(s, BoolC<false>{});
    step(NSEG - 1, BoolC<true>{});
    AWAIT(0);
    bar();
    if (w == 2) {
        if (rh == 0 && lane == 0) AST1(1u, vo_fw, ds, (unsigned)(NSEG - 1) * 512u);
        if (lane == 0) AST1(1u, vo_fxm, ds, (unsigned)(NSEG - 1) * 1024u);
    }
}

DEVI void chain_bw(const Params& p, int r, char* lds, u32x4 ds) {
    const rsrc_t rs = mkrsrc(p.ws, 0x10000000u);
    const int tid = threadIdx.x, lane = tid & 63, w = __builtin_amdgcn_readfirstlane(tid >> 6), hw = w & 3;
    const int col = lane & 15, q4 = lane >> 4;
    const int c7 = r % 7, beta = r >> 1, rh = r & 1;
    u32x4 bw[2][14];
#pragma unroll
    for (int a = 0; a < 2; ++a)
#pragma unroll
        for (int t = 0; t < 14; ++t) bw[a][t] = *(const u32x4*)(p.ws + OFF_BF + ((size_t)((2 * hw + a) * 56 + beta) * 14 + t) * FR + lane * 16);
    u32x4 sel;
    { const int j = col & 7; const unsigned one = (j & 1) ? 0x3F800000u : 0x00003F80u; sel.x = (j >> 1) == 0 ? one : 0; sel.y = (j >> 1) == 1 ? one : 0; sel.z = (j >> 1) == 2 ? one : 0; sel.w = (j >> 1) == 3 ? one : 0; }
    const unsigned cfg = get_cfg(p, rs, lds);
    WAITVM0();
    bool dead = false;
    const int hh = col >> 2, i2 = col & 3, hB = 4 * hw + hh;
    const unsigned vo_pa = (unsigned)((rh ? 16384 : 32768) + hB * 512 + q4 * 64 + i2 * 16);
    const unsigned vo_pb = (q4 == 3) ? OOB : vo_pa + 256u;
    const unsigned vo_stat = (unsigned)(STATS_OFF + rh * 512 + (hB * 4 + i2) * 4);
    const unsigned vo_ex = (unsigned)(49152 + rh * 4096 + hB * 256 + q4 * 64 + i2 * 16);
    const unsigned vo_u0 = (unsigned)(OFF_UF + ((size_t)beta * 8 + 2 * hw) * FR + lane * 16), vo_u1 = vo_u0 + 1024;
    const unsigned vo_x = (unsigned)(OFF_XF + (size_t)((hw >> 1) * 14 + 2 * c7 + (hw & 1)) * FR + lane * 16);
    const unsigned vo_fu = (unsigned)(OFF_FLAG + (FLAG_U + beta / 14) * 4);
    const unsigned vo_wc = (unsigned)(OFF_WC + ((size_t)beta * 8 + 2 * hw) * 256 + col * 16);
    const bool diag = (col >> 3) == (q4 >> 1);
    const bool lowh = hh < 2, outv = (hh & 1) == (q4 >> 1);
    u32x4 un0 = {0,0,0,0}, un1 = {0,0,0,0}, px = {0,0,0,0};
    unsigned fu = 0;
    u32x4 pa = {0,0,0,0}, pb = {0,0,0,0}, ex = {0,0,0,0}; unsigned st = 0;
    unsigned th = 0;
    volatile LAS unsigned* ldist = (volatile LAS unsigned*)((LAS char*)lds + L_RF + 4);
    await_flag(ds, vo_fu, 8, dead);
    ALD4R(un0, vo_u0, ds, 0u); ALD4R(un1, vo_u1, ds, 0u); ALD4PR(px, vo_x, ds, 0u);
    ALD1R(fu, vo_fu, ds, 64u);
    th = start_sync(p, ds, r, lds) + O1T;
    auto step = [&](const int s, auto LASTC) {
        constexpr bool LAST = decltype(LASTC)::value;
        const unsigned oM = chunk_off(p, cfg, s, 0);
        unsigned sv1 = (unsigned)(s + 1); PIN(sv1);
        asm volatile("s_waitcnt vmcnt(0)" : "+v"(un0), "+v"(un1), "+v"(px), "+v"(fu) :: "memory");
        SCHEDB();
        u32x4 uf0 = un0, uf1 = un1;
        asm volatile("" : "+v"(uf0), "+v"(uf1));
        const unsigned fu_s = (unsigned)__builtin_amdgcn_readfirstlane(fu);
        *(u32x4*)(lds + L_PX + hw * 1024 + lane * 16) = px;
        if (s + 1 < NSEG - 1 && fu_s < 8u) await_flag(ds, vo_fu + (unsigned)(s + 1) * 64u, 8, dead);
        bar();
        const unsigned tb1 = rt32();
        SCHEDB();
        u32x4 Rq[7];
#pragma unroll
        for (int q = 0; q < 7; ++q) Rq[q] = *(const u32x4*)(lds + L_R + q * 1024 + lane * 16);
        SCHEDB();
        const unsigned the = tmax(th, tb1 + MINH);
        f32x4 ae[2] = {{0,0,0,0},{0,0,0,0}}, ao[2] = {{0,0,0,0},{0,0,0,0}};
#pragma unroll
        for (int q = 0; q < WSPLIT; ++q)
#pragma unroll
            for (int a = 0; a < 2; ++a) { ae[a] = mfma32(Rq[q], bw[a][2 * q], ae[a]); ao[a] = mfma32(Rq[q], bw[a][2 * q + 1], ao[a]); }
        SCHEDB();
        if (!LAST) {
            wait_until(the);
            if (w == 6) EV(6);
            ALD4R(pa, vo_pa, ds, oM);
            ALD4R(pb, vo_pb, ds, oM);
            ALD1R(st, vo_stat, ds, oM);
            ALD4R(ex, vo_ex, ds, oM);
            SCHEDB();
        }
#pragma unroll
        for (int q = WSPLIT; q < 7; ++q)
#pragma unroll
            for (int a = 0; a < 2; ++a) { ae[a] = mfma32(Rq[q], bw[a][2 * q], ae[a]); ao[a] = mfma32(Rq[q], bw[a][2 * q + 1], ao[a]); }
        u32x2 wop[2]; u32x4 wst[2];
#pragma unroll
        for (int a = 0; a < 2; ++a) {
            float wv[4];
#pragma unroll
            for (int rr = 0; rr < 4; ++rr) { const float x = (lane < 32) ? ae[a][rr] : ao[a][rr]; float u, v; swap32(x, u, v); wv[rr] = u + v; }
            const u32x2 wp = {pk2(wv[0], wv[1]), pk2(wv[2], wv[3])};
            wop[a].x = diag ? wp.x : 0u; wop[a].y = diag ? wp.y : 0u;
            unsigned a0, b0, a1, b1; uswap16(wp.x, a0, b0); uswap16(wp.y, a1, b1); wst[a] = (u32x4){a0, a1, b0, b1};
        }
        asm volatile("" : "+v"(wop[0]), "+v"(wop[1]), "+v"(wst[0]), "+v"(wst[1]));
        SCHEDB();
        if (!LAST) {
            f32x4 ro; unsigned pchk;
            auto bstage = [&]() {
                f32x4 sm = mfma32(sel, pa, (f32x4){0, 0, 0, 0});
                sm = mfma32(sel, pb, sm);
                PIN(sm);
                SCHEDB();
                f32x4 dx0 = mfma32(uf0, ex, (f32x4){0, 0, 0, 0}), dx1 = mfma32(uf1, ex, (f32x4){0, 0, 0, 0});
                f32x4 sx4 = mfma32((u32x4){0x3F803F80u, 0x3F803F80u, 0x3F803F80u, 0x3F803F80u}, ex, (f32x4){0, 0, 0, 0});
                PIN(dx0); PIN(dx1); PIN(sx4);
                const float mxs = __uint_as_float(st), sxs = sx4[0];
                unsigned chk = max(max(max4(pa), max4(pb)), max(max4(ex), st));
                PIN(chk); pchk = chk;
                SCHEDB();
                float ml = fmx(fmx(sm[0], sm[1]), fmx(sm[2], sm[3]));
                { float a, b2; swap16(ml, a, b2); ml = fmx(fmx(a, b2), mxs); }
                const float nM = -ml * SC2;
                float e[4];
#pragma unroll
                for (int rr = 0; rr < 4; ++rr) e[rr] = __builtin_amdgcn_exp2f(__builtin_fmaf(sm[rr], SC2, nM));
                float al = __builtin_amdgcn_exp2f(__builtin_fmaf(mxs, SC2, nM));
                PIN(al);
                const u32x2 pp = {pk2(e[0], e[1]), pk2(e[2], e[3])};
                f32x4 c0;
#pragma unroll
                for (int rr = 0; rr < 4; ++rr) c0[rr] = (lowh ? dx0[rr] : dx1[rr]) * al;
                const f32x4 r0 = mfma16(wop[0], pp, c0), r1 = mfma16(wop[1], pp, c0);
                float ls = (e[0] + e[1]) + (e[2] + e[3]);
                { float a, b2; swap16(ls, a, b2); ls = a + b2; }
                const float inv = __builtin_amdgcn_rcpf(__builtin_fmaf(al, sxs, ls));
#pragma unroll
                for (int rr = 0; rr < 4; ++rr) ro[rr] = (lowh ? r0[rr] : r1[rr]) * inv;
            };
            {
                const unsigned sn = (unsigned)(s + 1);
                ALD4R(un0, vo_u0, ds, sn * (56u * 8u * 1024u));
                ALD4R(un1, vo_u1, ds, sn * (56u * 8u * 1024u));
                ALD4PR(px, vo_x, ds, sn * 28672u);
                ALD1R(fu, vo_fu, ds, (sn + 1u) * 64u);
            }
            PTB(0);
            asm volatile("s_waitcnt vmcnt(4)" : "+v"(pa), "+v"(pb), "+v"(st), "+v"(ex) :: "memory");
            PTB(1);
            SCHEDB();
            bstage();
            if (__builtin_expect(__any(pchk == POISON), 0)) {
              int spins = 0; const int lim = dead ? 1 : SPIN_MAX; bool bad;
              ldist[1] = sv1;
              _Pragma("nounroll") do {
                ALD4R(pa, vo_pa, ds, oM);
                ALD4R(pb, vo_pb, ds, oM);
                ALD1R(st, vo_stat, ds, oM);
                ALD4R(ex, vo_ex, ds, oM);
                asm volatile("s_waitcnt vmcnt(0)" : "+v"(pa), "+v"(pb), "+v"(st), "+v"(ex) :: "memory");
                bad = __any(max(max(max4(pa), max4(pb)), max(max4(ex), st)) == POISON);
              } while (bad && ++spins < lim);
              if (bad) dead = true;
              bstage();
            }
            if (w == 7) EV(3);
            if (outv) {
                float* red = (float*)(lds + L_RED2) + (i2 * 4 + 2 * (q4 & 1)) * 36 + hB * 2;
                *(f32x2*)(red) = (f32x2){ro[0], ro[1]};
                *(f32x2*)(red + 36) = (f32x2){ro[2], ro[3]};
            }
            PTB(3);
        }
        bar();
        if (!LAST) PTB(4);
        if (rh == 0 && lane < 16) { AST4(wst[0], vo_wc, ds, (unsigned)s * (56u * 8u * 256u)); AST4(wst[1], vo_wc + 256u, ds, (unsigned)s * (56u * 8u * 256u)); }
        if (!LAST) th = (unsigned)__builtin_amdgcn_readfirstlane(*(const unsigned*)(lds + L_TS + 4));
    };
    _Pragma("nounroll") for (int s = 0; s < NSEG - 1; ++s) step(s, BoolC<false>{});
    step(NSEG - 1, BoolC<true>{});
    AWAIT(0);
    bar();
}

DEVI void chain_g(const Params& p, int r, char* lds) {
    const int w = __builtin_amdgcn_readfirstlane(threadIdx.x >> 6);
    const u32x4 ds = mkdesc(p.ws, 0x10000000u);
    const unsigned t_entry = rt32();
    __builtin_amdgcn_s_sleep(GDELAY);
    if (w < 4) chain_gw(p, r, lds, ds, t_entry); else chain_bw(p, r, lds, ds);
}

struct XBuf { u32x4 g[2][2][2]; u32x4 x[2][2][2]; };

template <int WHAT = 3> DEVI void x_issue(u32x4 ds, XBuf& B, int s, int h, int w, int lane) {
#pragma unroll
    for (int qi = 0; qi < 2; ++qi)
#pragma unroll
        for (int e = 0; e < 2; ++e) {
            const int t = 2 * ((w < 3) ? 2 * w + qi : 2 * w) + e;
#pragma unroll
            for (int k = 0; k < 2; ++k) {
                const unsigned vg = (unsigned)(OFF_GX + ((size_t)(h * 2 + k) * 14 + t) * FR + lane * 16), vx = (unsigned)(OFF_XF + ((size_t)k * 14 + t) * FR + lane * 16);
                if (WHAT & 1) ALD4R(B.g[qi][e][k], vg, ds, (unsigned)s * (16u * 28u * 1024u));
                if (WHAT & 2) ALD4PR(B.x[qi][e][k], vx, ds, (unsigned)s * 28672u);
            }
        }
}
DEVI void x_pin(XBuf& B) {
#pragma unroll
    for (int qi = 0; qi < 2; ++qi)
#pragma unroll
        for (int e = 0; e < 2; ++e) asm volatile("" : "+v"(B.g[qi][e][0]), "+v"(B.g[qi][e][1]), "+v"(B.x[qi][e][0]), "+v"(B.x[qi][e][1]));
}
DEVI void x_consume(const XBuf& B, int s, int w, int lane, char* lds) {
    f32x4 xx[3] = {{0,0,0,0},{0,0,0,0},{0,0,0,0}};
    const int nq = (w < 3) ? 2 : 1;
#pragma unroll
    for (int qi = 0; qi < 2; ++qi) {
        if (qi < nq) {
#pragma unroll
            for (int e = 0; e < 2; ++e) {
                xx[0] = mfma32(B.g[qi][e][0], B.x[qi][e][0], xx[0]);
                xx[1] = mfma32(B.g[qi][e][0], B.x[qi][e][1], xx[1]);
                xx[2] = mfma32(B.g[qi][e][1], B.x[qi][e][1], xx[2]);
#pragma unroll
                for (int k = 0; k < 2; ++k) *(u32x4*)(lds + L_GX + (s & 1) * 28672 + (k * 14 + 2 * (2 * w + qi) + e) * 1024 + lane * 16) = B.g[qi][e][k];
            }
        }
    }
#pragma unroll
    for (int k = 0; k < 3; ++k) *(f32x4*)(lds + L_XX + (s % 6) * 12288 + (w * 3 + k) * 1024 + lane * 16) = xx[k];
}
DEVI void x_xm_issue(u32x4 ds, u32x4 (&xm)[7], int s, int h, int it, int lane, bool on = true) {
    const int col = lane & 15, q4 = lane >> 4;
#pragma unroll
    for (int c = 0; c < 7; ++c) { const unsigned vo = (q4 < 2 && on) ? (unsigned)(OFF_XM + ((size_t)h * 7 + c) * 1024 + ((it * 2 + q4) * 16 + col) * 16) : OOB; ALD4R(xm[c], vo, ds, (unsigned)s * (16u * 7u * 1024u)); }
}
DEVI void x_finalize(u32x4 ds, const u32x4 (&xm)[7], int s, int h, int it, int lane, char* lds) {
    const int col = lane & 15, q4 = lane >> 4;
    f32x4 sm = {0, 0, 0, 0};
#pragma unroll
    for (int c = 0; c < 7; ++c) sm += __builtin_bit_cast(f32x4, xm[c]);
    f32x4 s0 = {0,0,0,0}, s1 = {0,0,0,0};
    const char* xx = lds + L_XX + (s % 6) * 12288 + lane * 16;
#pragma unroll
    for (int ww = 0; ww < 4; ++ww) {
        s0 += *(const f32x4*)(xx + (ww * 3 + it) * 1024);
        s1 += *(const f32x4*)(xx + (ww * 3 + 2) * 1024);
    }
    const float NEG = -3.0e38f;
    float v0[4], v1[4], vm[4];
#pragma unroll
    for (int rr = 0; rr < 4; ++rr) {
        const bool causal = (4 * q4 + rr) <= col;
        v0[rr] = (it == 1 || causal) ? s0[rr] : NEG;
        v1[rr] = (it == 1 && causal) ? s1[rr] : NEG;
        vm[rr] = (q4 < 2) ? sm[rr] : NEG;
    }
    float m = NEG;
#pragma unroll
    for (int rr = 0; rr < 4; ++rr) m = fmaxf(m, fmaxf(fmaxf(v0[rr], v1[rr]), vm[rr]));
    { float a, b2; swap16(m, a, b2); m = fmaxf(a, b2); swap32(m, a, b2); m = fmaxf(a, b2); }
    const float nM = -m * SC2;
    float l = 0;
#pragma unroll
    for (int rr = 0; rr < 4; ++rr) {
        v0[rr] = (v0[rr] == NEG) ? 0.f : __builtin_amdgcn_exp2f(__builtin_fmaf(v0[rr], SC2, nM));
        v1[rr] = (v1[rr] == NEG) ? 0.f : __builtin_amdgcn_exp2f(__builtin_fmaf(v1[rr], SC2, nM));
        vm[rr] = (vm[rr] == NEG) ? 0.f : __builtin_amdgcn_exp2f(__builtin_fmaf(vm[rr], SC2, nM));
        l += v0[rr] + v1[rr] + vm[rr];
    }
    { float a, b2; swap16(l, a, b2); l = a + b2; swap32(l, a, b2); l = a + b2; }
    const float inv = __builtin_amdgcn_rcpf(l);
    const u32x4 px = {pk2(v0[0] * inv, v0[1] * inv), pk2(v0[2] * inv, v0[3] * inv), pk2(v1[0] * inv, v1[1] * inv), pk2(v1[2] * inv, v1[3] * inv)};
    const unsigned vpx = (unsigned)(OFF_PX + ((size_t)h * 2 + it) * FR + lane * 16);
    AST4(px, vpx, ds, (unsigned)s * 32768u);
    if (q4 < 2) { const u32x2 pm = {pk2(vm[0] * inv, vm[1] * inv), pk2(vm[2] * inv, vm[3] * inv)};
        const unsigned vpm = (unsigned)(OFF_PM + ((size_t)(h >> 1) * 2 + it) * 512 + (col + 16 * (2 * (h & 1) + q4)) * 8);
        AST2(pm, vpm, ds, (unsigned)s * 8192u); }
}

DEVI void chain_x(const Params& p, int h, char* lds) {
    const int tid = threadIdx.x, lane = tid & 63, w = __builtin_amdgcn_readfirstlane(tid >> 6);
    const int col = lane & 15, q4 = lane >> 4;
    const rsrc_t rs = mkrsrc(p.ws, 0x10000000u);
    const u32x4 ds = mkdesc(p.ws, 0x10000000u);
    if (w == 0 && (h & 3) == 0) probe_cfg(p, rs, h >> 2);
    const unsigned cfg = get_cfg(p, rs, lds);
    WAITVM0();
    bool dead = false;
    if (w < 4) {
        const unsigned mE = (col < 8) ? 0xFFFFFFFFu : 0u;
        const int nq = (w < 3) ? 2 : 1;
        const unsigned vo_m0 = (unsigned)(2 * w * 1024 + lane * 16), vo_m1 = (unsigned)((2 * w + nq - 1) * 1024 + lane * 16);
        const unsigned vo_e = (unsigned)(((col >> 2) & 1) * 4096 + h * 256 + q4 * 64 + (col & 3) * 16), vo_s = (unsigned)(STATS_OFF + ((lane >> 2) & 1) * 512 + (h * 4 + (lane & 3)) * 4);
        u32x4 R0 = {0,0,0,0}, R1 = {0,0,0,0};
        unsigned tmx = start_sync(p, ds, 112 + h, lds);
        unsigned thx = tmx + O1T;
        __builtin_amdgcn_s_setprio(3);
        for (int s = 0; s < NSEG; ++s) {
            const unsigned oM = chunk_off(p, cfg, s, 0), oE = oM + 3u * 16384u;
            if (s < NSEG - 1) {
                u32x4 g[2][2][2];
#pragma unroll
                for (int qi = 0; qi < 2; ++qi)
#pragma unroll
                    for (int e = 0; e < 2; ++e)
#pragma unroll
                        for (int k = 0; k < 2; ++k) g[qi][e][k] = *(const u32x4*)(lds + L_GX + (s & 1) * 28672 + (k * 14 + 2 * ((w < 3) ? 2 * w + qi : 2 * w) + e) * 1024 + lane * 16);
                if (s > 0) {
                    wait_until(thx);
                    unsigned t1 = 0; int spins = 0; const int lim = dead ? 1 : SPIN_MAX; bool bad;
                    const unsigned oT = chunk_off(p, cfg, s - 1, 0); const unsigned vts = (unsigned)(TS_OFF + (lane & 1) * 4);
                    _Pragma("nounroll") do {
                        ALD1R(t1, vts, ds, oT);
                        asm volatile("s_waitcnt vmcnt(0)" : "+v"(t1) :: "memory");
                        bad = __any(t1 == POISON);
                        if (bad) __builtin_amdgcn_s_sleep(4);
                    } while (bad && ++spins < lim);
                    if (bad) dead = true;
                    thx = (unsigned)__builtin_amdgcn_readlane(t1, 1);
                    tmx = (unsigned)__builtin_amdgcn_readlane(t1, 0);
                }
                wait_until(tmx + XOFFT);
                { int spins = 0; const int lim = dead ? 1 : SPIN_MAX; bool bad;
                  _Pragma("nounroll") do {
                    ALD4R(R0, vo_m0, ds, oM);
                    ALD4R(R1, vo_m1, ds, oM);
                    asm volatile("s_waitcnt vmcnt(0)" : "+v"(R0), "+v"(R1) :: "memory");
                    bad = __any(max(max4(R0), max4(R1)) == POISON);
                  } while (bad && ++spins < lim);
                  if (bad) dead = true; }
                f32x4 d[2] = {{0,0,0,0},{0,0,0,0}};
#pragma unroll
                for (int kt = 0; kt < 2; ++kt) { d[kt] = mfma32(g[0][0][kt], R0 & mE, d[kt]); d[kt] = mfma32(g[0][1][kt], R0 & ~mE, d[kt]); }
                if (nq == 2) {
#pragma unroll
                    for (int kt = 0; kt < 2; ++kt) { d[kt] = mfma32(g[1][0][kt], R1 & mE, d[kt]); d[kt] = mfma32(g[1][1][kt], R1 & ~mE, d[kt]); }
                }
                f32x4 o;
#pragma unroll
                for (int rr = 0; rr < 4; ++rr) {
                    const float a = d[0][rr] + dppf<DPP_ROR8>(d[0][rr]), b2 = d[1][rr] + dppf<DPP_ROR8>(d[1][rr]);
                    o[rr] = (col < 8) ? a : b2;
                }
                *(f32x4*)(lds + L_REDX + w * 1024 + lane * 16) = o;
            }
            bar();
            if (w == 0 && s < NSEG - 1) {
                f32x4 t4 = *(const f32x4*)(lds + L_REDX + lane * 16);
#pragma unroll
                for (int pp = 1; pp < 4; ++pp) t4 += *(const f32x4*)(lds + L_REDX + pp * 1024 + lane * 16);
                float m = fmx(fmx(t4.x, t4.y), fmx(t4.z, t4.w));
                m = fmx(m, dppf<DPP_ROR8>(m));
                { float a, b2; swap16(m, a, b2); m = fmx(a, b2); swap32(m, a, b2); m = fmx(a, b2); }
                if (lane < 8) AST1(nopoison(__float_as_uint(m)), vo_s, ds, oM);
                const float nM = -m * SC2;
                float e[4];
#pragma unroll
                for (int rr = 0; rr < 4; ++rr) e[rr] = __builtin_amdgcn_exp2f(__builtin_fmaf(t4[rr], SC2, nM));
                const unsigned o0 = nopoison(pk2(e[0], e[1])), o1 = nopoison(pk2(e[2], e[3]));
                const u32x4 o = {o0, o1, dppu<DPP_ROR8>(o0), dppu<DPP_ROR8>(o1)};
                if (col < 8) AST4(o, vo_e, ds, oE);
            }
        }
        bar();
    } else {
        const int hw = w - 4;
        XBuf A, B;
#pragma unroll
        for (int qi = 0; qi < 2; ++qi)
#pragma unroll
            for (int e = 0; e < 2; ++e)
#pragma unroll
                for (int k = 0; k < 2; ++k) { A.g[qi][e][k] = (u32x4){0,0,0,0}; A.x[qi][e][k] = (u32x4){0,0,0,0}; B.g[qi][e][k] = (u32x4){0,0,0,0}; B.x[qi][e][k] = (u32x4){0,0,0,0}; }
        u32x4 xm[7];
#pragma unroll
        for (int c = 0; c < 7; ++c) xm[c] = (u32x4){0,0,0,0};
        const unsigned vo_fg = (unsigned)(OFF_FLAG + (FLAG_GX + h) * 4), vo_fx = (unsigned)(OFF_FLAG + (FLAG_XM + h * 16 + ((lane & 7) < 7 ? (lane & 7) : 0)) * 4), vo_fp = (unsigned)(OFF_FLAG + (FLAG_P + h) * 4);
        x_issue<2>(ds, A, 0, h, hw, lane);
        x_issue<2>(ds, B, 1, h, hw, lane);
        await_flag(ds, vo_fg, 2, dead);
        await_flag(ds, vo_fg + 64u, 2, dead);
        x_issue<1>(ds, A, 0, h, hw, lane);
        x_issue<1>(ds, B, 1, h, hw, lane);
        AWAIT(8);
        x_pin(A);
        x_consume(A, 0, hw, lane, lds);
        unsigned fg = 0, fx = 0;
        ALD1R(fg, vo_fg, ds, 128u);
        (void)start_sync(p, ds, 112 + h, lds);
        auto interval = [&](XBuf& useB, XBuf& loadB, int s) {
            __builtin_amdgcn_s_sleep(XHD);
            asm volatile("s_waitcnt vmcnt(0)" : "+v"(fg), "+v"(fx) :: "memory");
            x_pin(useB);
            if (hw == 2 && s >= 6 && lane == 0) AST1(1u, vo_fp, ds, (unsigned)(s - 6) * 64u);
            const bool fin = (hw < 2 && s >= 4);
            const int sl = (s + 2 < NSEG) ? s + 2 : NSEG - 1;
            if (fin && __any(fx == 0u)) await_all(ds, vo_fx + (unsigned)(s - 4) * 1024u, dead);
            if ((unsigned)__builtin_amdgcn_readfirstlane(fg) < 2u) await_flag(ds, vo_fg + (unsigned)sl * 64u, 2, dead);
            SCHEDB();
            x_xm_issue(ds, xm, __builtin_amdgcn_readfirstlane(fin ? s - 4 : 0), h, hw & 1, lane, fin);
            x_issue(ds, loadB, sl, h, hw, lane);
            SCHEDB();
            if (s + 1 < NSEG) x_consume(useB, s + 1, hw, lane, lds);
            SCHEDB();
            asm volatile("s_waitcnt vmcnt(16)" : "+v"(xm[0]), "+v"(xm[1]), "+v"(xm[2]), "+v"(xm[3]), "+v"(xm[4]), "+v"(xm[5]), "+v"(xm[6]) :: "memory");
            SCHEDB();
            if (fin) x_finalize(ds, xm, s - 4, h, hw & 1, lane, lds);
            const int sf = (s + 3 < NSEG) ? s + 3 : NSEG - 1;
            ALD1R(fg, vo_fg, ds, (unsigned)sf * 64u);
            ALD1R(fx, vo_fx, ds, (unsigned)(s >= 3 ? s - 3 : 0) * 1024u);
            bar();
        };
        for (int s = 0; s < NSEG; s += 2) {
            interval(B, A, s);
            interval(A, B, s + 1);
        }
        for (int round = 0; round < 2; ++round) {
            const int sg = NSEG - 4 + 2 * round + (hw >> 1);
            {
                AWAIT(0);
                await_all(ds, vo_fx + (unsigned)sg * 1024u, dead);
                x_xm_issue(ds, xm, sg, h, hw & 1, lane);
                asm volatile("s_waitcnt vmcnt(0)" : "+v"(xm[0]), "+v"(xm[1]), "+v"(xm[2]), "+v"(xm[3]), "+v"(xm[4]), "+v"(xm[5]), "+v"(xm[6]) :: "memory");
                x_finalize(ds, xm, sg, h, hw & 1, lane, lds);
            }
        }
        AWAIT(0);
        bar();
        if (lane == 0) AST1(1u, vo_fp, ds, (unsigned)(NSEG - 4 + hw) * 64u);
        if (lane == 0 && hw < 2) AST1(1u, vo_fp, ds, (unsigned)(NSEG - 6 + hw) * 64u);
    }
}

DEVI void out_unit(const Params& p, int s, int bg, bool& dead) {
    const int tid = threadIdx.x, lane = tid & 63, w = __builtin_amdgcn_readfirstlane(tid >> 6);
    const int col = lane & 15, q4 = lane >> 4, it = w & 1;
    const rsrc_t rs = mkrsrc(p.ws, 0x10000000u);
    const int b0 = 8 * bg + 2 * (w >> 1);
    waitflag(p, FLAG_U + s * 16 + b0 / 14, 8, dead);
    waitflag(p, FLAG_U + s * 16 + (b0 + 1) / 14, 8, dead);
    u32x4 uf[2][8];
#pragma unroll
    for (int bi = 0; bi < 2; ++bi)
#pragma unroll
        for (int hp = 0; hp < 8; ++hp) uf[bi][hp] = bld4(rs, (unsigned)(OFF_UF + (((size_t)s * 56 + b0 + bi) * 8 + hp) * FR + lane * 16), 0);
    waitall(p, FLAG_W + (s * 8 + bg) * 16, 8, dead);
    const bool diag = (col >> 3) == (q4 >> 1);
    u32x2 wv[2][8];
#pragma unroll
    for (int bi = 0; bi < 2; ++bi)
#pragma unroll
        for (int hp = 0; hp < 8; ++hp) wv[bi][hp] = bld2(rs, diag ? (unsigned)(OFF_WC + (((size_t)s * 56 + b0 + bi) * 8 + hp) * 256 + col * 16 + (q4 & 1) * 8) : OOB, 0);
    waitall(p, FLAG_P + s * 16, 16, dead);
    f32x4 aa[2], ab[2], am[2];
#pragma unroll
    for (int bi = 0; bi < 2; ++bi) { aa[bi] = (f32x4){0,0,0,0}; ab[bi] = (f32x4){0,0,0,0}; am[bi] = (f32x4){0,0,0,0}; }
#pragma unroll
    for (int hp = 0; hp < 8; ++hp) {
        const u32x4 pa = bld4(rs, (unsigned)(OFF_PX + (((size_t)s * 16 + 2 * hp) * 2 + it) * FR + lane * 16), 0);
        const u32x4 pb = bld4(rs, (unsigned)(OFF_PX + (((size_t)s * 16 + 2 * hp + 1) * 2 + it) * FR + lane * 16), 0);
        const unsigned pmo = (unsigned)(OFF_PM + (((size_t)s * 8 + hp) * 2 + it) * 512 + lane * 8);
        const u32x2 pm = {bld1(rs, pmo, 0), bld1(rs, pmo + 4, 0)};
#pragma unroll
        for (int bi = 0; bi < 2; ++bi) {
            aa[bi] = mfma32(uf[bi][hp], pa, aa[bi]);
            ab[bi] = mfma32(uf[bi][hp], pb, ab[bi]);
            am[bi] = mfma16(wv[bi][hp], pm, am[bi]);
        }
    }
#pragma unroll
    for (int bi = 0; bi < 2; ++bi) {
        float v[4];
#pragma unroll
        for (int rr = 0; rr < 4; ++rr) { const float x = ((q4 >> 1) == 0 ? aa[bi][rr] : ab[bi][rr]) + am[bi][rr]; float a, b2; swap32(x, a, b2); v[rr] = a + b2; }
        if (lane < 32) { const u32x2 o = {pk2(v[0], v[1]), pk2(v[2], v[3])};
            *(u32x2*)(p.out + (size_t)(32 * s + 16 * it + col) * OUTW + 8 * (b0 + bi) + 4 * q4) = o; }
    }
}

DEVI float phi(float t) { return (t > 0.f ? t + 1.f : __builtin_amdgcn_exp2f(t * 1.4426950408889634f)) + 1e-6f; }
DEVI float rbf(float x) { return bf_lo(pk2(x, 0.f)); }

DEVI void b1_unit(const Params& p, int rb, int hp, char* lds) {
    const int tid = threadIdx.x, lane = tid & 63, w = __builtin_amdgcn_readfirstlane(tid >> 6);
    const int col = lane & 15, q4 = lane >> 4;
    const rsrc_t rs = mkrsrc(p.ws, 0x10000000u);
    const int h = 2 * hp + (w & 1), rt0 = rb * 8 + (w >> 1) * 2;
    f32x4 aq[2][2], ak[2][2], av[2][2];
#pragma unroll
    for (int a = 0; a < 2; ++a)
#pragma unroll
        for (int c = 0; c < 2; ++c) { aq[a][c] = (f32x4){0,0,0,0}; ak[a][c] = (f32x4){0,0,0,0}; av[a][c] = (f32x4){0,0,0,0}; }
#pragma unroll 2
    for (int t = 0; t < 14; ++t) {
        u32x4 xf[2], wq[2], wk[2], wv[2];
#pragma unroll
        for (int a = 0; a < 2; ++a) xf[a] = *(const u32x4*)(p.ws + OFF_BX + ((size_t)(rt0 + a) * 14 + t) * FR + lane * 16);
#pragma unroll
        for (int c = 0; c < 2; ++c) {
            wq[c] = *(const u32x4*)(p.ws + OFF_BW + ((size_t)((0 * 16 + h) * 2 + c) * 14 + t) * FR + lane * 16);
            wk[c] = *(const u32x4*)(p.ws + OFF_BW + ((size_t)((1 * 16 + h) * 2 + c) * 14 + t) * FR + lane * 16);
            wv[c] = *(const u32x4*)(p.ws + OFF_BW + ((size_t)((2 * 16 + h) * 2 + c) * 14 + t) * FR + lane * 16);
        }
#pragma unroll
        for (int a = 0; a < 2; ++a)
#pragma unroll
            for (int c = 0; c < 2; ++c) {
                aq[a][c] = mfma32(wq[c], xf[a], aq[a][c]);
                ak[a][c] = mfma32(xf[a], wk[c], ak[a][c]);
                av[a][c] = mfma32(xf[a], wv[c], av[a][c]);
            }
    }
#pragma unroll
    for (int a = 0; a < 2; ++a) {
        f32x4 p0, p1;
#pragma unroll
        for (int rr = 0; rr < 4; ++rr) {
            p0[rr] = phi(rbf(aq[a][0][rr]));
            p1[rr] = (8 * q4 + 4 + rr < 28) ? phi(rbf(aq[a][1][rr])) : 0.f;
        }
        bst4(rs, pack8(p0, p1), (unsigned)(OFF_BQ + ((size_t)(rt0 + a) * 16 + h) * FR + lane * 16), 0);
    }
    u32x4 pk[2], vv[2];
#pragma unroll
    for (int c = 0; c < 2; ++c) {
        f32x4 k0, k1, v0, v1;
        const int d = 16 * c + col;
#pragma unroll
        for (int rr = 0; rr < 4; ++rr) {
            k0[rr] = (d < 28) ? phi(rbf(ak[0][c][rr])) : 0.f; k1[rr] = (d < 28) ? phi(rbf(ak[1][c][rr])) : 0.f;
            v0[rr] = (d == 28) ? 1.f : av[0][c][rr]; v1[rr] = (d == 28) ? 1.f : av[1][c][rr];
        }
        pk[c] = pack8(k0, k1); vv[c] = pack8(v0, v1);
    }
    float* red = (float*)lds;
#pragma unroll
    for (int dc = 0; dc < 2; ++dc)
#pragma unroll
        for (int ec = 0; ec < 2; ++ec) {
            const f32x4 d = mfma32(pk[dc], vv[ec], (f32x4){0,0,0,0});
            *(f32x4*)(red + ((w * 4 + dc * 2 + ec) * 64 + lane) * 4) = d;
        }
    bar();
    {
        const int hh = w & 1, tile = w >> 1;
        f32x4 t4 = {0,0,0,0};
#pragma unroll
        for (int g = 0; g < 4; ++g) t4 += *(const f32x4*)(red + (((g * 2 + hh) * 4 + tile) * 64 + lane) * 4);
        bst4(rs, __builtin_bit_cast(u32x4, t4), (unsigned)(OFF_BKV + (((size_t)rb * 16 + 2 * hp + hh) * 4 + tile) * 1024 + lane * 16), 0);
    }
    WAITVM0();
    bar();
    if (tid == 0) signal(p, FLAG_B1);
}

DEVI void b2_unit(const Params& p, int h, char* lds, bool& dead) {
    const int tid = threadIdx.x, lane = tid & 63, w = __builtin_amdgcn_readfirstlane(tid >> 6);
    const int col = lane & 15, q4 = lane >> 4;
    const rsrc_t rs = mkrsrc(p.ws, 0x10000000u);
    float* kvs = (float*)lds;
    waitflag(p, FLAG_B1, 128, dead);
    if (w < 4) {
        f32x4 t4 = {0,0,0,0};
#pragma unroll
        for (int rb = 0; rb < 16; ++rb) t4 += __builtin_bit_cast(f32x4, bld4(rs, (unsigned)(OFF_BKV + (((size_t)rb * 16 + h) * 4 + w) * 1024 + lane * 16), 0));
        const int dc = w >> 1, ec = w & 1;
#pragma unroll
        for (int rr = 0; rr < 4; ++rr) kvs[(16 * dc + 4 * q4 + rr) * 33 + 16 * ec + col] = t4[rr];
    }
    bar();
    if (tid < 32) bst1(rs, __float_as_uint(kvs[tid * 33 + 28]), (unsigned)(OFF_BZ + (h * 32 + tid) * 4), 0);
    u32x4 ka[2];
#pragma unroll
    for (int bb = 0; bb < 2; ++bb) {
        const int d = 8 * (col >> 2) + 4 * bb + (col & 3);
        float e[8];
#pragma unroll
        for (int j = 0; j < 8; ++j) e[j] = (8 * q4 + j < 28) ? kvs[d * 33 + 8 * q4 + j] : 0.f;
        ka[bb] = (u32x4){pk2(e[0], e[1]), pk2(e[2], e[3]), pk2(e[4], e[5]), pk2(e[6], e[7])};
    }
    for (int ct = w; ct < 28; ct += 8) {
        u16 e[8];
#pragma unroll
        for (int j = 0; j < 8; ++j) e[j] = (8 * q4 + j < 28) ? p.bwo[(size_t)(28 * h + 8 * q4 + j) * DM + 16 * ct + col] : (u16)0;
        const u32x4 wf = {e[0] | ((unsigned)e[1] << 16), e[2] | ((unsigned)e[3] << 16), e[4] | ((unsigned)e[5] << 16), e[6] | ((unsigned)e[7] << 16)};
        const f32x4 d0 = mfma32(ka[0], wf, (f32x4){0,0,0,0}), d1 = mfma32(ka[1], wf, (f32x4){0,0,0,0});
        bst4(rs, pack8(d0, d1), (unsigned)(OFF_BM + ((size_t)h * 28 + ct) * FR + lane * 16), 0);
    }
    WAITVM0();
    bar();
    if (tid == 0) signal(p, FLAG_B2);
}

DEVI void b3_unit(const Params& p, int u, bool& dead) {
    const int tid = threadIdx.x, lane = tid & 63, w = __builtin_amdgcn_readfirstlane(tid >> 6);
    const int col = lane & 15, q4 = lane >> 4;
    const rsrc_t rs = mkrsrc(p.ws, 0x10000000u);
    const int rt = 2 * u + (w & 1), ct0 = 7 * (w >> 1);
    waitflag(p, FLAG_B2, 16, dead);
    u32x4 pn[16];
#pragma unroll
    for (int h = 0; h < 16; ++h) {
        const u32x4 pq = bld4(rs, (unsigned)(OFF_BQ + ((size_t)rt * 16 + h) * FR + lane * 16), 0);
        const f32x4 z0 = __builtin_bit_cast(f32x4, bld4(rs, (unsigned)(OFF_BZ + (h * 32 + 8 * q4) * 4), 0)), z1 = __builtin_bit_cast(f32x4, bld4(rs, (unsigned)(OFF_BZ + (h * 32 + 8 * q4 + 4) * 4), 0));
        float f[8] = {bf_lo(pq.x), bf_hi(pq.x), bf_lo(pq.y), bf_hi(pq.y), bf_lo(pq.z), bf_hi(pq.z), bf_lo(pq.w), bf_hi(pq.w)};
        float den = f[0] * z0.x + f[1] * z0.y + f[2] * z0.z + f[3] * z0.w + f[4] * z1.x + f[5] * z1.y + f[6] * z1.z + f[7] * z1.w;
        { float a, b2; swap16(den, a, b2); den = a + b2; swap32(den, a, b2); den = a + b2; }
        const float inv = 1.0f / (den + 1e-6f);
        pn[h] = (u32x4){pk2(f[0] * inv, f[1] * inv), pk2(f[2] * inv, f[3] * inv), pk2(f[4] * inv, f[5] * inv), pk2(f[6] * inv, f[7] * inv)};
    }
    for (int i = 0; i < 7; ++i) {
        const int ct = ct0 + i;
        f32x4 acc = {0,0,0,0};
#pragma unroll
        for (int h = 0; h < 16; ++h) acc = mfma32(bld4(rs, (unsigned)(OFF_BM + ((size_t)h * 28 + ct) * FR + lane * 16), 0), pn[h], acc);
        const u32x2 o = {pk2(acc[0], acc[1]), pk2(acc[2], acc[3])};
        *(u32x2*)(p.out + (size_t)(16 * rt + col) * OUTW + 448 + 16 * ct + 4 * q4) = o;
    }
}

DEVI void worker(const Params& p, int u, char* lds) {
    bool dead = false;
    if (p.mode != 2) gemm_worker(p, u, lds);
    if (p.mode == 1) return;
    if (p.mode == 2 && (p.mode2 & 1)) return;
    b1_unit(p, u >> 3, u & 7, lds);
    if (u < 16) b2_unit(p, u, lds, dead);
    for (int j = 0; j < 4; ++j) {
        const int idx = u + 128 * j;
        if (idx >= 224 && idx < 288) b3_unit(p, idx - 224, dead);
        else { const int o = (idx < 224) ? idx : idx - 64; out_unit(p, o / 7, o % 7, dead); }
    }
}

__global__ void __launch_bounds__(512, 2) k_main(Params p) {
    extern __shared__ __attribute__((aligned(16))) char lds[];
    const int b = blockIdx.x;
    const int idx = (b >> 3) * 4 + (b & 3);
    if ((b & 7) < 4) {
        if (p.mode == 1) return;
        if (idx < 112) chain_g(p, idx, lds); else chain_x(p, idx - 112, lds);
    } else {
        worker(p, idx, lds);
    }
}

extern "C" void kernel_launch(void* const* d_in, const int* in_sizes, int n_in, void* d_out, int out_size, void* d_ws, size_t ws_size, hipStream_t stream) {
    Params p;
    p.x = (const u16*)d_in[0]; p.mem0 = (const u16*)d_in[1]; p.Wq = (const u16*)d_in[2]; p.Wk = (const u16*)d_in[3];
    p.Wv = (const u16*)d_in[4]; p.Wo = (const u16*)d_in[5]; p.bx = (const u16*)d_in[7]; p.wqkv = (const u16*)d_in[8]; p.bwo = (const u16*)d_in[9];
    p.out = (u16*)d_out; p.ws = (char*)d_ws;
    const uintptr_t base = (uintptr_t)d_ws;
    const uintptr_t al = (base + OFF_COMM_RAW + ((2u << 20) - 1)) & ~(uintptr_t)((2u << 20) - 1);
    p.comm_off = (unsigned long long)(al - base);
    (void)hipFuncSetAttribute((const void*)k0_prep, hipFuncAttributeMaxDynamicSharedMemorySize, 136000);
    (void)hipFuncSetAttribute((const void*)k_main, hipFuncAttributeMaxDynamicSharedMemorySize, 160000);
    k0_prep<<<256, 512, 136000, stream>>>(p);
    p.mode = 0; p.mode2 = 0;
    k_main<<<256, 512, 160000, stream>>>(p);
}
```

```cpp
#include <hip/hip_runtime.h>
#include <hip/hip_bf16.h>
#include <stdint.h>

typedef __attribute__((ext_vector_type(8))) __bf16 bf16x8;
typedef __attribute__((ext_vector_type(4))) short s16x4;
typedef __attribute__((ext_vector_type(2))) __bf16 bf16x2;
typedef __attribute__((ext_vector_type(2))) float f32x2;
typedef __attribute__((ext_vector_type(4))) float f32x4;
typedef __attribute__((ext_vector_type(4))) unsigned u32x4;
typedef __attribute__((ext_vector_type(2))) unsigned u32x2;
typedef unsigned short u16;
#define LAS __attribute__((address_space(3)))
#define DEVI __device__ __forceinline__

constexpr int DM = 448, NH = 16, NSEG = 64, SEGL = 32, OUTW = 896;
constexpr size_t FR = 1024;
constexpr size_t OFF_FLAG = 0;
constexpr size_t SZ_FLAG = 65536;
constexpr size_t OFF_AF = OFF_FLAG + SZ_FLAG;
constexpr size_t SZ_AF = (size_t)16 * 28 * 14 * FR;
constexpr size_t OFF_BF = OFF_AF + SZ_AF;
constexpr size_t OFF_XF = OFF_BF + SZ_AF;
constexpr size_t SZ_XF = (size_t)64 * 28 * FR;
constexpr size_t OFF_GX = OFF_XF + SZ_XF;
constexpr size_t SZ_GX = (size_t)64 * 16 * 28 * FR;
constexpr size_t OFF_UF = OFF_GX + SZ_GX;
constexpr size_t SZ_UF = (size_t)64 * 56 * 8 * FR;
constexpr size_t OFF_XM = OFF_UF + SZ_UF;
constexpr size_t SZ_XM = (size_t)64 * 16 * 7 * 1024;
constexpr size_t OFF_WC = OFF_XM + SZ_XM;
constexpr size_t SZ_WC = (size_t)64 * 56 * 8 * 256;
constexpr size_t OFF_PX = OFF_WC + SZ_WC;
constexpr size_t SZ_PX = (size_t)64 * 16 * 2 * FR;
constexpr size_t OFF_PM = OFF_PX + SZ_PX;
constexpr size_t SZ_PM = (size_t)64 * 8 * 2 * 512;
constexpr size_t OFF_BQ = OFF_PM + SZ_PM;
constexpr size_t SZ_BQ = (size_t)128 * 16 * FR;
constexpr size_t OFF_BW = OFF_BQ + SZ_BQ;
constexpr size_t SZ_BW = (size_t)3 * 16 * 2 * 14 * FR;
constexpr size_t OFF_BX = OFF_BW + SZ_BW;
constexpr size_t SZ_BX = (size_t)128 * 14 * FR;
constexpr size_t OFF_BKV = OFF_BX + SZ_BX;
constexpr size_t SZ_BKV = (size_t)16 * 16 * 4 * 1024;
constexpr size_t OFF_BM = OFF_BKV + SZ_BKV;
constexpr size_t SZ_BM = (size_t)16 * 28 * FR;
constexpr size_t OFF_BZ = OFF_BM + SZ_BM;
constexpr size_t SZ_BZ = 16 * 32 * 4;
constexpr size_t OFF_COMM_RAW = OFF_BZ + SZ_BZ;
constexpr size_t COMM_BYTES = (size_t)6 << 20;
constexpr unsigned POISON = 0xFFFFFFFFu;
constexpr int FLAG_CFG = 0;
constexpr int FLAG_RDY = 256;
constexpr int FLAG_B1 = 32;
constexpr int FLAG_B2 = 64;
constexpr int FLAG_PROG = 8192;
constexpr int FLAG_DONE = 12288;
constexpr int FLAG_T0 = 384;
constexpr int FLAG_GX = 2048;
constexpr int FLAG_U = 3072;
constexpr int FLAG_P = 4096;
constexpr int STATS_OFF = 7168;
constexpr int TS_OFF = 32768 + 448;

struct Params {
    const u16 *x, *mem0, *Wq, *Wk, *Wv, *Wo, *bx, *wqkv, *bwo;
    u16* out;
    char* ws;
    unsigned long long comm_off;
    unsigned long long mode;
    unsigned long long mode2;
};

DEVI unsigned pk2(float a, float b) { f32x2 v = {a, b}; bf16x2 r = __builtin_convertvector(v, bf16x2); return __builtin_bit_cast(unsigned, r); }
DEVI f32x4 mfma32(u32x4 a, u32x4 b, f32x4 c) { return __builtin_amdgcn_mfma_f32_16x16x32_bf16(__builtin_bit_cast(bf16x8, a), __builtin_bit_cast(bf16x8, b), c, 0, 0, 0); }
DEVI f32x4 mfma16(u32x2 a, u32x2 b, f32x4 c) { return __builtin_amdgcn_mfma_f32_16x16x16bf16_1k(__builtin_bit_cast(s16x4, a), __builtin_bit_cast(s16x4, b), c, 0, 0, 0); }
DEVI u32x4 pack8(f32x4 a, f32x4 b) { u32x4 r; r.x = pk2(a.x, a.y); r.y = pk2(a.z, a.w); r.z = pk2(b.x, b.y); r.w = pk2(b.z, b.w); return r; }
DEVI float fmx(float a, float b) { return __builtin_amdgcn_fmed3f(a, b, 3.4028235e38f); }
template <int CTRL> DEVI float dppf(float x) { return __uint_as_float((unsigned)__builtin_amdgcn_update_dpp(0, (int)__float_as_uint(x), CTRL, 0xf, 0xf, true)); }
constexpr int DPP_ROR8 = 0x128, DPP_ROR4 = 0x124, DPP_QP_1032 = 0xB1, DPP_QP_2301 = 0x4E;
DEVI void swap16(float x, float& a, float& b) { auto s = __builtin_amdgcn_permlane16_swap(__float_as_uint(x), __float_as_uint(x), false, false); a = __uint_as_float(s[0]); b = __uint_as_float(s[1]); }
DEVI void swap32(float x, float& a, float& b) { auto s = __builtin_amdgcn_permlane32_swap(__float_as_uint(x), __float_as_uint(x), false, false); a = __uint_as_float(s[0]); b = __uint_as_float(s[1]); }
DEVI void uswap16(unsigned x, unsigned& a, unsigned& b) { auto s = __builtin_amdgcn_permlane16_swap(x, x, false, false); a = s[0]; b = s[1]; }
DEVI void uswap32(unsigned x, unsigned& a, unsigned& b) { auto s = __builtin_amdgcn_permlane32_swap(x, x, false, false); a = s[0]; b = s[1]; }
template <int CTRL> DEVI unsigned dppu(unsigned x) { return (unsigned)__builtin_amdgcn_update_dpp(0, (int)x, CTRL, 0xf, 0xf, true); }
constexpr int DPP_SHL4 = 0x104, DPP_SHL8 = 0x108;
DEVI float bf_lo(unsigned v) { return __uint_as_float(v << 16); }
DEVI float bf_hi(unsigned v) { return __uint_as_float(v & 0xFFFF0000u); }
DEVI int kcol(int t, int q4) { return 64 * (t >> 1) + 16 * q4 + 8 * (t & 1); }
DEVI unsigned nopoison(unsigned v) { return v < 0xFFFFFFFEu ? v : 0xFFFFFFFEu; }
DEVI unsigned max4(u32x4 v) { unsigned a = v.x > v.y ? v.x : v.y, b = v.z > v.w ? v.z : v.w; return a > b ? a : b; }

typedef __amdgpu_buffer_rsrc_t rsrc_t;
DEVI rsrc_t mkrsrc(const void* p, unsigned bytes) { return __builtin_amdgcn_make_buffer_rsrc((void*)p, 0, bytes, 0x00020000); }
DEVI u32x4 bld4(rsrc_t r, unsigned v, unsigned s) { return __builtin_amdgcn_raw_buffer_load_b128(r, v, s, 16); }
DEVI u32x2 bld2(rsrc_t r, unsigned v, unsigned s) { return __builtin_amdgcn_raw_buffer_load_b64(r, v, s, 16); }
DEVI unsigned bld1(rsrc_t r, unsigned v, unsigned s) { return __builtin_amdgcn_raw_buffer_load_b32(r, v, s, 16); }
DEVI u32x4 bld4p(rsrc_t r, unsigned v, unsigned s) { return __builtin_amdgcn_raw_buffer_load_b128(r, v, s, 0); }
DEVI void bst4(rsrc_t r, u32x4 d, unsigned v, unsigned s) { __builtin_amdgcn_raw_buffer_store_b128(d, r, v, s, 16); }
DEVI void bst2(rsrc_t r, u32x2 d, unsigned v, unsigned s) { __builtin_amdgcn_raw_buffer_store_b64(d, r, v, s, 16); }
DEVI void bst1(rsrc_t r, unsigned d, unsigned v, unsigned s) { __builtin_amdgcn_raw_buffer_store_b32(d, r, v, s, 16); }
#define MEMFENCE() asm volatile("" ::: "memory")
#define WAITVM0() do { asm volatile("s_waitcnt vmcnt(0)" ::: "memory"); __builtin_amdgcn_s_waitcnt(0x0F70); } while (0)
DEVI void bar() { asm volatile("s_waitcnt lgkmcnt(0)\n\ts_barrier" ::: "memory"); }
constexpr unsigned OOB = 0xF0000000u;
DEVI u32x4 mkdesc(const void* p, unsigned bytes) {
    const unsigned long long a = (unsigned long long)p;
    u32x4 d; d.x = __builtin_amdgcn_readfirstlane((unsigned)a); d.y = __builtin_amdgcn_readfirstlane((unsigned)(a >> 32)) & 0xFFFFu; d.z = bytes; d.w = 0x00020000u; return d;
}
#define ALD4(dst, vo, ds, so) asm volatile("buffer_load_dwordx4 %0, %1, %2, %3 offen sc1" : "=v"(dst) : "v"(vo), "s"(ds), "s"(so) : "memory")
#define ALD4R(dst, vo, ds, so) asm volatile("buffer_load_dwordx4 %0, %1, %2, %3 offen sc1" : "+v"(dst) : "v"(vo), "s"(ds), "s"(so) : "memory")
#define ALD4PR(dst, vo, ds, so) asm volatile("buffer_load_dwordx4 %0, %1, %2, %3 offen" : "+v"(dst) : "v"(vo), "s"(ds), "s"(so) : "memory")
#define ALD1(dst, vo, ds, so) asm volatile("buffer_load_dword %0, %1, %2, %3 offen sc1" : "=v"(dst) : "v"(vo), "s"(ds), "s"(so) : "memory")
#define ALD1R(dst, vo, ds, so) asm volatile("buffer_load_dword %0, %1, %2, %3 offen sc1" : "+v"(dst) : "v"(vo), "s"(ds), "s"(so) : "memory")
#define AST4(dat, vo, ds, so) asm volatile("buffer_store_dwordx4 %0, %1, %2, %3 offen sc1\n\ts_nop 1" :: "v"(dat), "v"(vo), "s"(ds), "s"(so) : "memory")
#define AST2(dat, vo, ds, so) asm volatile("buffer_store_dwordx2 %0, %1, %2, %3 offen sc1\n\ts_nop 1" :: "v"(dat), "v"(vo), "s"(ds), "s"(so) : "memory")
#define AST1(dat, vo, ds, so) asm volatile("buffer_store_dword %0, %1, %2, %3 offen sc1" :: "v"(dat), "v"(vo), "s"(ds), "s"(so) : "memory")
#define AADD1(vo, ds) asm volatile("buffer_atomic_add %0, %1, %2, 0 offen" :: "v"(1u), "v"(vo), "s"(ds) : "memory")
#define SCHEDB() __builtin_amdgcn_sched_barrier(0)
#define PIN(x) asm volatile("" : "+v"(x))
#define AWAIT(n) asm volatile("s_waitcnt vmcnt(" #n ")" ::: "memory")

__global__ void __launch_bounds__(512, 2) k0_prep(Params p) {
    extern __shared__ __attribute__((aligned(16))) char lds[];
    const int b = blockIdx.x, tid = threadIdx.x, lane = tid & 63, w = __builtin_amdgcn_readfirstlane(tid >> 6);
    const int l15 = lane & 15, q4 = lane >> 4;
    const int mat = b & 1, h = (b >> 1) & 15, sub = b >> 5, th = sub & 1, cg = sub >> 1;
    constexpr int K_SCR = 61440, K_XT = K_SCR + 7 * 8448;
    const int lr = lane >> 2, lp = lane & 3;
    u32x4 stg[7];
    if (mat == 0) {
#pragma unroll
        for (int i = 0; i < 7; ++i) {
            const int ct = 7 * cg + i;
            const int c = 64 * (ct >> 2) + 16 * (lr >> 2) + 4 * (ct & 3) + (lr & 3);
            stg[i] = *(const u32x4*)(p.Wq + ((size_t)c * 16 + h) * 256 + 32 * w + 8 * lp);
        }
    } else {
#pragma unroll
        for (int i = 0; i < 7; ++i) {
            const int n = tid + 512 * i, e = n / 14, ch = n % 14;
            stg[i] = *(const u32x4*)(p.Wo + ((size_t)h * 256 + e) * 448 + 112 * cg + 8 * ch);
        }
    }
    u32x4 rw[2][8];
    const int t = 7 * th + (w < 7 ? w : 0);
    {
        const u16* Wr = mat ? p.Wv : p.Wk;
#pragma unroll
        for (int bb = 0; bb < 2; ++bb) {
            const int d = 64 * (t >> 1) + 16 * (lr >> 2) + 8 * (t & 1) + 4 * bb + (lr & 3);
#pragma unroll
            for (int k = 0; k < 8; ++k) rw[bb][k] = *(const u32x4*)(Wr + ((size_t)d * 16 + h) * 256 + 32 * k + 8 * lp);
        }
    }
    const char* xsrc = (const char*)((b & 1) ? p.bx : p.x) + (size_t)(b >> 1) * 14336;
    const u32x4 xt0 = *(const u32x4*)(xsrc + tid * 16);
    const u32x4 xt1 = *(const u32x4*)(xsrc + 8192 + (tid < 384 ? tid : 0) * 16);
    const int fw = b * 8 + w;
    u16 ew[8];
    {
        const int f = (fw < 1344) ? fw : 0;
        const int tt = f % 14, tile = (f / 14) & 1, hh = (f / 28) & 15, t3 = f / 448;
        const int col = (t3 == 0) ? (8 * (l15 >> 2) + 4 * tile + (l15 & 3)) : (16 * tile + l15);
#pragma unroll
        for (int j = 0; j < 8; ++j) ew[j] = (col < 28) ? p.wqkv[(size_t)(kcol(tt, q4) + j) * 1344 + t3 * 448 + hh * 28 + col] : (u16)0;
    }
    if (b & 4) {
        const int q = (b >> 3) * 4 + (b & 3);
        char* cb = p.ws + p.comm_off + (size_t)q * 32768;
        const u32x4 pz = {POISON, POISON, POISON, POISON};
#pragma unroll
        for (int k = 0; k < 4; ++k) *(u32x4*)(cb + k * 8192 + tid * 16) = pz;
        if (q == 0 && tid < 448) {
            const int beta = tid >> 3, i = tid & 7;
            const u32x4 m = *(const u32x4*)(p.mem0 + i * DM + 8 * beta);
            *(u32x4*)(cb + tid * 16) = m;
            *(u32x4*)(cb + 8192 + tid * 16) = m;
        }
    }
    if (b < 32 && tid < 128) *(u32x4*)(p.ws + OFF_FLAG + (size_t)b * 2048 + tid * 16) = (u32x4){0, 0, 0, 0};
    if (mat == 0) {
#pragma unroll
        for (int i = 0; i < 7; ++i) *(u32x4*)(lds + (i * 8 + w) * 1024 + (lp * 16 + lr) * 16) = stg[i];
    } else {
#pragma unroll
        for (int i = 0; i < 7; ++i) { const int n = tid + 512 * i, e = n / 14, ch = n % 14; *(u32x4*)(lds + e * 240 + ch * 16) = stg[i]; }
    }
    { const int o0 = tid * 16, o1 = 8192 + tid * 16;
      *(u32x4*)(lds + K_XT + (o0 / 896) * 912 + (o0 % 896)) = xt0;
      if (tid < 384) *(u32x4*)(lds + K_XT + (o1 / 896) * 912 + (o1 % 896)) = xt1; }
    if (fw < 1344) {
        const u32x4 o = {ew[0] | ((unsigned)ew[1] << 16), ew[2] | ((unsigned)ew[3] << 16), ew[4] | ((unsigned)ew[5] << 16), ew[6] | ((unsigned)ew[7] << 16)};
        *(u32x4*)(p.ws + OFF_BW + (size_t)fw * FR + lane * 16) = o;
    }
    if (w < 7) {
        char* scr = lds + K_SCR + w * 8448;
#pragma unroll
        for (int bb = 0; bb < 2; ++bb) {
#pragma unroll
            for (int k = 0; k < 8; ++k) *(u32x4*)(scr + lr * 528 + 64 * k + 16 * lp) = rw[bb][k];
            asm volatile("s_waitcnt lgkmcnt(0)" ::: "memory");
#pragma unroll
            for (int k = 0; k < 8; ++k) rw[bb][k] = *(const u32x4*)(scr + l15 * 528 + 64 * k + 16 * q4);
            asm volatile("s_waitcnt lgkmcnt(0)" ::: "memory");
        }
    }
    __syncthreads();
#pragma unroll
    for (int j = 0; j < 2; ++j) {
        const int tt = w + 8 * j;
        if (tt < 14) {
            const u32x4 v = *(const u32x4*)(lds + K_XT + l15 * 912 + kcol(tt, q4) * 2);
            *(u32x4*)(p.ws + ((b & 1) ? OFF_BX : OFF_XF) + ((size_t)(b >> 1) * 14 + tt) * FR + lane * 16) = v;
        }
    }
    if (w < 7) {
        auto ldcf = [&](int i, u32x4 (&cf)[8]) {
#pragma unroll
            for (int k = 0; k < 8; ++k) {
                if (mat == 0) cf[k] = *(const u32x4*)(lds + (i * 8 + k) * 1024 + lane * 16);
                else {
                    const int qq = l15 >> 2, pp = l15 & 3;
                    const int row0 = 32 * k + 8 * q4 + qq;
                    typedef __attribute__((ext_vector_type(4))) short v4s;
                    v4s lo = __builtin_amdgcn_ds_read_tr16_b64_v4i16((LAS v4s*)(lds + row0 * 240 + (16 * i + 4 * pp) * 2));
                    v4s hi = __builtin_amdgcn_ds_read_tr16_b64_v4i16((LAS v4s*)(lds + (row0 + 4) * 240 + (16 * i + 4 * pp) * 2));
                    u32x2 l2 = __builtin_bit_cast(u32x2, lo), h2 = __builtin_bit_cast(u32x2, hi);
                    cf[k] = (u32x4){l2.x, l2.y, h2.x, h2.y};
                }
            }
        };
        u32x4 cfa[8], cfb[8];
        auto tile = [&](int i, const u32x4 (&cf)[8]) {
            f32x4 a0 = {0, 0, 0, 0}, a1 = {0, 0, 0, 0};
#pragma unroll
            for (int k = 0; k < 8; ++k) { a0 = mfma32(rw[0][k], cf[k], a0); a1 = mfma32(rw[1][k], cf[k], a1); }
            const u32x4 o = pack8(a0, a1);
            if (mat == 0) {
                const int ct = 7 * cg + i;
                *(u32x4*)(p.ws + OFF_AF + ((size_t)(h * 28 + ct) * 14 + t) * FR + lane * 16) = o;
            } else {
                const int beta = 14 * cg + 2 * i + (l15 >> 3);
                *(u32x4*)(p.ws + OFF_BF + ((size_t)((h >> 1) * 56 + beta) * 14 + t) * FR + (16 * q4 + 8 * (h & 1) + (l15 & 7)) * 16) = o;
            }
        };
        ldcf(0, cfa);
#pragma unroll
        for (int i = 0; i < 7; i += 2) {
            if (i + 1 < 7) ldcf(i + 1, cfb);
            tile(i, cfa);
            if (i + 2 < 7) ldcf(i + 2, cfa);
            if (i + 1 < 7) tile(i + 1, cfb);
        }
    }
}

constexpr int SPIN_MAX = 200000;
DEVI unsigned* flagp(const Params& p, int idx) { return (unsigned*)(p.ws + OFF_FLAG) + idx; }
DEVI void signal(const Params& p, int idx) { __hip_atomic_fetch_add(flagp(p, idx), 1u, __ATOMIC_RELAXED, __HIP_MEMORY_SCOPE_AGENT); }
DEVI unsigned ldflag(const Params& p, int idx) { return __hip_atomic_load(flagp(p, idx), __ATOMIC_RELAXED, __HIP_MEMORY_SCOPE_AGENT); }
DEVI void waitflag(const Params& p, int idx, unsigned target, bool& dead, int sleep = 4) {
    int k = dead ? SPIN_MAX : 0;
    _Pragma("nounroll") for (; k < SPIN_MAX; ++k) {
        const unsigned v = __builtin_amdgcn_readfirstlane(ldflag(p, idx));
        if (v >= target) break;
        __builtin_amdgcn_s_sleep(4);
    }
    if (k >= SPIN_MAX) dead = true;
    MEMFENCE();
}

DEVI void waitall(const Params& p, int idx, int n, bool& dead) {
    int k = dead ? SPIN_MAX : 0;
    const int _l = (threadIdx.x & 63) & (n - 1);
    _Pragma("nounroll") for (; k < SPIN_MAX; ++k) {
        const unsigned v = ldflag(p, idx + _l);
        if (!__any(v == 0u)) break;
        __builtin_amdgcn_s_sleep(8);
    }
    if (k >= SPIN_MAX) dead = true;
    MEMFENCE();
}

DEVI void gemm_worker(const Params& p, int u, char* lds) {
    const int tid = threadIdx.x, lane = tid & 63, w = __builtin_amdgcn_readfirstlane(tid >> 6);
    const rsrc_t rs = mkrsrc(p.ws, 0x10000000u);
    const int isU = u >> 6, par = (u >> 2) & 1, v = ((((u & 3) | (((u & 63) >> 3) << 2))) << 1) | par;
    u32x4 wt[2][14];
    unsigned vo_out[2];
    int flag_idx, flag_stride;
    u32x4 x0[4], x1[4];
#pragma unroll
    for (int j = 0; j < 4; ++j) { const int f = (w + 8 * j < 28) ? w + 8 * j : w;
        x0[j] = *(const u32x4*)(p.ws + OFF_XF + ((size_t)par * 28 + f) * FR + lane * 16);
        x1[j] = *(const u32x4*)(p.ws + OFF_XF + ((size_t)(par + 2) * 28 + f) * FR + lane * 16); }
    {
        const int ww = (w < 7) ? w : 0;
        const char* base;
        if (!isU) {
            const int h = v >> 2, tp = 7 * ((v >> 1) & 1) + ww; base = p.ws + OFF_AF + (size_t)(h * 28 + 2 * tp) * 14 * FR;
            vo_out[0] = (unsigned)(OFF_GX + ((size_t)(h * 2 + 0) * 14 + tp) * FR + lane * 16);
            vo_out[1] = (unsigned)(OFF_GX + ((size_t)(h * 2 + 1) * 14 + tp) * FR + lane * 16);
            flag_idx = FLAG_GX + h; flag_stride = 16;
        } else {
            const int hp = v >> 3, b0 = 14 * ((v >> 1) & 3) + 2 * ww; base = p.ws + OFF_BF + (size_t)(hp * 56 + b0) * 14 * FR;
            vo_out[0] = (unsigned)(OFF_UF + ((size_t)(b0 + 0) * 8 + hp) * FR + lane * 16);
            vo_out[1] = (unsigned)(OFF_UF + ((size_t)(b0 + 1) * 8 + hp) * FR + lane * 16);
            flag_idx = FLAG_U + ((v >> 1) & 3); flag_stride = 16;
        }
        if (w < 7) {
#pragma unroll
            for (int a = 0; a < 2; ++a)
#pragma unroll
                for (int t = 0; t < 14; ++t) wt[a][t] = *(const u32x4*)(base + (size_t)(a * 14 + t) * FR + lane * 16);
        } else {
#pragma unroll
            for (int a = 0; a < 2; ++a)
#pragma unroll
                for (int t = 0; t < 14; ++t) wt[a][t] = (u32x4){0, 0, 0, 0};
        }
    }
    WAITVM0();
    constexpr unsigned SEG_STRIDE = 16 * 28 * 1024;
    int buf = 0;
#pragma unroll
    for (int j = 0; j < 4; ++j) { const int f = w + 8 * j; if (f < 28) { *(u32x4*)(lds + f * 1024 + lane * 16) = x0[j]; *(u32x4*)(lds + 28672 + f * 1024 + lane * 16) = x1[j]; } }
    bar();
    for (int s = par; s < NSEG; s += 2) {
        u32x4 nx[4];
        const bool more = (s + 2 < NSEG) && (s >= 2);
        if (more) {
#pragma unroll
            for (int j = 0; j < 4; ++j) { const int f = (w + 8 * j < 28) ? w + 8 * j : w; nx[j] = *(const u32x4*)(p.ws + OFF_XF + ((size_t)(s + 2) * 28 + f) * FR + lane * 16); }
        }
        MEMFENCE();
        if (w < 7) {
            f32x4 acc[2][2];
#pragma unroll
            for (int a = 0; a < 2; ++a)
#pragma unroll
                for (int kt = 0; kt < 2; ++kt) acc[a][kt] = (f32x4){0, 0, 0, 0};
            const char* xb = lds + buf * 28672 + lane * 16;
            if (!isU) {
#pragma unroll
                for (int t = 0; t < 14; ++t)
#pragma unroll
                    for (int kt = 0; kt < 2; ++kt) {
                        const u32x4 xf = *(const u32x4*)(xb + (kt * 14 + t) * 1024);
                        acc[0][kt] = mfma32(wt[0][t], xf, acc[0][kt]); acc[1][kt] = mfma32(wt[1][t], xf, acc[1][kt]);
                    }
            } else {
#pragma unroll
                for (int t = 0; t < 14; ++t)
#pragma unroll
                    for (int kt = 0; kt < 2; ++kt) {
                        const u32x4 xf = *(const u32x4*)(xb + (kt * 14 + t) * 1024);
                        acc[0][kt] = mfma32(xf, wt[0][t], acc[0][kt]); acc[1][kt] = mfma32(xf, wt[1][t], acc[1][kt]);
                    }
            }
            WAITVM0();
            u32x4 o0, o1;
            if (!isU) { o0 = pack8(acc[0][0], acc[1][0]); o1 = pack8(acc[0][1], acc[1][1]); }
            else { o0 = pack8(acc[0][0], acc[0][1]); o1 = pack8(acc[1][0], acc[1][1]); }
            bst4(rs, o0, vo_out[0], (unsigned)s * SEG_STRIDE);
            bst4(rs, o1, vo_out[1], (unsigned)s * SEG_STRIDE);
        }
        if (more) {
#pragma unroll
            for (int j = 0; j < 4; ++j) { const int f = w + 8 * j; if (f < 28) *(u32x4*)(lds + (buf ^ 1) * 28672 + f * 1024 + lane * 16) = nx[j]; }
        }
        if (s < 2) WAITVM0();
        bar();
        if (s < 2) { if (tid == 448) signal(p, flag_idx + s * flag_stride);
        }
        else if (s >= 4 && tid == 448) signal(p, flag_idx + (s - 2) * flag_stride);
        buf ^= 1;
    }
    WAITVM0();
    bar();
    if (tid == 448) signal(p, flag_idx + (NSEG - 2 + par) * flag_stride);
}

constexpr float SC2 = 0.0625f * 1.4426950408889634f;
constexpr int L_R = 0;
constexpr int L_RED1 = 7168;
constexpr int L_RED2 = L_RED1 + 4352;
constexpr int L_REDX = L_RED2 + 2432;
constexpr int L_XX = L_REDX + 8192;
constexpr int L_CFG = L_XX + 73728;

DEVI unsigned chunk_off(const Params& p, unsigned cfg, int s, int j) {
    const int pg = s >> 5;
    return (unsigned)p.comm_off + (unsigned)pg * (2u << 20) + (unsigned)(2 * (4 * (s & 31) + j) + ((cfg >> pg) & 1)) * 8192u;
}

DEVI unsigned ts_off(const Params& p, unsigned cfg, int s) { return (unsigned)p.comm_off + (4u << 20) + (unsigned)(2 * (s >> 6) + ((cfg >> 2) & 1)) * 8192u + (unsigned)(s & 63) * 128u; }
DEVI unsigned rt32() { return (unsigned)__builtin_amdgcn_s_memrealtime(); }
DEVI void wait_until(unsigned target) {
    const unsigned now = rt32();
    const int d = (int)(target - now);
    if (d <= 0) return;
    if (d > 2000) target = now + 2000u;
    _Pragma("nounroll") while ((int)(rt32() - target) < 0) { }
}
#define C2T 110
#define O1T 96
#define C2BOOT 260
#define O1BOOT 200
#define C2H 86
#define GDELAY 100
#define WPOS 0
#define SDLY 3
#define TSOFF 10
#define XHD 6
#define BULKD 4
#define CUP 40
#define XOFFT 0
#define DEXTRA 0
#define STARTD 60
DEVI void probe_cfg(const Params& p, rsrc_t rs, int idx) {
    const int pg = idx >> 1, par = idx & 1;
    unsigned best = 1u << 20;
    for (int k = 0; k < 4; ++k) {
        const unsigned off = (unsigned)p.comm_off + pg * (2u << 20) + (unsigned)((pg ? 2 * (4 * 31 + 1) : 0) + par) * 8192u + 512u * k;
        const unsigned long long c0 = __builtin_amdgcn_s_memtime();
        unsigned vv = bld1(rs, off, 0);
        asm volatile("s_waitcnt vmcnt(0)" : "+v"(vv) :: "memory");
        const unsigned long long c1 = __builtin_amdgcn_s_memtime();
        const unsigned dt = (unsigned)(c1 - c0);
        if (k > 0 && dt < best) best = dt;
    }
    if ((threadIdx.x & 63) == 0) bst1(rs, best + 1u, OFF_FLAG + (FLAG_CFG + idx) * 4, 0);
}
DEVI unsigned get_cfg(const Params& p, rsrc_t rs, char* lds) {
    const int tid = threadIdx.x, lane = tid & 63, w = __builtin_amdgcn_readfirstlane(tid >> 6);
    if (w == 7) {
        unsigned v = 0;
        for (int it = 0; it < SPIN_MAX; ++it) { v = bld1(rs, OFF_FLAG + (FLAG_CFG + (lane & 3)) * 4, 0); MEMFENCE(); if (!__any(v == 0u)) break; __builtin_amdgcn_s_sleep(2); }
        const unsigned l0 = __builtin_amdgcn_readlane(v, 0), l1 = __builtin_amdgcn_readlane(v, 1), l2 = __builtin_amdgcn_readlane(v, 2), l3 = __builtin_amdgcn_readlane(v, 3);
        unsigned cfg = (l1 < l0 ? 1u : 0u) | (l3 < l2 ? 2u : 0u);
        if (lane == 0) *(unsigned*)(lds + L_CFG) = cfg;
    }
    __syncthreads();
    return __builtin_amdgcn_readfirstlane(*(unsigned*)(lds + L_CFG));
}
DEVI unsigned start_sync(const Params& p, u32x4 ds, int r, char* lds) {
    const int tid = threadIdx.x, lane = tid & 63, w = __builtin_amdgcn_readfirstlane(tid >> 6);
    bar();
    if (w == 3) {
        const unsigned base = rt32();
        if (lane == 0) AST1(base | 1u, (unsigned)(OFF_FLAG + (FLAG_RDY + r) * 4), ds, 0u);
        unsigned v0 = 0, v1 = 0;
        const unsigned vo = (unsigned)(OFF_FLAG + (FLAG_RDY + lane) * 4);
        for (int it = 0; it < SPIN_MAX; ++it) {
            ALD1R(v0, vo, ds, 0u); ALD1R(v1, vo, ds, 256u);
            asm volatile("s_waitcnt vmcnt(0)" : "+v"(v0), "+v"(v1) :: "memory");
            if (!__any(v0 == 0u || v1 == 0u)) break;
            __builtin_amdgcn_s_sleep(2);
        }
        int d = max((int)(v0 - base), (int)(v1 - base));
        d = max(d, (int)dppu<DPP_QP_1032>((unsigned)d));
        d = max(d, (int)dppu<DPP_QP_2301>((unsigned)d));
        d = max(d, (int)dppu<DPP_ROR4>((unsigned)d));
        d = max(d, (int)dppu<DPP_ROR8>((unsigned)d));
        { unsigned a, b2; uswap16((unsigned)d, a, b2); d = max((int)a, (int)b2); uswap32((unsigned)d, a, b2); d = max((int)a, (int)b2); }
        if (lane == 0) *(unsigned*)(lds + L_CFG + 4) = base + (unsigned)d + STARTD;
        if (r == 0 && lane == 0) AST1((base + (unsigned)d + STARTD) | 1u, (unsigned)(OFF_FLAG + FLAG_T0 * 4), ds, 0u);
    }
    bar();
    return __builtin_amdgcn_readfirstlane(*(unsigned*)(lds + L_CFG + 4));
}

#define D1 0
#define D2 0
template <int DELAY> DEVI void pause() { if (DELAY > 0) __builtin_amdgcn_s_sleep(DELAY); }

#define EV(k)
#define STAMP(k)

constexpr int L_TS = L_CFG + 64;
constexpr int L_RF = L_CFG + 128;
constexpr int L_PU = L_CFG + 256;
constexpr int L_PX = L_PU + 8192;
constexpr int L_GX = L_CFG + 256;
#define WSPLIT 5
#define MINH 30
#define MINM 30
DEVI unsigned tmax(unsigned a, unsigned b) { return ((int)(a - b) > 0) ? a : b; }

DEVI void await_flag(u32x4 ds, unsigned vo, unsigned target, bool& dead) {
    int spins = 0; const int lim = dead ? 1 : SPIN_MAX; bool bad;
    _Pragma("nounroll") do {
        unsigned v; ALD1(v, vo, ds, 0u);
        asm volatile("s_waitcnt vmcnt(0)" : "+v"(v) :: "memory");
        bad = (unsigned)__builtin_amdgcn_readfirstlane(v) < target;
        if (bad) __builtin_amdgcn_s_sleep(2);
    } while (bad && ++spins < lim);
    if (bad) dead = true;
}
DEVI void await_all(u32x4 ds, unsigned vo, bool& dead) {
    int spins = 0; const int lim = dead ? 1 : SPIN_MAX; bool bad;
    _Pragma("nounroll") do {
        unsigned v; ALD1(v, vo, ds, 0u);
        asm volatile("s_waitcnt vmcnt(0)" : "+v"(v) :: "memory");
        bad = __any(v == 0u);
        if (bad) __builtin_amdgcn_s_sleep(2);
    } while (bad && ++spins < lim);
    if (bad) dead = true;
}
#define LAT0()
#define LAT1(k)
#define WT(k)
#define TA0(v)
#define TA1(v, k, cond)

#define PTA()
#define PTB(k)
template <bool V> struct BoolC { static constexpr bool value = V; };

DEVI void chain_gw(const Params& p, int r, char* lds, u32x4 ds, unsigned t_entry) {
    const rsrc_t rs = mkrsrc(p.ws, 0x10000000u);
    const int tid = threadIdx.x, lane = tid & 63, w = __builtin_amdgcn_readfirstlane(tid >> 6);
    const int col = lane & 15, q4 = lane >> 4;
    const int h = r / 7, c7 = r % 7, beta = r >> 1, rh = r & 1;
    u32x4 aw[2][2][4];
    const int nq = (w < 3) ? 2 : 1;
    const int qf0 = (c7 + 2 * w) % 7, qf1 = (c7 + 2 * w + nq - 1) % 7;
#pragma unroll
    for (int qi = 0; qi < 2; ++qi)
#pragma unroll
        for (int e = 0; e < 2; ++e)
#pragma unroll
            for (int tau = 0; tau < 4; ++tau) {
                const int q = qi ? qf1 : qf0;
                aw[qi][e][tau] = *(const u32x4*)(p.ws + OFF_AF + ((size_t)(h * 28 + 4 * c7 + tau) * 14 + 2 * q + e) * FR + lane * 16);
                if (qi == 1 && nq == 1) aw[qi][e][tau] = (u32x4){0, 0, 0, 0};
            }
    unsigned mE = (col < 8) ? 0xFFFFFFFFu : 0u, nE = ~mE;
    asm volatile("" : "+v"(mE), "+v"(nE));
    const unsigned cfg = get_cfg(p, rs, lds);
    WAITVM0();
    bool dead = false;
    const unsigned vo_m0 = (unsigned)(qf0 * 1024 + lane * 16), vo_m1 = (unsigned)(qf1 * 1024 + lane * 16);
    const unsigned vo_pp = (lane >= 32) ? (unsigned)(TS_OFF + (lane & 1) * 4) : (unsigned)((((lane >> 4) & 1) ? 16384 : 32768) + h * 512 + c7 * 64 + (lane & 15) * 4);
    const int npub = (r == 0) ? 34 : 32;
    const unsigned vo_mp = (unsigned)(beta * 128 + rh * 64 + (lane & 15) * 4);
    const unsigned vo_xm = (unsigned)(OFF_XM + ((size_t)h * 7 + c7) * 1024 + lane * 16);
    const unsigned vo_ts = (unsigned)(TS_OFF + (lane & 1) * 4);
    u32x4 R0 = {0,0,0,0}, R1 = {0,0,0,0};
    unsigned ts1 = 0;
    unsigned tm = 0, th = 0;
    volatile LAS unsigned* ldist = (volatile LAS unsigned*)((LAS char*)lds + L_RF + 4);
    bool dh_prev = false;
    LAS char* lbase = (LAS char*)lds;
    LAS char* lr0 = lbase + L_R + vo_m0; LAS char* lr1 = lbase + L_R + vo_m1; LAS char* lrc = lbase + L_R + c7 * 1024 + lane * 16;
    volatile LAS unsigned* rflag = (volatile LAS unsigned*)(lbase + L_RF + (lane & 0));
    volatile LAS unsigned* rflagw = (volatile LAS unsigned*)(lbase + (w == 0 ? L_RF : L_RF + 16 + 4 * (w & 3)) + (lane & 0));
    asm volatile("" : "+v"(lr0), "+v"(lr1), "+v"(lrc), "+v"(rflag), "+v"(rflagw));
    if (tid == 0) { *rflag = 0u; ldist[0] = 0u; ldist[1] = 0u; }
    tm = start_sync(p, ds, r, lds);
    __builtin_amdgcn_s_setprio(2);
    th = tm + O1T;
    auto step = [&](const int s, auto LASTC) {
        constexpr bool LAST = decltype(LASTC)::value;
        const unsigned oM = (unsigned)__builtin_amdgcn_readfirstlane((int)chunk_off(p, cfg, s, 0)), oMn = (unsigned)__builtin_amdgcn_readfirstlane((int)chunk_off(p, cfg, s + 1, 0));
        u32x4 gE, gO;
        unsigned sv1 = (unsigned)(s + 1); PIN(sv1);
        wait_until(tm);
        if (w == 0) EV(4);
        f32x4 acc[4];
        u32x4 Re1;
        unsigned mx;
        auto ghalf1 = [&]() {
            ALD4R(R0, vo_m0, ds, oM);
            ALD4R(R1, vo_m1, ds, oM);
            asm volatile("s_waitcnt vmcnt(1)" : "+v"(R0) :: "memory");
            PTB(10);
            SCHEDB();
            u32x4 Re = R0 & mE;
            PIN(Re);
            SCHEDB();
            acc[0] = mfma32(aw[0][0][0], Re, (f32x4){0, 0, 0, 0});
            *(volatile LAS u32x4*)(lr0) = R0;
            SCHEDB();
            u32x4 Ro;
            acc[1] = mfma32(aw[0][0][1], Re, (f32x4){0, 0, 0, 0});
            Ro.x = R0.x & nE; Ro.y = R0.y & nE;
            SCHEDB();
            acc[2] = mfma32(aw[0][0][2], Re, (f32x4){0, 0, 0, 0});
            Ro.z = R0.z & nE; Ro.w = R0.w & nE;
            PIN(Ro);
            SCHEDB();
            acc[3] = mfma32(aw[0][0][3], Re, (f32x4){0, 0, 0, 0});
            mx = max(max(R0.x, R0.y), R0.z); mx = max(mx, R0.w);
            PIN(mx);
            SCHEDB();
            acc[0] = mfma32(aw[0][1][0], Ro, acc[0]);
            SCHEDB();
            asm volatile("s_waitcnt vmcnt(0)" : "+v"(R1) :: "memory");
            PTB(11);
            SCHEDB();
            *(volatile LAS u32x4*)(lr1) = R1;
            SCHEDB();
            acc[1] = mfma32(aw[0][1][1], Ro, acc[1]);
            mx = max(max(mx, R1.x), R1.y); mx = max(max(mx, R1.z), R1.w);
            PIN(mx);
            SCHEDB();
            acc[2] = mfma32(aw[0][1][2], Ro, acc[2]);
            Re1.x = R1.x & mE; Re1.y = R1.y & mE;
            SCHEDB();
            acc[3] = mfma32(aw[0][1][3], Ro, acc[3]);
            Re1.z = R1.z & mE; Re1.w = R1.w & mE;
            PIN(Re1);
            SCHEDB();
        };
        ghalf1();
        if (__builtin_expect(__any(mx == POISON), 0)) {
            int spins = 0; const int lim = dead ? 1 : SPIN_MAX; bool bad;
            ldist[0] = sv1;
            _Pragma("nounroll") do { ghalf1(); bad = __any(mx == POISON); } while (bad && ++spins < lim);
            if (bad) dead = true;
        }
        if (w == 0) EV(1);
        *rflagw = sv1;
        unsigned fl = *rflag;
        u32x4 Rc = *(volatile LAS u32x4*)(lrc);
        SCHEDB();
        u32x4 Rce, Rco, Ro1;
        acc[0] = mfma32(aw[1][0][0], Re1, acc[0]);
        Ro1.x = R1.x & nE; Ro1.y = R1.y & nE;
        SCHEDB();
        acc[1] = mfma32(aw[1][0][1], Re1, acc[1]);
        Ro1.z = R1.z & nE; Ro1.w = R1.w & nE;
        PIN(Ro1);
        SCHEDB();
        acc[2] = mfma32(aw[1][0][2], Re1, acc[2]);
        SCHEDB();
        acc[3] = mfma32(aw[1][0][3], Re1, acc[3]);
        SCHEDB();
        if (__builtin_expect((unsigned)__builtin_amdgcn_readfirstlane(fl) != (unsigned)(s + 1), 0)) {
            int g = 0;
            _Pragma("nounroll") do { fl = *rflag; Rc = *(volatile LAS u32x4*)(lrc); }
            while ((unsigned)__builtin_amdgcn_readfirstlane(fl) != (unsigned)(s + 1) && ++g < (1 << 22));
        }
        SCHEDB();
        acc[0] = mfma32(aw[1][1][0], Ro1, acc[0]);
        Rce.x = Rc.x & mE; Rce.y = Rc.y & mE;
        SCHEDB();
        acc[1] = mfma32(aw[1][1][1], Ro1, acc[1]);
        Rce.z = Rc.z & mE; Rce.w = Rc.w & mE;
        PIN(Rce);
        SCHEDB();
        acc[2] = mfma32(aw[1][1][2], Ro1, acc[2]);
        Rco.x = dppu<DPP_ROR8>(Rc.x) & mE; Rco.y = dppu<DPP_ROR8>(Rc.y) & mE;
        SCHEDB();
        acc[3] = mfma32(aw[1][1][3], Ro1, acc[3]);
        Rco.z = dppu<DPP_ROR8>(Rc.z) & mE; Rco.w = dppu<DPP_ROR8>(Rc.w) & mE;
        PIN(Rco);
        SCHEDB();
        gE = pack8(acc[0], acc[1]);
        f32x4 d2 = mfma32(gE, Rce, (f32x4){0, 0, 0, 0});
        gO = pack8(acc[2], acc[3]);
        d2 = mfma32(gO, Rco, d2);
        if (col < 8) {
            float* red = (float*)(lds + L_RED1) + (col * 4 + 2 * (q4 & 1)) * 20 + w * 4 + (q4 >> 1) * 2;
            *(f32x2*)(red) = (f32x2){d2[0], d2[1]};
            *(f32x2*)(red + 20) = (f32x2){d2[2], d2[3]};
        }
        unsigned tpre = 0;
        if (!LAST && r == 0 && w == 0) tpre = rt32();
        PTB(12);
        bar();
        PTB(13);
        if (!LAST && w == 0) {
            const float* red = (const float*)(lds + L_RED1) + (lane & 31) * 20;
            const f32x4 a = *(const f32x4*)(red), b2 = *(const f32x4*)(red + 4), c = *(const f32x4*)(red + 8), d = *(const f32x4*)(red + 12);
            const f32x4 t4 = (a + b2) + (c + d);
            unsigned o = nopoison(pk2(t4.x + t4.z, t4.y + t4.w));
            if (r == 0) {
                const bool dist0 = dh_prev || (unsigned)__builtin_amdgcn_readfirstlane(ldist[0]) == (unsigned)(s + 1);
                const unsigned tv = nopoison(tmax(th, tpre + 3u + MINH) + (unsigned)(C2H + (dist0 ? CUP : 0)) + ((lane & 1) ? (unsigned)O1T : 0u));
                o = (lane >= 32) ? tv : o;
            }
            if (lane < npub) AST1(o, vo_pp, ds, oM);
            PTB(14);
            EV(2);
            SCHEDB();
        }
        unsigned tb1 = 0, the = 0;
        if (!LAST && w == 1) { tb1 = rt32(); the = tmax(th, tb1 + MINH); }
#pragma unroll
        for (int it = 0; it < 2; ++it) {
            const char* pxb = lds + L_PX + lane * 16;
            const u32x4 x0 = *(const u32x4*)(pxb + (it * 2 + 0) * 1024), x1 = *(const u32x4*)(pxb + (it * 2 + 1) * 1024);
            f32x4 dx = mfma32(gE, x0, (f32x4){0, 0, 0, 0});
            dx = mfma32(gO, x1, dx);
            *(f32x4*)(lds + L_REDX + (2 * w + (q4 >> 1)) * 1024 + ((it * 2 + (q4 & 1)) * 16 + col) * 16) = dx;
        }
        if (!LAST && w == 1) {
            wait_until(the);
            ALD1R(ts1, vo_ts, ds, oM);
            asm volatile("s_waitcnt vmcnt(0)" : "+v"(ts1) :: "memory");
            if (__builtin_expect(__any(ts1 == POISON), 0)) {
                int spins = 0; const int lim = dead ? 1 : SPIN_MAX; bool bad;
                ldist[1] = sv1;
                _Pragma("nounroll") do {
                    ALD1R(ts1, vo_ts, ds, oM);
                    asm volatile("s_waitcnt vmcnt(0)" : "+v"(ts1) :: "memory");
                    bad = __any(ts1 == POISON);
                } while (bad && ++spins < lim);
                if (bad) dead = true;
            }
            if (lane < 2) *(unsigned*)(lds + L_TS + lane * 4) = ts1;
        }
        bar();
        if (!LAST && w == 3) {
            const float* red = (const float*)(lds + L_RED2) + (lane & 15) * 36 + ((lane >> 4) & 1) * 16;
            const f32x4 a = *(const f32x4*)(red), b2 = *(const f32x4*)(red + 4), c = *(const f32x4*)(red + 8), d = *(const f32x4*)(red + 12);
            const f32x4 t4 = (a + b2) + (c + d);
            float v0 = t4.x + t4.z, v1 = t4.y + t4.w;
            { float x0, x1; swap16(v0, x0, x1); v0 = x0 + x1; swap16(v1, x0, x1); v1 = x0 + x1; }
            const unsigned o = nopoison(pk2(v0, v1));
            if (lane < 16) AST1(o, vo_mp, ds, oMn);
            EV(0);
            SCHEDB();
        }
        const unsigned tb2 = rt32() - (w == 3 ? 9u : 0u);
        if (w == 1) EV(5);
        if (w == 2) {
            f32x4 t4 = *(const f32x4*)(lds + L_REDX + lane * 16);
#pragma unroll
            for (int pp = 1; pp < 8; ++pp) t4 += *(const f32x4*)(lds + L_REDX + pp * 1024 + lane * 16);
            const u32x4 xm = __builtin_bit_cast(u32x4, t4);
            AST4(xm, vo_xm, ds, (unsigned)s * (16u * 7u * 1024u));
        }
        if (!LAST) {
          const u32x2 t2 = *(const u32x2*)(lds + L_TS);
          dh_prev = (unsigned)__builtin_amdgcn_readfirstlane(ldist[1]) == (unsigned)(s + 1);
          tm = tmax((unsigned)__builtin_amdgcn_readfirstlane(t2.x), tb2 + MINM); th = (unsigned)__builtin_amdgcn_readfirstlane(t2.y);
          if (r == 0 && w == 1 && s >= 1) {
              __builtin_amdgcn_s_sleep(5);
              if (lane == 0) AST1(1u, (unsigned)(OFF_FLAG + FLAG_PROG * 4), ds, (unsigned)(s - 1) * 128u);
          }
        }
    };
    _Pragma("nounroll") for (int s = 0; s < NSEG - 1; ++s) step(s, BoolC<false>{});
    step(NSEG - 1, BoolC<true>{});
    AWAIT(0);
    bar();
    if (w == 2 && lane == 0) AST1(1u, (unsigned)(OFF_FLAG + (FLAG_DONE + r) * 4), ds, 0u);
}

DEVI void chain_bw(const Params& p, int r, char* lds, u32x4 ds) {
    const rsrc_t rs = mkrsrc(p.ws, 0x10000000u);
    const int tid = threadIdx.x, lane = tid & 63, w = __builtin_amdgcn_readfirstlane(tid >> 6), hw = w & 3;
    const int col = lane & 15, q4 = lane >> 4;
    const int c7 = r % 7, beta = r >> 1, rh = r & 1;
    u32x4 bw[2][14];
#pragma unroll
    for (int a = 0; a < 2; ++a)
#pragma unroll
        for (int t = 0; t < 14; ++t) bw[a][t] = *(const u32x4*)(p.ws + OFF_BF + ((size_t)((2 * hw + a) * 56 + beta) * 14 + t) * FR + lane * 16);
    u32x4 sel;
    { const int j = col & 7; const unsigned one = (j & 1) ? 0x3F800000u : 0x00003F80u; sel.x = (j >> 1) == 0 ? one : 0; sel.y = (j >> 1) == 1 ? one : 0; sel.z = (j >> 1) == 2 ? one : 0; sel.w = (j >> 1) == 3 ? one : 0; }
    const unsigned cfg = get_cfg(p, rs, lds);
    WAITVM0();
    bool dead = false;
    const int hh = col >> 2, i2 = col & 3, hB = 4 * hw + hh;
    const unsigned vo_pa = (unsigned)((rh ? 16384 : 32768) + hB * 512 + q4 * 64 + i2 * 16);
    const unsigned vo_pb = (q4 == 3) ? OOB : vo_pa + 256u;
    const unsigned vo_stat = (unsigned)(STATS_OFF + rh * 512 + (hB * 4 + i2) * 4);
    const unsigned vo_ex = (unsigned)(49152 + rh * 4096 + hB * 256 + q4 * 64 + i2 * 16);
    const unsigned vo_u0 = (unsigned)(OFF_UF + ((size_t)beta * 8 + 2 * hw) * FR + lane * 16), vo_u1 = vo_u0 + 1024;
    const unsigned vo_x = (unsigned)(OFF_XF + (size_t)((hw >> 1) * 14 + 2 * c7 + (hw & 1)) * FR + lane * 16);
    const unsigned vo_fu = (unsigned)(OFF_FLAG + (FLAG_U + beta / 14) * 4);
    const unsigned vo_wc = (rh == 0 && lane < 16) ? (unsigned)(OFF_WC + ((size_t)beta * 8 + 2 * hw) * 256 + col * 16) : OOB;
    const bool diag = (col >> 3) == (q4 >> 1);
    const bool lowh = hh < 2, outv = (hh & 1) == (q4 >> 1);
    u32x4 un0 = {0,0,0,0}, un1 = {0,0,0,0}, px = {0,0,0,0};
    unsigned fu = 0;
    u32x4 pa = {0,0,0,0}, pb = {0,0,0,0}, ex = {0,0,0,0}; unsigned st = 0;
    unsigned th = 0;
    volatile LAS unsigned* ldist = (volatile LAS unsigned*)((LAS char*)lds + L_RF + 4);
    await_flag(ds, vo_fu, 8, dead);
    ALD4R(un0, vo_u0, ds, 0u); ALD4R(un1, vo_u1, ds, 0u); ALD4PR(px, vo_x, ds, 0u);
    ALD1R(fu, vo_fu, ds, 64u);
    th = start_sync(p, ds, r, lds) + O1T;
    auto step = [&](const int s, auto LASTC) {
        constexpr bool LAST = decltype(LASTC)::value;
        const unsigned oM = chunk_off(p, cfg, s, 0);
        unsigned sv1 = (unsigned)(s + 1); PIN(sv1);
        asm volatile("s_waitcnt vmcnt(0)" : "+v"(un0), "+v"(un1), "+v"(px), "+v"(fu) :: "memory");
        SCHEDB();
        u32x4 uf0 = un0, uf1 = un1;
        asm volatile("" : "+v"(uf0), "+v"(uf1));
        const unsigned fu_s = (unsigned)__builtin_amdgcn_readfirstlane(fu);
        *(u32x4*)(lds + L_PX + hw * 1024 + lane * 16) = px;
        if (s + 1 < NSEG - 1 && fu_s < 8u) await_flag(ds, vo_fu + (unsigned)(s + 1) * 64u, 8, dead);
        bar();
        const unsigned tb1 = rt32();
        SCHEDB();
        u32x4 Rq[7];
#pragma unroll
        for (int q = 0; q < 7; ++q) Rq[q] = *(const u32x4*)(lds + L_R + q * 1024 + lane * 16);
        SCHEDB();
        const unsigned the = tmax(th, tb1 + MINH);
        f32x4 ae[2] = {{0,0,0,0},{0,0,0,0}}, ao[2] = {{0,0,0,0},{0,0,0,0}};
#pragma unroll
        for (int q = 0; q < WSPLIT; ++q)
#pragma unroll
            for (int a = 0; a < 2; ++a) { ae[a] = mfma32(Rq[q], bw[a][2 * q], ae[a]); ao[a] = mfma32(Rq[q], bw[a][2 * q + 1], ao[a]); }
        SCHEDB();
        if (!LAST) {
            wait_until(the);
            if (w == 6) EV(6);
            ALD4R(pa, vo_pa, ds, oM);
            ALD4R(pb, vo_pb, ds, oM);
            ALD1R(st, vo_stat, ds, oM);
            ALD4R(ex, vo_ex, ds, oM);
            SCHEDB();
        }
#pragma unroll
        for (int q = WSPLIT; q < 7; ++q)
#pragma unroll
            for (int a = 0; a < 2; ++a) { ae[a] = mfma32(Rq[q], bw[a][2 * q], ae[a]); ao[a] = mfma32(Rq[q], bw[a][2 * q + 1], ao[a]); }
        u32x2 wop[2]; u32x4 wst[2];
#pragma unroll
        for (int a = 0; a < 2; ++a) {
            float wv[4];
#pragma unroll
            for (int rr = 0; rr < 4; ++rr) { const float x = (lane < 32) ? ae[a][rr] : ao[a][rr]; float u, v; swap32(x, u, v); wv[rr] = u + v; }
            const u32x2 wp = {pk2(wv[0], wv[1]), pk2(wv[2], wv[3])};
            wop[a].x = diag ? wp.x : 0u; wop[a].y = diag ? wp.y : 0u;
            unsigned a0, b0, a1, b1; uswap16(wp.x, a0, b0); uswap16(wp.y, a1, b1); wst[a] = (u32x4){a0, a1, b0, b1};
        }
        asm volatile("" : "+v"(wop[0]), "+v"(wop[1]), "+v"(wst[0]), "+v"(wst[1]));
        SCHEDB();
        if (!LAST) {
            f32x4 ro; unsigned pchk;
            auto bstage = [&]() {
                f32x4 sm = mfma32(sel, pa, (f32x4){0, 0, 0, 0});
                sm = mfma32(sel, pb, sm);
                PIN(sm);
                SCHEDB();
                f32x4 dx0 = mfma32(uf0, ex, (f32x4){0, 0, 0, 0}), dx1 = mfma32(uf1, ex, (f32x4){0, 0, 0, 0});
                f32x4 sx4 = mfma32((u32x4){0x3F803F80u, 0x3F803F80u, 0x3F803F80u, 0x3F803F80u}, ex, (f32x4){0, 0, 0, 0});
                PIN(dx0); PIN(dx1); PIN(sx4);
                const float mxs = __uint_as_float(st), sxs = sx4[0];
                unsigned chk = max(max(max4(pa), max4(pb)), max(max4(ex), st));
                PIN(chk); pchk = chk;
                SCHEDB();
                float ml = fmx(fmx(sm[0], sm[1]), fmx(sm[2], sm[3]));
                { float a, b2; swap16(ml, a, b2); ml = fmx(fmx(a, b2), mxs); }
                const float nM = -ml * SC2;
                float e[4];
#pragma unroll
                for (int rr = 0; rr < 4; ++rr) e[rr] = __builtin_amdgcn_exp2f(__builtin_fmaf(sm[rr], SC2, nM));
                float al = __builtin_amdgcn_exp2f(__builtin_fmaf(mxs, SC2, nM));
                PIN(al);
                const u32x2 pp = {pk2(e[0], e[1]), pk2(e[2], e[3])};
                f32x4 c0;
#pragma unroll
                for (int rr = 0; rr < 4; ++rr) c0[rr] = (lowh ? dx0[rr] : dx1[rr]) * al;
                const f32x4 r0 = mfma16(wop[0], pp, c0), r1 = mfma16(wop[1], pp, c0);
                float ls = (e[0] + e[1]) + (e[2] + e[3]);
                { float a, b2; swap16(ls, a, b2); ls = a + b2; }
                const float inv = __builtin_amdgcn_rcpf(__builtin_fmaf(al, sxs, ls));
#pragma unroll
                for (int rr = 0; rr < 4; ++rr) ro[rr] = (lowh ? r0[rr] : r1[rr]) * inv;
            };
            {
                const unsigned sn = (unsigned)(s + 1);
                ALD4R(un0, vo_u0, ds, sn * (56u * 8u * 1024u));
                ALD4R(un1, vo_u1, ds, sn * (56u * 8u * 1024u));
                ALD4PR(px, vo_x, ds, sn * 28672u);
                ALD1R(fu, vo_fu, ds, (sn + 1u) * 64u);
            }
            PTB(0);
            asm volatile("s_waitcnt vmcnt(4)" : "+v"(pa), "+v"(pb), "+v"(st), "+v"(ex) :: "memory");
            PTB(1);
            SCHEDB();
            bstage();
            if (__builtin_expect(__any(pchk == POISON), 0)) {
              int spins = 0; const int lim = dead ? 1 : SPIN_MAX; bool bad;
              ldist[1] = sv1;
              _Pragma("nounroll") do {
                ALD4R(pa, vo_pa, ds, oM);
                ALD4R(pb, vo_pb, ds, oM);
                ALD1R(st, vo_stat, ds, oM);
                ALD4R(ex, vo_ex, ds, oM);
                asm volatile("s_waitcnt vmcnt(0)" : "+v"(pa), "+v"(pb), "+v"(st), "+v"(ex) :: "memory");
                bad = __any(max(max(max4(pa), max4(pb)), max(max4(ex), st)) == POISON);
              } while (bad && ++spins < lim);
              if (bad) dead = true;
              bstage();
            }
            if (w == 7) EV(3);
            if (outv) {
                float* red = (float*)(lds + L_RED2) + (i2 * 4 + 2 * (q4 & 1)) * 36 + hB * 2;
                *(f32x2*)(red) = (f32x2){ro[0], ro[1]};
                *(f32x2*)(red + 36) = (f32x2){ro[2], ro[3]};
            }
            PTB(3);
        }
        bar();
        if (!LAST) PTB(4);
        if (LAST || WPOS != 1) { AST4(wst[0], vo_wc, ds, (unsigned)s * (56u * 8u * 256u)); AST4(wst[1], vo_wc + 256u, ds, (unsigned)s * (56u * 8u * 256u)); }
        if (!LAST) th = (unsigned)__builtin_amdgcn_readfirstlane(*(const unsigned*)(lds + L_TS + 4));
    };
    _Pragma("nounroll") for (int s = 0; s < NSEG - 1; ++s) step(s, BoolC<false>{});
    step(NSEG - 1, BoolC<true>{});
    AWAIT(0);
    bar();
}

DEVI void chain_g(const Params& p, int r, char* lds) {
    const int w = __builtin_amdgcn_readfirstlane(threadIdx.x >> 6);
    const u32x4 ds = mkdesc(p.ws, 0x10000000u);
    const unsigned t_entry = rt32();
    __builtin_amdgcn_s_sleep(GDELAY);
    if (w < 4) chain_gw(p, r, lds, ds, t_entry); else chain_bw(p, r, lds, ds);
}

DEVI void x_finalize(rsrc_t rs, const u32x4 (&xm)[7], f32x4 s0, f32x4 s1, int s, int h, int it, int lane) {
    const int col = lane & 15, q4 = lane >> 4;
    f32x4 sm = {0, 0, 0, 0};
#pragma unroll
    for (int c = 0; c < 7; ++c) sm += __builtin_bit_cast(f32x4, xm[c]);
    const float NEG = -3.0e38f;
    float v0[4], v1[4], vm[4];
#pragma unroll
    for (int rr = 0; rr < 4; ++rr) {
        const bool causal = (4 * q4 + rr) <= col;
        v0[rr] = (it == 1 || causal) ? s0[rr] : NEG;
        v1[rr] = (it == 1 && causal) ? s1[rr] : NEG;
        vm[rr] = (q4 < 2) ? sm[rr] : NEG;
    }
    float m = NEG;
#pragma unroll
    for (int rr = 0; rr < 4; ++rr) m = fmaxf(m, fmaxf(fmaxf(v0[rr], v1[rr]), vm[rr]));
    { float a, b2; swap16(m, a, b2); m = fmaxf(a, b2); swap32(m, a, b2); m = fmaxf(a, b2); }
    const float nM = -m * SC2;
    float l = 0;
#pragma unroll
    for (int rr = 0; rr < 4; ++rr) {
        v0[rr] = (v0[rr] == NEG) ? 0.f : __builtin_amdgcn_exp2f(__builtin_fmaf(v0[rr], SC2, nM));
        v1[rr] = (v1[rr] == NEG) ? 0.f : __builtin_amdgcn_exp2f(__builtin_fmaf(v1[rr], SC2, nM));
        vm[rr] = (vm[rr] == NEG) ? 0.f : __builtin_amdgcn_exp2f(__builtin_fmaf(vm[rr], SC2, nM));
        l += v0[rr] + v1[rr] + vm[rr];
    }
    { float a, b2; swap16(l, a, b2); l = a + b2; swap32(l, a, b2); l = a + b2; }
    const float inv = __builtin_amdgcn_rcpf(l);
    const u32x4 px = {pk2(v0[0] * inv, v0[1] * inv), pk2(v0[2] * inv, v0[3] * inv), pk2(v1[0] * inv, v1[1] * inv), pk2(v1[2] * inv, v1[3] * inv)};
    bst4(rs, px, (unsigned)(OFF_PX + (((size_t)s * 16 + h) * 2 + it) * FR + lane * 16), 0);
    if (q4 < 2) { const u32x2 pm = {pk2(vm[0] * inv, vm[1] * inv), pk2(vm[2] * inv, vm[3] * inv)};
        bst2(rs, pm, (unsigned)(OFF_PM + (((size_t)s * 8 + (h >> 1)) * 2 + it) * 512 + (col + 16 * (2 * (h & 1) + q4)) * 8), 0); }
}

DEVI void chain_x(const Params& p, int h, char* lds) {
    const int tid = threadIdx.x, lane = tid & 63, w = __builtin_amdgcn_readfirstlane(tid >> 6);
    const int col = lane & 15, q4 = lane >> 4;
    const rsrc_t rs = mkrsrc(p.ws, 0x10000000u);
    const u32x4 ds = mkdesc(p.ws, 0x10000000u);
    if (w == 0 && (h & 3) == 0) probe_cfg(p, rs, h >> 2);
    const unsigned cfg = get_cfg(p, rs, lds);
    WAITVM0();
    bool dead = false;
    if (w < 4) {
        const unsigned mE = (col < 8) ? 0xFFFFFFFFu : 0u;
        const int nq = (w < 3) ? 2 : 1;
        const unsigned vo_m0 = (unsigned)(2 * w * 1024 + lane * 16), vo_m1 = (unsigned)((2 * w + nq - 1) * 1024 + lane * 16);
        const unsigned vo_e = (col < 8) ? (unsigned)(49152 + ((col >> 2) & 1) * 4096 + h * 256 + q4 * 64 + (col & 3) * 16)
                            : ((lane == 8 || lane == 12) ? (unsigned)(STATS_OFF + ((lane >> 2) & 1) * 512 + h * 16) : OOB);
        u32x4 R0 = {0,0,0,0}, R1 = {0,0,0,0};
        unsigned tmx = start_sync(p, ds, 112 + h, lds);
        unsigned thx = tmx + O1T;
        __builtin_amdgcn_s_setprio(3);
        for (int s = 0; s < NSEG; ++s) {
            const unsigned oM = chunk_off(p, cfg, s, 0), oE = oM + 3u * 16384u;
            if (s < NSEG - 1) {
                u32x4 g[2][2][2];
#pragma unroll
                for (int qi = 0; qi < 2; ++qi)
#pragma unroll
                    for (int e = 0; e < 2; ++e)
#pragma unroll
                        for (int k = 0; k < 2; ++k) g[qi][e][k] = *(const u32x4*)(lds + L_GX + (s & 1) * 28672 + (k * 14 + 2 * ((w < 3) ? 2 * w + qi : 2 * w) + e) * 1024 + lane * 16);
                if (s > 0) {
                    wait_until(thx);
                    unsigned t1 = 0; int spins = 0; const int lim = dead ? 1 : SPIN_MAX; bool bad;
                    const unsigned oT = chunk_off(p, cfg, s - 1, 0); const unsigned vts = (unsigned)(TS_OFF + (lane & 1) * 4);
                    _Pragma("nounroll") do {
                        ALD1R(t1, vts, ds, oT);
                        asm volatile("s_waitcnt vmcnt(0)" : "+v"(t1) :: "memory");
                        bad = __any(t1 == POISON);
                        if (bad) __builtin_amdgcn_s_sleep(4);
                    } while (bad && ++spins < lim);
                    if (bad) dead = true;
                    thx = (unsigned)__builtin_amdgcn_readlane(t1, 1);
                    tmx = (unsigned)__builtin_amdgcn_readlane(t1, 0);
                }
                wait_until(tmx + XOFFT);
                { int spins = 0; const int lim = dead ? 1 : SPIN_MAX; bool bad;
                  _Pragma("nounroll") do {
                    ALD4R(R0, vo_m0, ds, oM);
                    ALD4R(R1, vo_m1, ds, oM);
                    asm volatile("s_waitcnt vmcnt(0)" : "+v"(R0), "+v"(R1) :: "memory");
                    bad = __any(max(max4(R0), max4(R1)) == POISON);
                  } while (bad && ++spins < lim);
                  if (bad) dead = true; }
                f32x4 d[2] = {{0,0,0,0},{0,0,0,0}};
#pragma unroll
                for (int kt = 0; kt < 2; ++kt) { d[kt] = mfma32(g[0][0][kt], R0 & mE, d[kt]); d[kt] = mfma32(g[0][1][kt], R0 & ~mE, d[kt]); }
                if (nq == 2) {
#pragma unroll
                    for (int kt = 0; kt < 2; ++kt) { d[kt] = mfma32(g[1][0][kt], R1 & mE, d[kt]); d[kt] = mfma32(g[1][1][kt], R1 & ~mE, d[kt]); }
                }
                f32x4 o;
#pragma unroll
                for (int rr = 0; rr < 4; ++rr) {
                    const float a = d[0][rr] + dppf<DPP_ROR8>(d[0][rr]), b2 = d[1][rr] + dppf<DPP_ROR8>(d[1][rr]);
                    o[rr] = (col < 8) ? a : b2;
                }
                *(f32x4*)(lds + L_REDX + w * 1024 + lane * 16) = o;
            }
            bar();
            if (w == 0 && s < NSEG - 1) {
                f32x4 t4 = *(const f32x4*)(lds + L_REDX + lane * 16);
#pragma unroll
                for (int pp = 1; pp < 4; ++pp) t4 += *(const f32x4*)(lds + L_REDX + pp * 1024 + lane * 16);
                float m = fmx(fmx(t4.x, t4.y), fmx(t4.z, t4.w));
                m = fmx(m, dppf<DPP_ROR8>(m));
                { float a, b2; swap16(m, a, b2); m = fmx(a, b2); swap32(m, a, b2); m = fmx(a, b2); }
                const unsigned mb = nopoison(__float_as_uint(m));
                u32x4 ms = {dppu<0x00>(mb), dppu<0x55>(mb), dppu<0xAA>(mb), dppu<0xFF>(mb)};
                const float nM = -m * SC2;
                float e[4];
#pragma unroll
                for (int rr = 0; rr < 4; ++rr) e[rr] = __builtin_amdgcn_exp2f(__builtin_fmaf(t4[rr], SC2, nM));
                const unsigned o0 = nopoison(pk2(e[0], e[1])), o1 = nopoison(pk2(e[2], e[3]));
                const unsigned o2 = dppu<DPP_ROR8>(o0), o3 = dppu<DPP_ROR8>(o1);
                const u32x4 o = {col < 8 ? o0 : ms.x, col < 8 ? o1 : ms.y, col < 8 ? o2 : ms.z, col < 8 ? o3 : ms.w};
                AST4(o, vo_e, ds, oM);
            }
        }
        bar();
    } else {
        const int hw = w - 4;
        u32x4 gb[7];
#pragma unroll
        for (int k = 0; k < 7; ++k) gb[k] = (u32x4){0, 0, 0, 0};
        const unsigned vo_fg = (unsigned)(OFF_FLAG + (FLAG_GX + h) * 4);
        const unsigned vo_g = (unsigned)(OFF_GX + ((size_t)h * 28 + hw * 7) * FR + lane * 16);
        auto issue = [&](int sg) {
#pragma unroll
            for (int k = 0; k < 7; ++k) ALD4R(gb[k], vo_g + (unsigned)k * 1024u, ds, (unsigned)sg * (16u * 28u * 1024u));
        };
        auto put = [&](int sg) {
            asm volatile("s_waitcnt vmcnt(0)" : "+v"(gb[0]), "+v"(gb[1]), "+v"(gb[2]), "+v"(gb[3]), "+v"(gb[4]), "+v"(gb[5]), "+v"(gb[6]) :: "memory");
            SCHEDB();
#pragma unroll
            for (int k = 0; k < 7; ++k) *(u32x4*)(lds + L_GX + (sg & 1) * 28672 + (hw * 7 + k) * 1024 + lane * 16) = gb[k];
            SCHEDB();
        };
        await_flag(ds, vo_fg, 2, dead);
        issue(0);
        put(0);
        await_flag(ds, vo_fg + 64u, 2, dead);
        issue(1);
        (void)start_sync(p, ds, 112 + h, lds);
        _Pragma("nounroll") for (int s = 0; s < NSEG; ++s) {
            __builtin_amdgcn_s_sleep(XHD);
            put(s + 1);
            const int sn = (s + 2 < NSEG) ? s + 2 : NSEG - 1;
            await_flag(ds, vo_fg + (unsigned)sn * 64u, 2, dead);
            issue(__builtin_amdgcn_readfirstlane(sn));
            bar();
        }
        AWAIT(0);
        bar();
    }
}

DEVI void wait_prog(const Params& p, int s, bool& dead) {
    const int w = __builtin_amdgcn_readfirstlane(threadIdx.x >> 6), lane = threadIdx.x & 63;
    if (w == 0) {
        const unsigned t0 = __builtin_amdgcn_readfirstlane(ldflag(p, FLAG_T0));
        if (t0) {
            const unsigned target = t0 + (unsigned)((s + 2) * (O1T + C2H) + 10);
            int g = 0;
            _Pragma("nounroll") while ((int)(rt32() - target) < 0 && ++g < 20000) __builtin_amdgcn_s_sleep(8);
        }
        int k = dead ? SPIN_MAX : 0;
        _Pragma("nounroll") for (; k < SPIN_MAX; ++k) {
            unsigned v;
            if (s <= NSEG - 3) v = ldflag(p, FLAG_PROG + s * 32);
            else { const unsigned a = ldflag(p, FLAG_DONE + lane), b2 = ldflag(p, FLAG_DONE + (lane < 48 ? 64 + lane : 0)); v = a & b2; }
            if (!__any(v == 0u)) break;
            __builtin_amdgcn_s_sleep(8);
        }
        if (k >= SPIN_MAX) dead = true;
    }
    __syncthreads();
}

DEVI void fin_unit(const Params& p, int s, int half, char* lds, bool& dead) {
    const int tid = threadIdx.x, lane = tid & 63, w = __builtin_amdgcn_readfirstlane(tid >> 6);
    const int col = lane & 15, q4 = lane >> 4, h = 8 * half + w;
    const rsrc_t rs = mkrsrc(p.ws, 0x10000000u);
    __syncthreads();
#pragma unroll
    for (int j = 0; j < 4; ++j) { const int f = w + 8 * j; if (f < 28) *(u32x4*)(lds + f * 1024 + lane * 16) = *(const u32x4*)(p.ws + OFF_XF + ((size_t)s * 28 + f) * FR + lane * 16); }
    waitflag(p, FLAG_GX + s * 16 + h, 2, dead);
    __syncthreads();
    f32x4 xx[3] = {{0,0,0,0},{0,0,0,0},{0,0,0,0}};
#pragma unroll
    for (int half2 = 0; half2 < 2; ++half2) {
        u32x4 g0[7], g1[7];
#pragma unroll
        for (int t = 0; t < 7; ++t) {
            g0[t] = bld4(rs, (unsigned)(OFF_GX + (((size_t)s * 16 + h) * 28 + 7 * half2 + t) * FR + lane * 16), 0);
            g1[t] = bld4(rs, (unsigned)(OFF_GX + (((size_t)s * 16 + h) * 28 + 14 + 7 * half2 + t) * FR + lane * 16), 0);
        }
#pragma unroll
        for (int t = 0; t < 7; ++t) {
            const u32x4 x0 = *(const u32x4*)(lds + (7 * half2 + t) * 1024 + lane * 16), x1 = *(const u32x4*)(lds + (14 + 7 * half2 + t) * 1024 + lane * 16);
            xx[0] = mfma32(g0[t], x0, xx[0]);
            xx[1] = mfma32(g0[t], x1, xx[1]);
            xx[2] = mfma32(g1[t], x1, xx[2]);
        }
    }
    wait_prog(p, s, dead);
    u32x4 xm[2][7];
#pragma unroll
    for (int it = 0; it < 2; ++it)
#pragma unroll
        for (int c = 0; c < 7; ++c) xm[it][c] = bld4(rs, (q4 < 2) ? (unsigned)(OFF_XM + (((size_t)s * 16 + h) * 7 + c) * 1024 + ((it * 2 + q4) * 16 + col) * 16) : OOB, 0);
    x_finalize(rs, xm[0], xx[0], xx[0], s, h, 0, lane);
    x_finalize(rs, xm[1], xx[1], xx[2], s, h, 1, lane);
    WAITVM0();
    __syncthreads();
    if (tid == 0) bst1(rs, 1u, (unsigned)(OFF_FLAG + (FLAG_P + s * 32 + half) * 4), 0);
}

DEVI void out_unit(const Params& p, int s, int bg, bool& dead) {
    const int tid = threadIdx.x, lane = tid & 63, w = __builtin_amdgcn_readfirstlane(tid >> 6);
    const int col = lane & 15, q4 = lane >> 4, it = w & 1;
    const rsrc_t rs = mkrsrc(p.ws, 0x10000000u);
    const int b0 = 8 * bg + 2 * (w >> 1);
    waitflag(p, FLAG_U + s * 16 + b0 / 14, 8, dead);
    waitflag(p, FLAG_U + s * 16 + (b0 + 1) / 14, 8, dead);
    u32x4 uf[2][8];
#pragma unroll
    for (int bi = 0; bi < 2; ++bi)
#pragma unroll
        for (int hp = 0; hp < 8; ++hp) uf[bi][hp] = bld4(rs, (unsigned)(OFF_UF + (((size_t)s * 56 + b0 + bi) * 8 + hp) * FR + lane * 16), 0);
    wait_prog(p, s, dead);
    const bool diag = (col >> 3) == (q4 >> 1);
    u32x2 wv[2][8];
#pragma unroll
    for (int bi = 0; bi < 2; ++bi)
#pragma unroll
        for (int hp = 0; hp < 8; ++hp) wv[bi][hp] = bld2(rs, diag ? (unsigned)(OFF_WC + (((size_t)s * 56 + b0 + bi) * 8 + hp) * 256 + col * 16 + (q4 & 1) * 8) : OOB, 0);
    waitall(p, FLAG_P + s * 32, 2, dead);
    f32x4 aa[2], ab[2], am[2];
#pragma unroll
    for (int bi = 0; bi < 2; ++bi) { aa[bi] = (f32x4){0,0,0,0}; ab[bi] = (f32x4){0,0,0,0}; am[bi] = (f32x4){0,0,0,0}; }
#pragma unroll
    for (int hp = 0; hp < 8; ++hp) {
        const u32x4 pa = bld4(rs, (unsigned)(OFF_PX + (((size_t)s * 16 + 2 * hp) * 2 + it) * FR + lane * 16), 0);
        const u32x4 pb = bld4(rs, (unsigned)(OFF_PX + (((size_t)s * 16 + 2 * hp + 1) * 2 + it) * FR + lane * 16), 0);
        const unsigned pmo = (unsigned)(OFF_PM + (((size_t)s * 8 + hp) * 2 + it) * 512 + lane * 8);
        const u32x2 pm = {bld1(rs, pmo, 0), bld1(rs, pmo + 4, 0)};
#pragma unroll
        for (int bi = 0; bi < 2; ++bi) {
            aa[bi] = mfma32(uf[bi][hp], pa, aa[bi]);
            ab[bi] = mfma32(uf[bi][hp], pb, ab[bi]);
            am[bi] = mfma16(wv[bi][hp], pm, am[bi]);
        }
    }
#pragma unroll
    for (int bi = 0; bi < 2; ++bi) {
        float v[4];
#pragma unroll
        for (int rr = 0; rr < 4; ++rr) { const float x = ((q4 >> 1) == 0 ? aa[bi][rr] : ab[bi][rr]) + am[bi][rr]; float a, b2; swap32(x, a, b2); v[rr] = a + b2; }
        if (lane < 32) { const u32x2 o = {pk2(v[0], v[1]), pk2(v[2], v[3])};
            *(u32x2*)(p.out + (size_t)(32 * s + 16 * it + col) * OUTW + 8 * (b0 + bi) + 4 * q4) = o; }
    }
}

DEVI float phi(float t) { return (t > 0.f ? t + 1.f : __builtin_amdgcn_exp2f(t * 1.4426950408889634f)) + 1e-6f; }
DEVI float rbf(float x) { return bf_lo(pk2(x, 0.f)); }

DEVI void b1_unit(const Params& p, int rb, int hp, char* lds) {
    const int tid = threadIdx.x, lane = tid & 63, w = __builtin_amdgcn_readfirstlane(tid >> 6);
    const int col = lane & 15, q4 = lane >> 4;
    const rsrc_t rs = mkrsrc(p.ws, 0x10000000u);
    const int h = 2 * hp + (w & 1), rt0 = rb * 8 + (w >> 1) * 2;
    f32x4 aq[2][2], ak[2][2], av[2][2];
#pragma unroll
    for (int a = 0; a < 2; ++a)
#pragma unroll
        for (int c = 0; c < 2; ++c) { aq[a][c] = (f32x4){0,0,0,0}; ak[a][c] = (f32x4){0,0,0,0}; av[a][c] = (f32x4){0,0,0,0}; }
#pragma unroll 2
    for (int t = 0; t < 14; ++t) {
        u32x4 xf[2], wq[2], wk[2], wv[2];
#pragma unroll
        for (int a = 0; a < 2; ++a) xf[a] = *(const u32x4*)(p.ws + OFF_BX + ((size_t)(rt0 + a) * 14 + t) * FR + lane * 16);
#pragma unroll
        for (int c = 0; c < 2; ++c) {
            wq[c] = *(const u32x4*)(p.ws + OFF_BW + ((size_t)((0 * 16 + h) * 2 + c) * 14 + t) * FR + lane * 16);
            wk[c] = *(const u32x4*)(p.ws + OFF_BW + ((size_t)((1 * 16 + h) * 2 + c) * 14 + t) * FR + lane * 16);
            wv[c] = *(const u32x4*)(p.ws + OFF_BW + ((size_t)((2 * 16 + h) * 2 + c) * 14 + t) * FR + lane * 16);
        }
#pragma unroll
        for (int a = 0; a < 2; ++a)
#pragma unroll
            for (int c = 0; c < 2; ++c) {
                aq[a][c] = mfma32(wq[c], xf[a], aq[a][c]);
                ak[a][c] = mfma32(xf[a], wk[c], ak[a][c]);
                av[a][c] = mfma32(xf[a], wv[c], av[a][c]);
            }
    }
#pragma unroll
    for (int a = 0; a < 2; ++a) {
        f32x4 p0, p1;
#pragma unroll
        for (int rr = 0; rr < 4; ++rr) {
            p0[rr] = phi(rbf(aq[a][0][rr]));
            p1[rr] = (8 * q4 + 4 + rr < 28) ? phi(rbf(aq[a][1][rr])) : 0.f;
        }
        bst4(rs, pack8(p0, p1), (unsigned)(OFF_BQ + ((size_t)(rt0 + a) * 16 + h) * FR + lane * 16), 0);
    }
    u32x4 pk[2], vv[2];
#pragma unroll
    for (int c = 0; c < 2; ++c) {
        f32x4 k0, k1, v0, v1;
        const int d = 16 * c + col;
#pragma unroll
        for (int rr = 0; rr < 4; ++rr) {
            k0[rr] = (d < 28) ? phi(rbf(ak[0][c][rr])) : 0.f; k1[rr] = (d < 28) ? phi(rbf(ak[1][c][rr])) : 0.f;
            v0[rr] = (d == 28) ? 1.f : av[0][c][rr]; v1[rr] = (d == 28) ? 1.f : av[1][c][rr];
        }
        pk[c] = pack8(k0, k1); vv[c] = pack8(v0, v1);
    }
    float* red = (float*)lds;
#pragma unroll
    for (int dc = 0; dc < 2; ++dc)
#pragma unroll
        for (int ec = 0; ec < 2; ++ec) {
            const f32x4 d = mfma32(pk[dc], vv[ec], (f32x4){0,0,0,0});
            *(f32x4*)(red + ((w * 4 + dc * 2 + ec) * 64 + lane) * 4) = d;
        }
    bar();
    {
        const int hh = w & 1, tile = w >> 1;
        f32x4 t4 = {0,0,0,0};
#pragma unroll
        for (int g = 0; g < 4; ++g) t4 += *(const f32x4*)(red + (((g * 2 + hh) * 4 + tile) * 64 + lane) * 4);
        bst4(rs, __builtin_bit_cast(u32x4, t4), (unsigned)(OFF_BKV + (((size_t)rb * 16 + 2 * hp + hh) * 4 + tile) * 1024 + lane * 16), 0);
    }
    WAITVM0();
    bar();
    if (tid == 0) signal(p, FLAG_B1);
}

DEVI void b2_unit(const Params& p, int h, char* lds, bool& dead) {
    const int tid = threadIdx.x, lane = tid & 63, w = __builtin_amdgcn_readfirstlane(tid >> 6);
    const int col = lane & 15, q4 = lane >> 4;
    const rsrc_t rs = mkrsrc(p.ws, 0x10000000u);
    float* kvs = (float*)lds;
    waitflag(p, FLAG_B1, 128, dead);
    if (w < 4) {
        f32x4 t4 = {0,0,0,0};
#pragma unroll
        for (int rb = 0; rb < 16; ++rb) t4 += __builtin_bit_cast(f32x4, bld4(rs, (unsigned)(OFF_BKV + (((size_t)rb * 16 + h) * 4 + w) * 1024 + lane * 16), 0));
        const int dc = w >> 1, ec = w & 1;
#pragma unroll
        for (int rr = 0; rr < 4; ++rr) kvs[(16 * dc + 4 * q4 + rr) * 33 + 16 * ec + col] = t4[rr];
    }
    bar();
    if (tid < 32) bst1(rs, __float_as_uint(kvs[tid * 33 + 28]), (unsigned)(OFF_BZ + (h * 32 + tid) * 4), 0);
    u32x4 ka[2];
#pragma unroll
    for (int bb = 0; bb < 2; ++bb) {
        const int d = 8 * (col >> 2) + 4 * bb + (col & 3);
        float e[8];
#pragma unroll
        for (int j = 0; j < 8; ++j) e[j] = (8 * q4 + j < 28) ? kvs[d * 33 + 8 * q4 + j] : 0.f;
        ka[bb] = (u32x4){pk2(e[0], e[1]), pk2(e[2], e[3]), pk2(e[4], e[5]), pk2(e[6], e[7])};
    }
    for (int ct = w; ct < 28; ct += 8) {
        u16 e[8];
#pragma unroll
        for (int j = 0; j < 8; ++j) e[j] = (8 * q4 + j < 28) ? p.bwo[(size_t)(28 * h + 8 * q4 + j) * DM + 16 * ct + col] : (u16)0;
        const u32x4 wf = {e[0] | ((unsigned)e[1] << 16), e[2] | ((unsigned)e[3] << 16), e[4] | ((unsigned)e[5] << 16), e[6] | ((unsigned)e[7] << 16)};
        const f32x4 d0 = mfma32(ka[0], wf, (f32x4){0,0,0,0}), d1 = mfma32(ka[1], wf, (f32x4){0,0,0,0});
        bst4(rs, pack8(d0, d1), (unsigned)(OFF_BM + ((size_t)h * 28 + ct) * FR + lane * 16), 0);
    }
    WAITVM0();
    bar();
    if (tid == 0) signal(p, FLAG_B2);
}

DEVI void b3_unit(const Params& p, int u, bool& dead) {
    const int tid = threadIdx.x, lane = tid & 63, w = __builtin_amdgcn_readfirstlane(tid >> 6);
    const int col = lane & 15, q4 = lane >> 4;
    const rsrc_t rs = mkrsrc(p.ws, 0x10000000u);
    const int rt = 2 * u + (w & 1), ct0 = 7 * (w >> 1);
    waitflag(p, FLAG_B2, 16, dead);
    u32x4 pn[16];
#pragma unroll
    for (int h = 0; h < 16; ++h) {
        const u32x4 pq = bld4(rs, (unsigned)(OFF_BQ + ((size_t)rt * 16 + h) * FR + lane * 16), 0);
        const f32x4 z0 = __builtin_bit_cast(f32x4, bld4(rs, (unsigned)(OFF_BZ + (h * 32 + 8 * q4) * 4), 0)), z1 = __builtin_bit_cast(f32x4, bld4(rs, (unsigned)(OFF_BZ + (h * 32 + 8 * q4 + 4) * 4), 0));
        float f[8] = {bf_lo(pq.x), bf_hi(pq.x), bf_lo(pq.y), bf_hi(pq.y), bf_lo(pq.z), bf_hi(pq.z), bf_lo(pq.w), bf_hi(pq.w)};
        float den = f[0] * z0.x + f[1] * z0.y + f[2] * z0.z + f[3] * z0.w + f[4] * z1.x + f[5] * z1.y + f[6] * z1.z + f[7] * z1.w;
        { float a, b2; swap16(den, a, b2); den = a + b2; swap32(den, a, b2); den = a + b2; }
        const float inv = 1.0f / (den + 1e-6f);
        pn[h] = (u32x4){pk2(f[0] * inv, f[1] * inv), pk2(f[2] * inv, f[3] * inv), pk2(f[4] * inv, f[5] * inv), pk2(f[6] * inv, f[7] * inv)};
    }
    for (int i = 0; i < 7; ++i) {
        const int ct = ct0 + i;
        f32x4 acc = {0,0,0,0};
#pragma unroll
        for (int h = 0; h < 16; ++h) acc = mfma32(bld4(rs, (unsigned)(OFF_BM + ((size_t)h * 28 + ct) * FR + lane * 16), 0), pn[h], acc);
        const u32x2 o = {pk2(acc[0], acc[1]), pk2(acc[2], acc[3])};
        *(u32x2*)(p.out + (size_t)(16 * rt + col) * OUTW + 448 + 16 * ct + 4 * q4) = o;
    }
}

DEVI void worker(const Params& p, int u, char* lds) {
    bool dead = false;
    if (p.mode != 2) gemm_worker(p, u, lds);
    if (p.mode == 1) return;
    if (p.mode == 2 && (p.mode2 & 1)) return;
    b1_unit(p, u >> 3, u & 7, lds);
    if (u < 16) b2_unit(p, u, lds, dead);
    if (u >= 64) b3_unit(p, u - 64, dead);
    _Pragma("nounroll") for (int j = u; j < 9 * NSEG; j += 128) {
        const int s = j / 9, k = j % 9;
        if (k < 2) fin_unit(p, s, k, lds, dead); else out_unit(p, s, k - 2, dead);
    }
}

__global__ void __launch_bounds__(512, 2) k_main(Params p) {
    extern __shared__ __attribute__((aligned(16))) char lds[];
    const int b = blockIdx.x;
    const int idx = (b >> 3) * 4 + (b & 3);
    if ((b & 7) < 4) {
        if (p.mode == 1) return;
        if (idx < 112) chain_g(p, idx, lds); else chain_x(p, idx - 112, lds);
    } else {
        worker(p, idx, lds);
    }
}

extern "C" void kernel_launch(void* const* d_in, const int* in_sizes, int n_in, void* d_out, int out_size, void* d_ws, size_t ws_size, hipStream_t stream) {
    Params p;
    p.x = (const u16*)d_in[0]; p.mem0 = (const u16*)d_in[1]; p.Wq = (const u16*)d_in[2]; p.Wk = (const u16*)d_in[3];
    p.Wv = (const u16*)d_in[4]; p.Wo = (const u16*)d_in[5]; p.bx = (const u16*)d_in[7]; p.wqkv = (const u16*)d_in[8]; p.bwo = (const u16*)d_in[9];
    p.out = (u16*)d_out; p.ws = (char*)d_ws;
    const uintptr_t base = (uintptr_t)d_ws;
    const uintptr_t al = (base + OFF_COMM_RAW + ((2u << 20) - 1)) & ~(uintptr_t)((2u << 20) - 1);
    p.comm_off = (unsigned long long)(al - base);
    (void)hipFuncSetAttribute((const void*)k0_prep, hipFuncAttributeMaxDynamicSharedMemorySize, 136000);
    (void)hipFuncSetAttribute((const void*)k_main, hipFuncAttributeMaxDynamicSharedMemorySize, 160000);
    k0_prep<<<256, 512, 136000, stream>>>(p);
    p.mode = 0; p.mode2 = 0;
    k_main<<<256, 512, 160000, stream>>>(p);
}
```

```cpp
#include <hip/hip_runtime.h>
#include <hip/hip_bf16.h>
#include <stdint.h>

typedef __attribute__((ext_vector_type(8))) __bf16 bf16x8;
typedef __attribute__((ext_vector_type(4))) short s16x4;
typedef __attribute__((ext_vector_type(2))) __bf16 bf16x2;
typedef __attribute__((ext_vector_type(2))) float f32x2;
typedef __attribute__((ext_vector_type(4))) float f32x4;
typedef __attribute__((ext_vector_type(4))) unsigned u32x4;
typedef __attribute__((ext_vector_type(2))) unsigned u32x2;
typedef unsigned short u16;
#define LAS __attribute__((address_space(3)))
#define DEVI __device__ __forceinline__

constexpr int DM = 448, NH = 16, NSEG = 64, SEGL = 32, OUTW = 896;
constexpr size_t FR = 1024;
constexpr size_t OFF_FLAG = 0;
constexpr size_t SZ_FLAG = 65536;
constexpr size_t OFF_AF = OFF_FLAG + SZ_FLAG;
constexpr size_t SZ_AF = (size_t)16 * 28 * 14 * FR;
constexpr size_t OFF_BF = OFF_AF + SZ_AF;
constexpr size_t OFF_XF = OFF_BF + SZ_AF;
constexpr size_t SZ_XF = (size_t)64 * 28 * FR;
constexpr size_t OFF_GX = OFF_XF + SZ_XF;
constexpr size_t SZ_GX = (size_t)64 * 16 * 28 * FR;
constexpr size_t OFF_UF = OFF_GX + SZ_GX;
constexpr size_t SZ_UF = (size_t)64 * 56 * 8 * FR;
constexpr size_t OFF_XM = OFF_UF + SZ_UF;
constexpr size_t SZ_XM = (size_t)64 * 16 * 7 * 1024;
constexpr size_t OFF_WC = OFF_XM + SZ_XM;
constexpr size_t SZ_WC = (size_t)64 * 56 * 8 * 256;
constexpr size_t OFF_PX = OFF_WC + SZ_WC;
constexpr size_t SZ_PX = (size_t)64 * 16 * 2 * FR;
constexpr size_t OFF_PM = OFF_PX + SZ_PX;
constexpr size_t SZ_PM = (size_t)64 * 8 * 2 * 512;
constexpr size_t OFF_BQ = OFF_PM + SZ_PM;
constexpr size_t SZ_BQ = (size_t)128 * 16 * FR;
constexpr size_t OFF_BW = OFF_BQ + SZ_BQ;
constexpr size_t SZ_BW = (size_t)3 * 16 * 2 * 14 * FR;
constexpr size_t OFF_BX = OFF_BW + SZ_BW;
constexpr size_t SZ_BX = (size_t)128 * 14 * FR;
constexpr size_t OFF_BKV = OFF_BX + SZ_BX;
constexpr size_t SZ_BKV = (size_t)16 * 16 * 4 * 1024;
constexpr size_t OFF_BM = OFF_BKV + SZ_BKV;
constexpr size_t SZ_BM = (size_t)16 * 28 * FR;
constexpr size_t OFF_BZ = OFF_BM + SZ_BM;
constexpr size_t SZ_BZ = 16 * 32 * 4;
constexpr size_t OFF_COMM_RAW = OFF_BZ + SZ_BZ;
constexpr size_t COMM_BYTES = (size_t)6 << 20;
constexpr unsigned POISON = 0xFFFFFFFFu;
constexpr int FLAG_CFG = 0;
constexpr int FLAG_RDY = 256;
constexpr int FLAG_B1 = 32;
constexpr int FLAG_B2 = 64;
constexpr int FLAG_PROG = 8192;
constexpr int FLAG_DONE = 12288;
constexpr int FLAG_T0 = 384;
constexpr int FLAG_GX = 2048;
constexpr int FLAG_U = 3072;
constexpr int FLAG_P = 4096;
constexpr int STATS_OFF = 7168;
constexpr int TS_OFF = 32768 + 448;

struct Params {
    const u16 *x, *mem0, *Wq, *Wk, *Wv, *Wo, *bx, *wqkv, *bwo;
    u16* out;
    char* ws;
    unsigned long long comm_off;
    unsigned long long mode;
    unsigned long long mode2;
};

DEVI unsigned pk2(float a, float b) { f32x2 v = {a, b}; bf16x2 r = __builtin_convertvector(v, bf16x2); return __builtin_bit_cast(unsigned, r); }
DEVI f32x4 mfma32(u32x4 a, u32x4 b, f32x4 c) { return __builtin_amdgcn_mfma_f32_16x16x32_bf16(__builtin_bit_cast(bf16x8, a), __builtin_bit_cast(bf16x8, b), c, 0, 0, 0); }
DEVI f32x4 mfma16(u32x2 a, u32x2 b, f32x4 c) { return __builtin_amdgcn_mfma_f32_16x16x16bf16_1k(__builtin_bit_cast(s16x4, a), __builtin_bit_cast(s16x4, b), c, 0, 0, 0); }
DEVI u32x4 pack8(f32x4 a, f32x4 b) { u32x4 r; r.x = pk2(a.x, a.y); r.y = pk2(a.z, a.w); r.z = pk2(b.x, b.y); r.w = pk2(b.z, b.w); return r; }
DEVI float fmx(float a, float b) { return __builtin_amdgcn_fmed3f(a, b, 3.4028235e38f); }
template <int CTRL> DEVI float dppf(float x) { return __uint_as_float((unsigned)__builtin_amdgcn_update_dpp(0, (int)__float_as_uint(x), CTRL, 0xf, 0xf, true)); }
constexpr int DPP_ROR8 = 0x128, DPP_ROR4 = 0x124, DPP_QP_1032 = 0xB1, DPP_QP_2301 = 0x4E;
DEVI void swap16(float x, float& a, float& b) { auto s = __builtin_amdgcn_permlane16_swap(__float_as_uint(x), __float_as_uint(x), false, false); a = __uint_as_float(s[0]); b = __uint_as_float(s[1]); }
DEVI void swap32(float x, float& a, float& b) { auto s = __builtin_amdgcn_permlane32_swap(__float_as_uint(x), __float_as_uint(x), false, false); a = __uint_as_float(s[0]); b = __uint_as_float(s[1]); }
DEVI void uswap16(unsigned x, unsigned& a, unsigned& b) { auto s = __builtin_amdgcn_permlane16_swap(x, x, false, false); a = s[0]; b = s[1]; }
DEVI void uswap32(unsigned x, unsigned& a, unsigned& b) { auto s = __builtin_amdgcn_permlane32_swap(x, x, false, false); a = s[0]; b = s[1]; }
template <int CTRL> DEVI unsigned dppu(unsigned x) { return (unsigned)__builtin_amdgcn_update_dpp(0, (int)x, CTRL, 0xf, 0xf, true); }
constexpr int DPP_SHL4 = 0x104, DPP_SHL8 = 0x108;
DEVI float bf_lo(unsigned v) { return __uint_as_float(v << 16); }
DEVI float bf_hi(unsigned v) { return __uint_as_float(v & 0xFFFF0000u); }
DEVI int kcol(int t, int q4) { return 64 * (t >> 1) + 16 * q4 + 8 * (t & 1); }
DEVI unsigned nopoison(unsigned v) { return v < 0xFFFFFFFEu ? v : 0xFFFFFFFEu; }
DEVI unsigned max4(u32x4 v) { unsigned a = v.x > v.y ? v.x : v.y, b = v.z > v.w ? v.z : v.w; return a > b ? a : b; }

typedef __amdgpu_buffer_rsrc_t rsrc_t;
DEVI rsrc_t mkrsrc(const void* p, unsigned bytes) { return __builtin_amdgcn_make_buffer_rsrc((void*)p, 0, bytes, 0x00020000); }
DEVI u32x4 bld4(rsrc_t r, unsigned v, unsigned s) { return __builtin_amdgcn_raw_buffer_load_b128(r, v, s, 16); }
DEVI u32x2 bld2(rsrc_t r, unsigned v, unsigned s) { return __builtin_amdgcn_raw_buffer_load_b64(r, v, s, 16); }
DEVI unsigned bld1(rsrc_t r, unsigned v, unsigned s) { return __builtin_amdgcn_raw_buffer_load_b32(r, v, s, 16); }
DEVI u32x4 bld4p(rsrc_t r, unsigned v, unsigned s) { return __builtin_amdgcn_raw_buffer_load_b128(r, v, s, 0); }
DEVI void bst4(rsrc_t r, u32x4 d, unsigned v, unsigned s) { __builtin_amdgcn_raw_buffer_store_b128(d, r, v, s, 16); }
DEVI void bst2(rsrc_t r, u32x2 d, unsigned v, unsigned s) { __builtin_amdgcn_raw_buffer_store_b64(d, r, v, s, 16); }
DEVI void bst1(rsrc_t r, unsigned d, unsigned v, unsigned s) { __builtin_amdgcn_raw_buffer_store_b32(d, r, v, s, 16); }
#define MEMFENCE() asm volatile("" ::: "memory")
#define WAITVM0() do { asm volatile("s_waitcnt vmcnt(0)" ::: "memory"); __builtin_amdgcn_s_waitcnt(0x0F70); } while (0)
DEVI void bar() { asm volatile("s_waitcnt lgkmcnt(0)\n\ts_barrier" ::: "memory"); }
constexpr unsigned OOB = 0xF0000000u;
DEVI u32x4 mkdesc(const void* p, unsigned bytes) {
    const unsigned long long a = (unsigned long long)p;
    u32x4 d; d.x = __builtin_amdgcn_readfirstlane((unsigned)a); d.y = __builtin_amdgcn_readfirstlane((unsigned)(a >> 32)) & 0xFFFFu; d.z = bytes; d.w = 0x00020000u; return d;
}
#define ALD4(dst, vo, ds, so) asm volatile("buffer_load_dwordx4 %0, %1, %2, %3 offen sc1" : "=v"(dst) : "v"(vo), "s"(ds), "s"(so) : "memory")
#define ALD4R(dst, vo, ds, so) asm volatile("buffer_load_dwordx4 %0, %1, %2, %3 offen sc1" : "+v"(dst) : "v"(vo), "s"(ds), "s"(so) : "memory")
#define ALD4PR(dst, vo, ds, so) asm volatile("buffer_load_dwordx4 %0, %1, %2, %3 offen" : "+v"(dst) : "v"(vo), "s"(ds), "s"(so) : "memory")
#define ALD1(dst, vo, ds, so) asm volatile("buffer_load_dword %0, %1, %2, %3 offen sc1" : "=v"(dst) : "v"(vo), "s"(ds), "s"(so) : "memory")
#define ALD1R(dst, vo, ds, so) asm volatile("buffer_load_dword %0, %1, %2, %3 offen sc1" : "+v"(dst) : "v"(vo), "s"(ds), "s"(so) : "memory")
#define AST4(dat, vo, ds, so) asm volatile("buffer_store_dwordx4 %0, %1, %2, %3 offen sc1\n\ts_nop 1" :: "v"(dat), "v"(vo), "s"(ds), "s"(so) : "memory")
#define AST2(dat, vo, ds, so) asm volatile("buffer_store_dwordx2 %0, %1, %2, %3 offen sc1\n\ts_nop 1" :: "v"(dat), "v"(vo), "s"(ds), "s"(so) : "memory")
#define AST1(dat, vo, ds, so) asm volatile("buffer_store_dword %0, %1, %2, %3 offen sc1" :: "v"(dat), "v"(vo), "s"(ds), "s"(so) : "memory")
#define AADD1(vo, ds) asm volatile("buffer_atomic_add %0, %1, %2, 0 offen" :: "v"(1u), "v"(vo), "s"(ds) : "memory")
#define SCHEDB() __builtin_amdgcn_sched_barrier(0)
#define PIN(x) asm volatile("" : "+v"(x))
#define AWAIT(n) asm volatile("s_waitcnt vmcnt(" #n ")" ::: "memory")

#define K0AUX 16
#define K0ST(v, off) __builtin_amdgcn_raw_buffer_store_b128(v, rs, off, 0, K0AUX)
__global__ void __launch_bounds__(512, 2) k0_prep(Params p) {
    extern __shared__ __attribute__((aligned(16))) char lds[];
    const rsrc_t rs = mkrsrc(p.ws, 0x10000000u);
    const int b = blockIdx.x, tid = threadIdx.x, lane = tid & 63, w = __builtin_amdgcn_readfirstlane(tid >> 6);
    const int l15 = lane & 15, q4 = lane >> 4;
    const int mat = b & 1, h = (b >> 1) & 15, sub = b >> 5, th = sub & 1, cg = sub >> 1;
    constexpr int K_SCR = 61440, K_XT = K_SCR + 7 * 8448;
    const int lr = lane >> 2, lp = lane & 3;
    u32x4 stg[7];
    if (mat == 0) {
#pragma unroll
        for (int i = 0; i < 7; ++i) {
            const int ct = 7 * cg + i;
            const int c = 64 * (ct >> 2) + 16 * (lr >> 2) + 4 * (ct & 3) + (lr & 3);
            stg[i] = *(const u32x4*)(p.Wq + ((size_t)c * 16 + h) * 256 + 32 * w + 8 * lp);
        }
    } else {
#pragma unroll
        for (int i = 0; i < 7; ++i) {
            const int n = tid + 512 * i, e = n / 14, ch = n % 14;
            stg[i] = *(const u32x4*)(p.Wo + ((size_t)h * 256 + e) * 448 + 112 * cg + 8 * ch);
        }
    }
    u32x4 rw[2][8];
    const int t = 7 * th + (w < 7 ? w : 0);
    {
        const u16* Wr = mat ? p.Wv : p.Wk;
#pragma unroll
        for (int bb = 0; bb < 2; ++bb) {
            const int d = 64 * (t >> 1) + 16 * (lr >> 2) + 8 * (t & 1) + 4 * bb + (lr & 3);
#pragma unroll
            for (int k = 0; k < 8; ++k) rw[bb][k] = *(const u32x4*)(Wr + ((size_t)d * 16 + h) * 256 + 32 * k + 8 * lp);
        }
    }
    const char* xsrc = (const char*)((b & 1) ? p.bx : p.x) + (size_t)(b >> 1) * 14336;
    const u32x4 xt0 = *(const u32x4*)(xsrc + tid * 16);
    const u32x4 xt1 = *(const u32x4*)(xsrc + 8192 + (tid < 384 ? tid : 0) * 16);
    const int fw = b * 8 + w;
    u16 ew[8];
    {
        const int f = (fw < 1344) ? fw : 0;
        const int tt = f % 14, tile = (f / 14) & 1, hh = (f / 28) & 15, t3 = f / 448;
        const int col = (t3 == 0) ? (8 * (l15 >> 2) + 4 * tile + (l15 & 3)) : (16 * tile + l15);
#pragma unroll
        for (int j = 0; j < 8; ++j) ew[j] = (col < 28) ? p.wqkv[(size_t)(kcol(tt, q4) + j) * 1344 + t3 * 448 + hh * 28 + col] : (u16)0;
    }
    if (b & 4) {
        const int q = (b >> 3) * 4 + (b & 3);
        const unsigned cb = (unsigned)p.comm_off + (unsigned)q * 32768u;
        const u32x4 pz = {POISON, POISON, POISON, POISON};
#pragma unroll
        for (int k = 0; k < 4; ++k) K0ST(pz, cb + k * 8192 + tid * 16);
        if (q == 0 && tid < 448) {
            const int beta = tid >> 3, i = tid & 7;
            const u32x4 m = *(const u32x4*)(p.mem0 + i * DM + 8 * beta);
            K0ST(m, cb + tid * 16);
            K0ST(m, cb + 8192 + tid * 16);
        }
    }
    if (b < 32 && tid < 128) K0ST(((u32x4){0, 0, 0, 0}), (unsigned)(OFF_FLAG + (size_t)b * 2048 + tid * 16));
    if (mat == 0) {
#pragma unroll
        for (int i = 0; i < 7; ++i) *(u32x4*)(lds + (i * 8 + w) * 1024 + (lp * 16 + lr) * 16) = stg[i];
    } else {
#pragma unroll
        for (int i = 0; i < 7; ++i) { const int n = tid + 512 * i, e = n / 14, ch = n % 14; *(u32x4*)(lds + e * 240 + ch * 16) = stg[i]; }
    }
    { const int o0 = tid * 16, o1 = 8192 + tid * 16;
      *(u32x4*)(lds + K_XT + (o0 / 896) * 912 + (o0 % 896)) = xt0;
      if (tid < 384) *(u32x4*)(lds + K_XT + (o1 / 896) * 912 + (o1 % 896)) = xt1; }
    if (fw < 1344) {
        const u32x4 o = {ew[0] | ((unsigned)ew[1] << 16), ew[2] | ((unsigned)ew[3] << 16), ew[4] | ((unsigned)ew[5] << 16), ew[6] | ((unsigned)ew[7] << 16)};
        K0ST(o, (unsigned)(OFF_BW + (size_t)fw * FR + lane * 16));
    }
    if (w < 7) {
        char* scr = lds + K_SCR + w * 8448;
#pragma unroll
        for (int bb = 0; bb < 2; ++bb) {
#pragma unroll
            for (int k = 0; k < 8; ++k) *(u32x4*)(scr + lr * 528 + 64 * k + 16 * lp) = rw[bb][k];
            asm volatile("s_waitcnt lgkmcnt(0)" ::: "memory");
#pragma unroll
            for (int k = 0; k < 8; ++k) rw[bb][k] = *(const u32x4*)(scr + l15 * 528 + 64 * k + 16 * q4);
            asm volatile("s_waitcnt lgkmcnt(0)" ::: "memory");
        }
    }
    __syncthreads();
#pragma unroll
    for (int j = 0; j < 2; ++j) {
        const int tt = w + 8 * j;
        if (tt < 14) {
            const u32x4 v = *(const u32x4*)(lds + K_XT + l15 * 912 + kcol(tt, q4) * 2);
            K0ST(v, (unsigned)(((b & 1) ? OFF_BX : OFF_XF) + ((size_t)(b >> 1) * 14 + tt) * FR + lane * 16));
        }
    }
    if (w < 7) {
        auto ldcf = [&](int i, u32x4 (&cf)[8]) {
#pragma unroll
            for (int k = 0; k < 8; ++k) {
                if (mat == 0) cf[k] = *(const u32x4*)(lds + (i * 8 + k) * 1024 + lane * 16);
                else {
                    const int qq = l15 >> 2, pp = l15 & 3;
                    const int row0 = 32 * k + 8 * q4 + qq;
                    typedef __attribute__((ext_vector_type(4))) short v4s;
                    v4s lo = __builtin_amdgcn_ds_read_tr16_b64_v4i16((LAS v4s*)(lds + row0 * 240 + (16 * i + 4 * pp) * 2));
                    v4s hi = __builtin_amdgcn_ds_read_tr16_b64_v4i16((LAS v4s*)(lds + (row0 + 4) * 240 + (16 * i + 4 * pp) * 2));
                    u32x2 l2 = __builtin_bit_cast(u32x2, lo), h2 = __builtin_bit_cast(u32x2, hi);
                    cf[k] = (u32x4){l2.x, l2.y, h2.x, h2.y};
                }
            }
        };
        u32x4 cfa[8], cfb[8];
        auto tile = [&](int i, const u32x4 (&cf)[8]) {
            f32x4 a0 = {0, 0, 0, 0}, a1 = {0, 0, 0, 0};
#pragma unroll
            for (int k = 0; k < 8; ++k) { a0 = mfma32(rw[0][k], cf[k], a0); a1 = mfma32(rw[1][k], cf[k], a1); }
            const u32x4 o = pack8(a0, a1);
            if (mat == 0) {
                const int ct = 7 * cg + i;
                K0ST(o, (unsigned)(OFF_AF + ((size_t)(h * 28 + ct) * 14 + t) * FR + lane * 16));
            } else {
                const int beta = 14 * cg + 2 * i + (l15 >> 3);
                K0ST(o, (unsigned)(OFF_BF + ((size_t)((h >> 1) * 56 + beta) * 14 + t) * FR + (16 * q4 + 8 * (h & 1) + (l15 & 7)) * 16));
            }
        };
        ldcf(0, cfa);
#pragma unroll
        for (int i = 0; i < 7; i += 2) {
            if (i + 1 < 7) ldcf(i + 1, cfb);
            tile(i, cfa);
            if (i + 2 < 7) ldcf(i + 2, cfa);
            if (i + 1 < 7) tile(i + 1, cfb);
        }
    }
}

constexpr int SPIN_MAX = 200000;
DEVI unsigned* flagp(const Params& p, int idx) { return (unsigned*)(p.ws + OFF_FLAG) + idx; }
DEVI void signal(const Params& p, int idx) { __hip_atomic_fetch_add(flagp(p, idx), 1u, __ATOMIC_RELAXED, __HIP_MEMORY_SCOPE_AGENT); }
DEVI unsigned ldflag(const Params& p, int idx) { return __hip_atomic_load(flagp(p, idx), __ATOMIC_RELAXED, __HIP_MEMORY_SCOPE_AGENT); }
DEVI void waitflag(const Params& p, int idx, unsigned target, bool& dead, int sleep = 4) {
    int k = dead ? SPIN_MAX : 0;
    _Pragma("nounroll") for (; k < SPIN_MAX; ++k) {
        const unsigned v = __builtin_amdgcn_readfirstlane(ldflag(p, idx));
        if (v >= target) break;
        __builtin_amdgcn_s_sleep(4);
    }
    if (k >= SPIN_MAX) dead = true;
    MEMFENCE();
}

DEVI void waitall(const Params& p, int idx, int n, bool& dead) {
    int k = dead ? SPIN_MAX : 0;
    const int _l = (threadIdx.x & 63) & (n - 1);
    _Pragma("nounroll") for (; k < SPIN_MAX; ++k) {
        const unsigned v = ldflag(p, idx + _l);
        if (!__any(v == 0u)) break;
        __builtin_amdgcn_s_sleep(3);
    }
    if (k >= SPIN_MAX) dead = true;
    MEMFENCE();
}

DEVI void gemm_worker(const Params& p, int u, char* lds) {
    const int tid = threadIdx.x, lane = tid & 63, w = __builtin_amdgcn_readfirstlane(tid >> 6);
    const u32x4 ds = mkdesc(p.ws, 0x10000000u);
    const int isU = u >> 6, par = (u >> 2) & 1, v = ((((u & 3) | (((u & 63) >> 3) << 2))) << 1) | par;
    constexpr unsigned SEG_STRIDE = 16 * 28 * 1024;
    const int flag_idx = isU ? FLAG_U + ((v >> 1) & 3) : FLAG_GX + (v >> 2);
    u32x4 x0[4];
#pragma unroll
    for (int j = 0; j < 4; ++j) { const int f = (w + 8 * j < 28) ? w + 8 * j : w; x0[j] = *(const u32x4*)(p.ws + OFF_XF + ((size_t)par * 28 + f) * FR + lane * 16); }
    if (w < 7) {
        u32x4 wt[2][14];
        unsigned vo_out[2];
        const char* base;
        if (!isU) {
            const int h = v >> 2, tp = 7 * ((v >> 1) & 1) + w; base = p.ws + OFF_AF + (size_t)(h * 28 + 2 * tp) * 14 * FR;
            vo_out[0] = (unsigned)(OFF_GX + ((size_t)(h * 2 + 0) * 14 + tp) * FR + lane * 16);
            vo_out[1] = (unsigned)(OFF_GX + ((size_t)(h * 2 + 1) * 14 + tp) * FR + lane * 16);
        } else {
            const int hp = v >> 3, b0 = 14 * ((v >> 1) & 3) + 2 * w; base = p.ws + OFF_BF + (size_t)(hp * 56 + b0) * 14 * FR;
            vo_out[0] = (unsigned)(OFF_UF + ((size_t)(b0 + 0) * 8 + hp) * FR + lane * 16);
            vo_out[1] = (unsigned)(OFF_UF + ((size_t)(b0 + 1) * 8 + hp) * FR + lane * 16);
        }
#pragma unroll
        for (int a = 0; a < 2; ++a)
#pragma unroll
            for (int t = 0; t < 14; ++t) wt[a][t] = *(const u32x4*)(base + (size_t)(a * 14 + t) * FR + lane * 16);
        WAITVM0();
#pragma unroll
        for (int j = 0; j < 4; ++j) { const int f = w + 8 * j; if (f < 28) *(u32x4*)(lds + f * 1024 + lane * 16) = x0[j]; }
        bar();
        int buf = 0;
        _Pragma("nounroll") for (int s = par; s < NSEG; s += 2) {
            f32x4 acc[2][2];
#pragma unroll
            for (int a = 0; a < 2; ++a)
#pragma unroll
                for (int kt = 0; kt < 2; ++kt) acc[a][kt] = (f32x4){0, 0, 0, 0};
            const char* xb = lds + buf * 28672 + lane * 16;
            u32x4 xf[28];
#pragma unroll
            for (int k = 0; k < 14; ++k) xf[k] = *(const u32x4*)(xb + ((k & 1) * 14 + (k >> 1)) * 1024);
            SCHEDB();
            if (!isU) {
#pragma unroll
                for (int k = 0; k < 28; ++k) {
                    if (k + 14 < 28) xf[k + 14] = *(const u32x4*)(xb + (((k + 14) & 1) * 14 + ((k + 14) >> 1)) * 1024);
                    acc[0][k & 1] = mfma32(wt[0][k >> 1], xf[k], acc[0][k & 1]); acc[1][k & 1] = mfma32(wt[1][k >> 1], xf[k], acc[1][k & 1]);
                    SCHEDB();
                }
            } else {
#pragma unroll
                for (int k = 0; k < 28; ++k) {
                    if (k + 14 < 28) xf[k + 14] = *(const u32x4*)(xb + (((k + 14) & 1) * 14 + ((k + 14) >> 1)) * 1024);
                    acc[0][k & 1] = mfma32(xf[k], wt[0][k >> 1], acc[0][k & 1]); acc[1][k & 1] = mfma32(xf[k], wt[1][k >> 1], acc[1][k & 1]);
                    SCHEDB();
                }
            }
            u32x4 o0, o1;
            if (!isU) { o0 = pack8(acc[0][0], acc[1][0]); o1 = pack8(acc[0][1], acc[1][1]); }
            else { o0 = pack8(acc[0][0], acc[0][1]); o1 = pack8(acc[1][0], acc[1][1]); }
            const unsigned so = (unsigned)s * SEG_STRIDE;
            AST4(o0, vo_out[0], ds, so);
            AST4(o1, vo_out[1], ds, so);
            if (s < 2) AWAIT(0); else AWAIT(2);
            bar();
            buf ^= 1;
        }
        AWAIT(0);
        bar();
    } else {
        u32x4 nx[28];
#pragma unroll
        for (int f = 0; f < 28; ++f) nx[f] = (u32x4){0, 0, 0, 0};
        const unsigned vo_x = (unsigned)(OFF_XF + lane * 16);
        auto issue = [&](int sg) {
#pragma unroll
            for (int f = 0; f < 28; ++f) ALD4PR(nx[f], vo_x + (unsigned)f * 1024u, ds, (unsigned)sg * 28672u);
        };
        WAITVM0();
#pragma unroll
        for (int j = 0; j < 4; ++j) { const int f = w + 8 * j; if (f < 28) *(u32x4*)(lds + f * 1024 + lane * 16) = x0[j]; }
        issue(par + 2);
        bar();
        int buf = 0;
        _Pragma("nounroll") for (int s = par; s < NSEG; s += 2) {
            AWAIT(0);
#pragma unroll
            for (int f = 0; f < 28; ++f) asm volatile("" : "+v"(nx[f]));
            SCHEDB();
#pragma unroll
            for (int f = 0; f < 28; ++f) *(u32x4*)(lds + (buf ^ 1) * 28672 + f * 1024 + lane * 16) = nx[f];
            SCHEDB();
            const int sn = (s + 4 < NSEG) ? s + 4 : s;
            issue(__builtin_amdgcn_readfirstlane(sn));
            bar();
            if (lane == 0) {
                if (s < 2) signal(p, flag_idx + s * 16);
                else if (s >= 4) signal(p, flag_idx + (s - 2) * 16);
            }
            buf ^= 1;
        }
        AWAIT(0);
        bar();
        if (lane == 0) signal(p, flag_idx + (NSEG - 2 + par) * 16);
    }
}

constexpr float SC2 = 0.0625f * 1.4426950408889634f;
constexpr int L_R = 0;
constexpr int L_RED1 = 7168;
constexpr int L_RED2 = L_RED1 + 4352;
constexpr int L_REDX = L_RED2 + 2432;
constexpr int L_XX = L_REDX + 8192;
constexpr int L_CFG = L_XX + 73728;

DEVI unsigned chunk_off(const Params& p, unsigned cfg, int s, int j) {
    const int pg = s >> 5;
    return (unsigned)p.comm_off + (unsigned)pg * (2u << 20) + (unsigned)(2 * (4 * (s & 31) + j) + ((cfg >> pg) & 1)) * 8192u;
}

DEVI unsigned ts_off(const Params& p, unsigned cfg, int s) { return (unsigned)p.comm_off + (4u << 20) + (unsigned)(2 * (s >> 6) + ((cfg >> 2) & 1)) * 8192u + (unsigned)(s & 63) * 128u; }
DEVI unsigned rt32() { return (unsigned)__builtin_amdgcn_s_memrealtime(); }
DEVI void wait_until(unsigned target) {
    const unsigned now = rt32();
    const int d = (int)(target - now);
    if (d <= 0) return;
    if (d > 2000) target = now + 2000u;
    _Pragma("nounroll") while ((int)(rt32() - target) < 0) { }
}
#define C2T 110
#define O1T 98
#define C2BOOT 260
#define O1BOOT 200
#define C2H 86
#define GDELAY 100
#define WPOS 0
#define SDLY 3
#define TSOFF 10
#define XHD 6
#define BULKD 4
#define CUP 40
#define XOFFT 0
#define DEXTRA 0
#define STARTD 60
DEVI void probe_cfg(const Params& p, rsrc_t rs, int idx) {
    const int pg = idx >> 1, par = idx & 1;
    unsigned best = 1u << 20;
    for (int k = 0; k < 4; ++k) {
        const unsigned off = (unsigned)p.comm_off + pg * (2u << 20) + (unsigned)((pg ? 2 * (4 * 31 + 1) : 0) + par) * 8192u + 512u * k;
        const unsigned long long c0 = __builtin_amdgcn_s_memtime();
        unsigned vv = bld1(rs, off, 0);
        asm volatile("s_waitcnt vmcnt(0)" : "+v"(vv) :: "memory");
        const unsigned long long c1 = __builtin_amdgcn_s_memtime();
        const unsigned dt = (unsigned)(c1 - c0);
        if (k > 0 && dt < best) best = dt;
    }
    if ((threadIdx.x & 63) == 0) bst1(rs, best + 1u, OFF_FLAG + (FLAG_CFG + idx) * 4, 0);
}
DEVI unsigned get_cfg(const Params& p, rsrc_t rs, char* lds) {
    const int tid = threadIdx.x, lane = tid & 63, w = __builtin_amdgcn_readfirstlane(tid >> 6);
    if (w == 7) {
        unsigned v = 0;
        for (int it = 0; it < SPIN_MAX; ++it) { v = bld1(rs, OFF_FLAG + (FLAG_CFG + (lane & 3)) * 4, 0); MEMFENCE(); if (!__any(v == 0u)) break; __builtin_amdgcn_s_sleep(2); }
        const unsigned l0 = __builtin_amdgcn_readlane(v, 0), l1 = __builtin_amdgcn_readlane(v, 1), l2 = __builtin_amdgcn_readlane(v, 2), l3 = __builtin_amdgcn_readlane(v, 3);
        unsigned cfg = (l1 < l0 ? 1u : 0u) | (l3 < l2 ? 2u : 0u);
        if (lane == 0) *(unsigned*)(lds + L_CFG) = cfg;
    }
    __syncthreads();
    return __builtin_amdgcn_readfirstlane(*(unsigned*)(lds + L_CFG));
}
DEVI unsigned start_sync(const Params& p, u32x4 ds, int r, char* lds) {
    const int tid = threadIdx.x, lane = tid & 63, w = __builtin_amdgcn_readfirstlane(tid >> 6);
    bar();
    if (w == 3) {
        const unsigned base = rt32();
        if (lane == 0) AST1(base | 1u, (unsigned)(OFF_FLAG + (FLAG_RDY + r) * 4), ds, 0u);
        unsigned v0 = 0, v1 = 0;
        const unsigned vo = (unsigned)(OFF_FLAG + (FLAG_RDY + lane) * 4);
        for (int it = 0; it < SPIN_MAX; ++it) {
            ALD1R(v0, vo, ds, 0u); ALD1R(v1, vo, ds, 256u);
            asm volatile("s_waitcnt vmcnt(0)" : "+v"(v0), "+v"(v1) :: "memory");
            if (!__any(v0 == 0u || v1 == 0u)) break;
            __builtin_amdgcn_s_sleep(2);
        }
        int d = max((int)(v0 - base), (int)(v1 - base));
        d = max(d, (int)dppu<DPP_QP_1032>((unsigned)d));
        d = max(d, (int)dppu<DPP_QP_2301>((unsigned)d));
        d = max(d, (int)dppu<DPP_ROR4>((unsigned)d));
        d = max(d, (int)dppu<DPP_ROR8>((unsigned)d));
        { unsigned a, b2; uswap16((unsigned)d, a, b2); d = max((int)a, (int)b2); uswap32((unsigned)d, a, b2); d = max((int)a, (int)b2); }
        if (lane == 0) *(unsigned*)(lds + L_CFG + 4) = base + (unsigned)d + STARTD;
        if (r == 0 && lane == 0) AST1((base + (unsigned)d + STARTD) | 1u, (unsigned)(OFF_FLAG + FLAG_T0 * 4), ds, 0u);
    }
    bar();
    return __builtin_amdgcn_readfirstlane(*(unsigned*)(lds + L_CFG + 4));
}

#define D1 0
#define D2 0
template <int DELAY> DEVI void pause() { if (DELAY > 0) __builtin_amdgcn_s_sleep(DELAY); }

#define EV(k)
#define STAMP(k)

constexpr int L_TS = L_CFG + 64;
constexpr int L_RF = L_CFG + 128;
constexpr int L_PU = L_CFG + 256;
constexpr int L_PX = L_PU + 8192;
constexpr int L_GX = L_CFG + 256;
#define WSPLIT 5
#define MINH 30
#define MINM 30
DEVI unsigned tmax(unsigned a, unsigned b) { return ((int)(a - b) > 0) ? a : b; }

DEVI void await_flag(u32x4 ds, unsigned vo, unsigned target, bool& dead) {
    int spins = 0; const int lim = dead ? 1 : SPIN_MAX; bool bad;
    _Pragma("nounroll") do {
        unsigned v; ALD1(v, vo, ds, 0u);
        asm volatile("s_waitcnt vmcnt(0)" : "+v"(v) :: "memory");
        bad = (unsigned)__builtin_amdgcn_readfirstlane(v) < target;
        if (bad) __builtin_amdgcn_s_sleep(2);
    } while (bad && ++spins < lim);
    if (bad) dead = true;
}
DEVI void await_all(u32x4 ds, unsigned vo, bool& dead) {
    int spins = 0; const int lim = dead ? 1 : SPIN_MAX; bool bad;
    _Pragma("nounroll") do {
        unsigned v; ALD1(v, vo, ds, 0u);
        asm volatile("s_waitcnt vmcnt(0)" : "+v"(v) :: "memory");
        bad = __any(v == 0u);
        if (bad) __builtin_amdgcn_s_sleep(2);
    } while (bad && ++spins < lim);
    if (bad) dead = true;
}
#define LAT0()
#define LAT1(k)
#define WT(k)
#define TA0(v)
#define TA1(v, k, cond)

#define PTA()
#define PTB(k)
template <bool V> struct BoolC { static constexpr bool value = V; };

DEVI void chain_gw(const Params& p, int r, char* lds, u32x4 ds, unsigned t_entry) {
    const rsrc_t rs = mkrsrc(p.ws, 0x10000000u);
    const int tid = threadIdx.x, lane = tid & 63, w = __builtin_amdgcn_readfirstlane(tid >> 6);
    const int col = lane & 15, q4 = lane >> 4;
    const int h = r / 7, c7 = r % 7, beta = r >> 1, rh = r & 1;
    u32x4 aw[2][2][4];
    const int nq = (w < 3) ? 2 : 1;
    const int qf0 = (c7 + 2 * w) % 7, qf1 = (c7 + 2 * w + nq - 1) % 7;
#pragma unroll
    for (int qi = 0; qi < 2; ++qi)
#pragma unroll
        for (int e = 0; e < 2; ++e)
#pragma unroll
            for (int tau = 0; tau < 4; ++tau) {
                const int q = qi ? qf1 : qf0;
                aw[qi][e][tau] = *(const u32x4*)(p.ws + OFF_AF + ((size_t)(h * 28 + 4 * c7 + tau) * 14 + 2 * q + e) * FR + lane * 16);
                if (qi == 1 && nq == 1) aw[qi][e][tau] = (u32x4){0, 0, 0, 0};
            }
    unsigned mE = (col < 8) ? 0xFFFFFFFFu : 0u, nE = ~mE;
    asm volatile("" : "+v"(mE), "+v"(nE));
    const unsigned cfg = get_cfg(p, rs, lds);
    WAITVM0();
    bool dead = false;
    const unsigned vo_m0 = (unsigned)(qf0 * 1024 + lane * 16), vo_m1 = (unsigned)(qf1 * 1024 + lane * 16);
    const unsigned vo_pp = (lane >= 32) ? (unsigned)(TS_OFF + (lane & 1) * 4) : (unsigned)((((lane >> 4) & 1) ? 16384 : 32768) + h * 512 + c7 * 64 + (lane & 15) * 4);
    const int npub = (r == 0) ? 34 : 32;
    const unsigned vo_mp = (unsigned)(beta * 128 + rh * 64 + (lane & 15) * 4);
    const unsigned vo_xm = (unsigned)(OFF_XM + ((size_t)h * 7 + c7) * 1024 + lane * 16);
    const unsigned vo_ts = (unsigned)(TS_OFF + (lane & 1) * 4);
    u32x4 R0 = {0,0,0,0}, R1 = {0,0,0,0};
    unsigned ts1 = 0;
    unsigned tm = 0, th = 0;
    volatile LAS unsigned* ldist = (volatile LAS unsigned*)((LAS char*)lds + L_RF + 4);
    bool dh_prev = false;
    LAS char* lbase = (LAS char*)lds;
    LAS char* lr0 = lbase + L_R + vo_m0; LAS char* lr1 = lbase + L_R + vo_m1; LAS char* lrc = lbase + L_R + c7 * 1024 + lane * 16;
    volatile LAS unsigned* rflag = (volatile LAS unsigned*)(lbase + L_RF + (lane & 0));
    volatile LAS unsigned* rflagw = (volatile LAS unsigned*)(lbase + (w == 0 ? L_RF : L_RF + 16 + 4 * (w & 3)) + (lane & 0));
    asm volatile("" : "+v"(lr0), "+v"(lr1), "+v"(lrc), "+v"(rflag), "+v"(rflagw));
    if (tid == 0) { *rflag = 0u; ldist[0] = 0u; ldist[1] = 0u; }
    tm = start_sync(p, ds, r, lds);
    __builtin_amdgcn_s_setprio(2);
    th = tm + O1T;
    auto step = [&](const int s, auto LASTC) {
        constexpr bool LAST = decltype(LASTC)::value;
        const unsigned oM = (unsigned)__builtin_amdgcn_readfirstlane((int)chunk_off(p, cfg, s, 0)), oMn = (unsigned)__builtin_amdgcn_readfirstlane((int)chunk_off(p, cfg, s + 1, 0));
        u32x4 gE, gO;
        unsigned sv1 = (unsigned)(s + 1); PIN(sv1);
        wait_until(tm);
        if (w == 0) EV(4);
        f32x4 acc[4];
        u32x4 Re1, Rc;
        unsigned mx, fl;
        auto ghalf1 = [&]() {
            ALD4R(R0, vo_m0, ds, oM);
            ALD4R(R1, vo_m1, ds, oM);
            asm volatile("s_waitcnt vmcnt(1)" : "+v"(R0) :: "memory");
            PTB(10);
            SCHEDB();
            u32x4 Re = R0 & mE;
            PIN(Re);
            SCHEDB();
            acc[0] = mfma32(aw[0][0][0], Re, (f32x4){0, 0, 0, 0});
            *(volatile LAS u32x4*)(lr0) = R0;
            SCHEDB();
            u32x4 Ro;
            acc[1] = mfma32(aw[0][0][1], Re, (f32x4){0, 0, 0, 0});
            Ro.x = R0.x & nE; Ro.y = R0.y & nE;
            SCHEDB();
            acc[2] = mfma32(aw[0][0][2], Re, (f32x4){0, 0, 0, 0});
            Ro.z = R0.z & nE; Ro.w = R0.w & nE;
            PIN(Ro);
            SCHEDB();
            acc[3] = mfma32(aw[0][0][3], Re, (f32x4){0, 0, 0, 0});
            mx = max(max(R0.x, R0.y), R0.z); mx = max(mx, R0.w);
            PIN(mx);
            SCHEDB();
            acc[0] = mfma32(aw[0][1][0], Ro, acc[0]);
            if (__builtin_expect(!__any(mx == POISON), 1)) *rflagw = sv1;
            SCHEDB();
            asm volatile("s_waitcnt vmcnt(0)" : "+v"(R1) :: "memory");
            PTB(11);
            SCHEDB();
            *(volatile LAS u32x4*)(lr1) = R1;
            SCHEDB();
            acc[1] = mfma32(aw[0][1][1], Ro, acc[1]);
            mx = max(max(mx, R1.x), R1.y); mx = max(max(mx, R1.z), R1.w);
            PIN(mx);
            SCHEDB();
            acc[2] = mfma32(aw[0][1][2], Ro, acc[2]);
            Re1.x = R1.x & mE; Re1.y = R1.y & mE;
            fl = *rflag;
            Rc = *(volatile LAS u32x4*)(lrc);
            SCHEDB();
            acc[3] = mfma32(aw[0][1][3], Ro, acc[3]);
            Re1.z = R1.z & mE; Re1.w = R1.w & mE;
            PIN(Re1);
            SCHEDB();
        };
        ghalf1();
        if (__builtin_expect(__any(mx == POISON), 0)) {
            int spins = 0; const int lim = dead ? 1 : SPIN_MAX; bool bad;
            ldist[0] = sv1;
            _Pragma("nounroll") do { ghalf1(); bad = __any(mx == POISON); } while (bad && ++spins < lim);
            if (bad) dead = true;
        }
        if (w == 0) EV(1);
        SCHEDB();
        u32x4 Rce, Rco, Ro1;
        acc[0] = mfma32(aw[1][0][0], Re1, acc[0]);
        Ro1.x = R1.x & nE; Ro1.y = R1.y & nE;
        SCHEDB();
        acc[1] = mfma32(aw[1][0][1], Re1, acc[1]);
        Ro1.z = R1.z & nE; Ro1.w = R1.w & nE;
        PIN(Ro1);
        SCHEDB();
        acc[2] = mfma32(aw[1][0][2], Re1, acc[2]);
        SCHEDB();
        acc[3] = mfma32(aw[1][0][3], Re1, acc[3]);
        SCHEDB();
        if (__builtin_expect((unsigned)__builtin_amdgcn_readfirstlane(fl) != (unsigned)(s + 1), 0)) {
            int g = 0;
            _Pragma("nounroll") do { fl = *rflag; Rc = *(volatile LAS u32x4*)(lrc); }
            while ((unsigned)__builtin_amdgcn_readfirstlane(fl) != (unsigned)(s + 1) && ++g < (1 << 22));
        }
        SCHEDB();
        acc[0] = mfma32(aw[1][1][0], Ro1, acc[0]);
        Rce.x = Rc.x & mE; Rce.y = Rc.y & mE;
        SCHEDB();
        acc[1] = mfma32(aw[1][1][1], Ro1, acc[1]);
        Rce.z = Rc.z & mE; Rce.w = Rc.w & mE;
        PIN(Rce);
        SCHEDB();
        acc[2] = mfma32(aw[1][1][2], Ro1, acc[2]);
        Rco.x = dppu<DPP_ROR8>(Rc.x) & mE; Rco.y = dppu<DPP_ROR8>(Rc.y) & mE;
        SCHEDB();
        acc[3] = mfma32(aw[1][1][3], Ro1, acc[3]);
        Rco.z = dppu<DPP_ROR8>(Rc.z) & mE; Rco.w = dppu<DPP_ROR8>(Rc.w) & mE;
        PIN(Rco);
        SCHEDB();
        gE = pack8(acc[0], acc[1]);
        f32x4 d2 = mfma32(gE, Rce, (f32x4){0, 0, 0, 0});
        gO = pack8(acc[2], acc[3]);
        d2 = mfma32(gO, Rco, d2);
        if (col < 8) {
            float* red = (float*)(lds + L_RED1) + (col * 4 + 2 * (q4 & 1)) * 20 + w * 4 + (q4 >> 1) * 2;
            *(f32x2*)(red) = (f32x2){d2[0], d2[1]};
            *(f32x2*)(red + 20) = (f32x2){d2[2], d2[3]};
        }
        unsigned tpre = 0;
        if (!LAST && r == 0 && w == 0) tpre = rt32();
        PTB(12);
        bar();
        PTB(13);
        if (!LAST && w == 0) {
            const float* red = (const float*)(lds + L_RED1) + (lane & 31) * 20;
            const f32x4 a = *(const f32x4*)(red), b2 = *(const f32x4*)(red + 4), c = *(const f32x4*)(red + 8), d = *(const f32x4*)(red + 12);
            const f32x4 t4 = (a + b2) + (c + d);
            unsigned o = nopoison(pk2(t4.x + t4.z, t4.y + t4.w));
            if (r == 0) {
                const bool dist0 = dh_prev || (unsigned)__builtin_amdgcn_readfirstlane(ldist[0]) == (unsigned)(s + 1);
                const unsigned tv = nopoison(tmax(th, tpre + 3u + MINH) + (unsigned)(C2H + (dist0 ? CUP : 0)) + ((lane & 1) ? (unsigned)O1T : 0u));
                o = (lane >= 32) ? tv : o;
            }
            if (lane < npub) AST1(o, vo_pp, ds, oM);
            PTB(14);
            EV(2);
            SCHEDB();
        }
        unsigned tb1 = 0, the = 0;
        if (!LAST && w == 1) { tb1 = rt32(); the = tmax(th, tb1 + MINH); }
#pragma unroll
        for (int it = 0; it < 2; ++it) {
            const char* pxb = lds + L_PX + lane * 16;
            const u32x4 x0 = *(const u32x4*)(pxb + (it * 2 + 0) * 1024), x1 = *(const u32x4*)(pxb + (it * 2 + 1) * 1024);
            f32x4 dx = mfma32(gE, x0, (f32x4){0, 0, 0, 0});
            dx = mfma32(gO, x1, dx);
            *(f32x4*)(lds + L_REDX + (2 * w + (q4 >> 1)) * 1024 + ((it * 2 + (q4 & 1)) * 16 + col) * 16) = dx;
        }
        if (!LAST && w == 1) {
            wait_until(the);
            ALD1R(ts1, vo_ts, ds, oM);
            asm volatile("s_waitcnt vmcnt(0)" : "+v"(ts1) :: "memory");
            if (__builtin_expect(__any(ts1 == POISON), 0)) {
                int spins = 0; const int lim = dead ? 1 : SPIN_MAX; bool bad;
                ldist[1] = sv1;
                _Pragma("nounroll") do {
                    ALD1R(ts1, vo_ts, ds, oM);
                    asm volatile("s_waitcnt vmcnt(0)" : "+v"(ts1) :: "memory");
                    bad = __any(ts1 == POISON);
                } while (bad && ++spins < lim);
                if (bad) dead = true;
            }
            if (lane < 2) *(unsigned*)(lds + L_TS + lane * 4) = ts1;
        }
        bar();
        if (!LAST && w == 3) {
            const float* red = (const float*)(lds + L_RED2) + (lane & 15) * 36 + ((lane >> 4) & 1) * 16;
            const f32x4 a = *(const f32x4*)(red), b2 = *(const f32x4*)(red + 4), c = *(const f32x4*)(red + 8), d = *(const f32x4*)(red + 12);
            const f32x4 t4 = (a + b2) + (c + d);
            float v0 = t4.x + t4.z, v1 = t4.y + t4.w;
            { float x0, x1; swap16(v0, x0, x1); v0 = x0 + x1; swap16(v1, x0, x1); v1 = x0 + x1; }
            const unsigned o = nopoison(pk2(v0, v1));
            if (lane < 16) AST1(o, vo_mp, ds, oMn);
            EV(0);
            SCHEDB();
        }
        const unsigned tb2 = rt32() - (w == 3 ? 9u : 0u);
        if (w == 1) EV(5);
        if (w == 2) {
            f32x4 t4 = *(const f32x4*)(lds + L_REDX + lane * 16);
#pragma unroll
            for (int pp = 1; pp < 8; ++pp) t4 += *(const f32x4*)(lds + L_REDX + pp * 1024 + lane * 16);
            const u32x4 xm = __builtin_bit_cast(u32x4, t4);
            AST4(xm, vo_xm, ds, (unsigned)s * (16u * 7u * 1024u));
        }
        if (!LAST) {
          const u32x2 t2 = *(const u32x2*)(lds + L_TS);
          dh_prev = (unsigned)__builtin_amdgcn_readfirstlane(ldist[1]) == (unsigned)(s + 1);
          tm = tmax((unsigned)__builtin_amdgcn_readfirstlane(t2.x), tb2 + MINM); th = (unsigned)__builtin_amdgcn_readfirstlane(t2.y);
          if (r == 0 && w == 1 && s >= 1) {
              __builtin_amdgcn_s_sleep(5);
              if (lane == 0) AST1(1u, (unsigned)(OFF_FLAG + FLAG_PROG * 4), ds, (unsigned)(s - 1) * 128u);
          }
        }
    };
    _Pragma("nounroll") for (int s = 0; s < NSEG - 1; ++s) step(s, BoolC<false>{});
    step(NSEG - 1, BoolC<true>{});
    AWAIT(0);
    bar();
    if (w == 2 && lane == 0) AST1(1u, (unsigned)(OFF_FLAG + (FLAG_DONE + r) * 4), ds, 0u);
}

DEVI void chain_bw(const Params& p, int r, char* lds, u32x4 ds) {
    const rsrc_t rs = mkrsrc(p.ws, 0x10000000u);
    const int tid = threadIdx.x, lane = tid & 63, w = __builtin_amdgcn_readfirstlane(tid >> 6), hw = w & 3;
    const int col = lane & 15, q4 = lane >> 4;
    const int c7 = r % 7, beta = r >> 1, rh = r & 1;
    u32x4 bw[2][14];
#pragma unroll
    for (int a = 0; a < 2; ++a)
#pragma unroll
        for (int t = 0; t < 14; ++t) bw[a][t] = *(const u32x4*)(p.ws + OFF_BF + ((size_t)((2 * hw + a) * 56 + beta) * 14 + t) * FR + lane * 16);
    u32x4 sel;
    { const int j = col & 7; const unsigned one = (j & 1) ? 0x3F800000u : 0x00003F80u; sel.x = (j >> 1) == 0 ? one : 0; sel.y = (j >> 1) == 1 ? one : 0; sel.z = (j >> 1) == 2 ? one : 0; sel.w = (j >> 1) == 3 ? one : 0; }
    const unsigned cfg = get_cfg(p, rs, lds);
    WAITVM0();
    bool dead = false;
    const int hh = col >> 2, i2 = col & 3, hB = 4 * hw + hh;
    const unsigned vo_pa = (unsigned)((rh ? 16384 : 32768) + hB * 512 + q4 * 64 + i2 * 16);
    const unsigned vo_pb = (q4 == 3) ? OOB : vo_pa + 256u;
    const unsigned vo_stat = (unsigned)(STATS_OFF + rh * 512 + (hB * 4 + i2) * 4);
    const unsigned vo_ex = (unsigned)(49152 + rh * 4096 + hB * 256 + q4 * 64 + i2 * 16);
    const unsigned vo_u0 = (unsigned)(OFF_UF + ((size_t)beta * 8 + 2 * hw) * FR + lane * 16), vo_u1 = vo_u0 + 1024;
    const unsigned vo_x = (unsigned)(OFF_XF + (size_t)((hw >> 1) * 14 + 2 * c7 + (hw & 1)) * FR + lane * 16);
    const unsigned vo_fu = (unsigned)(OFF_FLAG + (FLAG_U + beta / 14) * 4);
    const unsigned vo_wc = (rh == 0 && lane < 16) ? (unsigned)(OFF_WC + ((size_t)beta * 8 + 2 * hw) * 256 + col * 16) : OOB;
    const bool diag = (col >> 3) == (q4 >> 1);
    const bool lowh = hh < 2, outv = (hh & 1) == (q4 >> 1);
    u32x4 un0 = {0,0,0,0}, un1 = {0,0,0,0}, px = {0,0,0,0};
    unsigned fu = 0;
    u32x4 pa = {0,0,0,0}, pb = {0,0,0,0}, ex = {0,0,0,0}; unsigned st = 0;
    unsigned th = 0;
    volatile LAS unsigned* ldist = (volatile LAS unsigned*)((LAS char*)lds + L_RF + 4);
    await_flag(ds, vo_fu, 8, dead);
    ALD4R(un0, vo_u0, ds, 0u); ALD4R(un1, vo_u1, ds, 0u); ALD4PR(px, vo_x, ds, 0u);
    ALD1R(fu, vo_fu, ds, 64u);
    th = start_sync(p, ds, r, lds) + O1T;
    auto step = [&](const int s, auto LASTC) {
        constexpr bool LAST = decltype(LASTC)::value;
        const unsigned oM = chunk_off(p, cfg, s, 0);
        unsigned sv1 = (unsigned)(s + 1); PIN(sv1);
        asm volatile("s_waitcnt vmcnt(0)" : "+v"(un0), "+v"(un1), "+v"(px), "+v"(fu) :: "memory");
        SCHEDB();
        u32x4 uf0 = un0, uf1 = un1;
        asm volatile("" : "+v"(uf0), "+v"(uf1));
        const unsigned fu_s = (unsigned)__builtin_amdgcn_readfirstlane(fu);
        *(u32x4*)(lds + L_PX + hw * 1024 + lane * 16) = px;
        if (s + 1 < NSEG - 1 && fu_s < 8u) await_flag(ds, vo_fu + (unsigned)(s + 1) * 64u, 8, dead);
        bar();
        const unsigned tb1 = rt32();
        SCHEDB();
        u32x4 Rq[7];
#pragma unroll
        for (int q = 0; q < 7; ++q) Rq[q] = *(const u32x4*)(lds + L_R + q * 1024 + lane * 16);
        SCHEDB();
        const unsigned the = tmax(th, tb1 + MINH);
        f32x4 ae[2] = {{0,0,0,0},{0,0,0,0}}, ao[2] = {{0,0,0,0},{0,0,0,0}};
#pragma unroll
        for (int q = 0; q < WSPLIT; ++q)
#pragma unroll
            for (int a = 0; a < 2; ++a) { ae[a] = mfma32(Rq[q], bw[a][2 * q], ae[a]); ao[a] = mfma32(Rq[q], bw[a][2 * q + 1], ao[a]); }
        SCHEDB();
        if (!LAST) {
            wait_until(the);
            if (w == 6) EV(6);
            ALD4R(pa, vo_pa, ds, oM);
            ALD4R(pb, vo_pb, ds, oM);
            ALD1R(st, vo_stat, ds, oM);
            ALD4R(ex, vo_ex, ds, oM);
            SCHEDB();
        }
#pragma unroll
        for (int q = WSPLIT; q < 7; ++q)
#pragma unroll
            for (int a = 0; a < 2; ++a) { ae[a] = mfma32(Rq[q], bw[a][2 * q], ae[a]); ao[a] = mfma32(Rq[q], bw[a][2 * q + 1], ao[a]); }
        u32x2 wop[2]; u32x4 wst[2];
#pragma unroll
        for (int a = 0; a < 2; ++a) {
            float wv[4];
#pragma unroll
            for (int rr = 0; rr < 4; ++rr) { const float x = (lane < 32) ? ae[a][rr] : ao[a][rr]; float u, v; swap32(x, u, v); wv[rr] = u + v; }
            const u32x2 wp = {pk2(wv[0], wv[1]), pk2(wv[2], wv[3])};
            wop[a].x = diag ? wp.x : 0u; wop[a].y = diag ? wp.y : 0u;
            unsigned a0, b0, a1, b1; uswap16(wp.x, a0, b0); uswap16(wp.y, a1, b1); wst[a] = (u32x4){a0, a1, b0, b1};
        }
        asm volatile("" : "+v"(wop[0]), "+v"(wop[1]), "+v"(wst[0]), "+v"(wst[1]));
        SCHEDB();
        if (!LAST) {
            f32x4 ro;
            auto bstage = [&]() {
                f32x4 sm = mfma32(sel, pa, (f32x4){0, 0, 0, 0});
                sm = mfma32(sel, pb, sm);
                PIN(sm);
                SCHEDB();
                f32x4 dx0 = mfma32(uf0, ex, (f32x4){0, 0, 0, 0}), dx1 = mfma32(uf1, ex, (f32x4){0, 0, 0, 0});
                f32x4 sx4 = mfma32((u32x4){0x3F803F80u, 0x3F803F80u, 0x3F803F80u, 0x3F803F80u}, ex, (f32x4){0, 0, 0, 0});
                PIN(dx0); PIN(dx1); PIN(sx4);
                const float mxs = __uint_as_float(st), sxs = sx4[0];
                SCHEDB();
                float ml = fmx(fmx(sm[0], sm[1]), fmx(sm[2], sm[3]));
                { float a, b2; swap16(ml, a, b2); ml = fmx(fmx(a, b2), mxs); }
                const float nM = -ml * SC2;
                float e[4];
#pragma unroll
                for (int rr = 0; rr < 4; ++rr) e[rr] = __builtin_amdgcn_exp2f(__builtin_fmaf(sm[rr], SC2, nM));
                float al = __builtin_amdgcn_exp2f(__builtin_fmaf(mxs, SC2, nM));
                PIN(al);
                const u32x2 pp = {pk2(e[0], e[1]), pk2(e[2], e[3])};
                const f32x4 r0 = mfma16(wop[0], pp, dx0 * al), r1 = mfma16(wop[1], pp, dx1 * al);
                float ls = (e[0] + e[1]) + (e[2] + e[3]);
                { float a, b2; swap16(ls, a, b2); ls = a + b2; }
                const float inv = __builtin_amdgcn_rcpf(__builtin_fmaf(al, sxs, ls));
#pragma unroll
                for (int rr = 0; rr < 4; ++rr) ro[rr] = (lowh ? r0[rr] : r1[rr]) * inv;
                if (outv) {
                    float* red = (float*)(lds + L_RED2) + (i2 * 4 + 2 * (q4 & 1)) * 36 + hB * 2;
                    *(f32x2*)(red) = (f32x2){ro[0], ro[1]};
                    *(f32x2*)(red + 36) = (f32x2){ro[2], ro[3]};
                }
                SCHEDB();
            };
            {
                const unsigned sn = (unsigned)(s + 1);
                ALD4R(un0, vo_u0, ds, sn * (56u * 8u * 1024u));
                ALD4R(un1, vo_u1, ds, sn * (56u * 8u * 1024u));
                ALD4PR(px, vo_x, ds, sn * 28672u);
                ALD1R(fu, vo_fu, ds, (sn + 1u) * 64u);
            }
            PTB(0);
            asm volatile("s_waitcnt vmcnt(4)" : "+v"(pa), "+v"(pb), "+v"(st), "+v"(ex) :: "memory");
            PTB(1);
            SCHEDB();
            bstage();
            if (__builtin_expect(__any(max(max(max4(pa), max4(pb)), max(max4(ex), st)) == POISON), 0)) {
              int spins = 0; const int lim = dead ? 1 : SPIN_MAX; bool bad;
              ldist[1] = sv1;
              _Pragma("nounroll") do {
                ALD4R(pa, vo_pa, ds, oM);
                ALD4R(pb, vo_pb, ds, oM);
                ALD1R(st, vo_stat, ds, oM);
                ALD4R(ex, vo_ex, ds, oM);
                asm volatile("s_waitcnt vmcnt(0)" : "+v"(pa), "+v"(pb), "+v"(st), "+v"(ex) :: "memory");
                bad = __any(max(max(max4(pa), max4(pb)), max(max4(ex), st)) == POISON);
              } while (bad && ++spins < lim);
              if (bad) dead = true;
              bstage();
            }
            if (w == 7) EV(3);
            PTB(3);
        }
        bar();
        if (!LAST) PTB(4);
        if (LAST || WPOS != 1) { AST4(wst[0], vo_wc, ds, (unsigned)s * (56u * 8u * 256u)); AST4(wst[1], vo_wc + 256u, ds, (unsigned)s * (56u * 8u * 256u)); }
        if (!LAST) th = (unsigned)__builtin_amdgcn_readfirstlane(*(const unsigned*)(lds + L_TS + 4));
    };
    _Pragma("nounroll") for (int s = 0; s < NSEG - 1; ++s) step(s, BoolC<false>{});
    step(NSEG - 1, BoolC<true>{});
    AWAIT(0);
    bar();
}

DEVI void chain_g(const Params& p, int r, char* lds) {
    const int w = __builtin_amdgcn_readfirstlane(threadIdx.x >> 6);
    const u32x4 ds = mkdesc(p.ws, 0x10000000u);
    const unsigned t_entry = rt32();
    __builtin_amdgcn_s_sleep(GDELAY);
    if (w < 4) chain_gw(p, r, lds, ds, t_entry); else chain_bw(p, r, lds, ds);
}

DEVI void x_finalize(rsrc_t rs, f32x4 sm, f32x4 s0, f32x4 s1, int s, int h, int it, int lane) {
    const int col = lane & 15, q4 = lane >> 4;
    const float NEG = -3.0e38f;
    float v0[4], v1[4], vm[4];
#pragma unroll
    for (int rr = 0; rr < 4; ++rr) {
        const bool causal = (4 * q4 + rr) <= col;
        v0[rr] = (it == 1 || causal) ? s0[rr] : NEG;
        v1[rr] = (it == 1 && causal) ? s1[rr] : NEG;
        vm[rr] = (q4 < 2) ? sm[rr] : NEG;
    }
    float m = NEG;
#pragma unroll
    for (int rr = 0; rr < 4; ++rr) m = fmaxf(m, fmaxf(fmaxf(v0[rr], v1[rr]), vm[rr]));
    { float a, b2; swap16(m, a, b2); m = fmaxf(a, b2); swap32(m, a, b2); m = fmaxf(a, b2); }
    const float nM = -m * SC2;
    float l = 0;
#pragma unroll
    for (int rr = 0; rr < 4; ++rr) {
        v0[rr] = (v0[rr] == NEG) ? 0.f : __builtin_amdgcn_exp2f(__builtin_fmaf(v0[rr], SC2, nM));
        v1[rr] = (v1[rr] == NEG) ? 0.f : __builtin_amdgcn_exp2f(__builtin_fmaf(v1[rr], SC2, nM));
        vm[rr] = (vm[rr] == NEG) ? 0.f : __builtin_amdgcn_exp2f(__builtin_fmaf(vm[rr], SC2, nM));
        l += v0[rr] + v1[rr] + vm[rr];
    }
    { float a, b2; swap16(l, a, b2); l = a + b2; swap32(l, a, b2); l = a + b2; }
    const float inv = __builtin_amdgcn_rcpf(l);
    const u32x4 px = {pk2(v0[0] * inv, v0[1] * inv), pk2(v0[2] * inv, v0[3] * inv), pk2(v1[0] * inv, v1[1] * inv), pk2(v1[2] * inv, v1[3] * inv)};
    bst4(rs, px, (unsigned)(OFF_PX + (((size_t)s * 16 + h) * 2 + it) * FR + lane * 16), 0);
    if (q4 < 2) { const u32x2 pm = {pk2(vm[0] * inv, vm[1] * inv), pk2(vm[2] * inv, vm[3] * inv)};
        bst2(rs, pm, (unsigned)(OFF_PM + (((size_t)s * 8 + (h >> 1)) * 2 + it) * 512 + (col + 16 * (2 * (h & 1) + q4)) * 8), 0); }
}

DEVI void chain_x(const Params& p, int h, char* lds) {
    const int tid = threadIdx.x, lane = tid & 63, w = __builtin_amdgcn_readfirstlane(tid >> 6);
    const int col = lane & 15, q4 = lane >> 4;
    const rsrc_t rs = mkrsrc(p.ws, 0x10000000u);
    const u32x4 ds = mkdesc(p.ws, 0x10000000u);
    if (w == 0 && (h & 3) == 0) probe_cfg(p, rs, h >> 2);
    const unsigned cfg = get_cfg(p, rs, lds);
    WAITVM0();
    bool dead = false;
    if (w < 4) {
        const unsigned mE = (col < 8) ? 0xFFFFFFFFu : 0u;
        const int nq = (w < 3) ? 2 : 1;
        const unsigned vo_m0 = (unsigned)(2 * w * 1024 + lane * 16), vo_m1 = (unsigned)((2 * w + nq - 1) * 1024 + lane * 16);
        const unsigned vo_e = (col < 8) ? (unsigned)(49152 + ((col >> 2) & 1) * 4096 + h * 256 + q4 * 64 + (col & 3) * 16)
                            : ((lane == 8 || lane == 12) ? (unsigned)(STATS_OFF + ((lane >> 2) & 1) * 512 + h * 16) : OOB);
        u32x4 R0 = {0,0,0,0}, R1 = {0,0,0,0};
        unsigned tmx = start_sync(p, ds, 112 + h, lds);
        unsigned thx = tmx + O1T;
        __builtin_amdgcn_s_setprio(3);
        bar();
        for (int s = 0; s < NSEG; ++s) {
            const unsigned oM = chunk_off(p, cfg, s, 0), oE = oM + 3u * 16384u;
            if (s < NSEG - 1) {
                u32x4 g[2][2][2];
#pragma unroll
                for (int qi = 0; qi < 2; ++qi)
#pragma unroll
                    for (int e = 0; e < 2; ++e)
#pragma unroll
                        for (int k = 0; k < 2; ++k) g[qi][e][k] = *(const u32x4*)(lds + L_GX + (s & 1) * 28672 + (k * 14 + 2 * ((w < 3) ? 2 * w + qi : 2 * w) + e) * 1024 + lane * 16);
                if (s > 0) {
                    wait_until(thx);
                    unsigned t1 = 0; int spins = 0; const int lim = dead ? 1 : SPIN_MAX; bool bad;
                    const unsigned oT = chunk_off(p, cfg, s - 1, 0); const unsigned vts = (unsigned)(TS_OFF + (lane & 1) * 4);
                    _Pragma("nounroll") do {
                        ALD1R(t1, vts, ds, oT);
                        asm volatile("s_waitcnt vmcnt(0)" : "+v"(t1) :: "memory");
                        bad = __any(t1 == POISON);
                        if (bad) __builtin_amdgcn_s_sleep(4);
                    } while (bad && ++spins < lim);
                    if (bad) dead = true;
                    thx = (unsigned)__builtin_amdgcn_readlane(t1, 1);
                    tmx = (unsigned)__builtin_amdgcn_readlane(t1, 0);
                }
                wait_until(tmx + XOFFT);
                { int spins = 0; const int lim = dead ? 1 : SPIN_MAX; bool bad;
                  _Pragma("nounroll") do {
                    ALD4R(R0, vo_m0, ds, oM);
                    ALD4R(R1, vo_m1, ds, oM);
                    asm volatile("s_waitcnt vmcnt(0)" : "+v"(R0), "+v"(R1) :: "memory");
                    bad = __any(max(max4(R0), max4(R1)) == POISON);
                  } while (bad && ++spins < lim);
                  if (bad) dead = true; }
                f32x4 d[2] = {{0,0,0,0},{0,0,0,0}};
#pragma unroll
                for (int kt = 0; kt < 2; ++kt) { d[kt] = mfma32(g[0][0][kt], R0 & mE, d[kt]); d[kt] = mfma32(g[0][1][kt], R0 & ~mE, d[kt]); }
                if (nq == 2) {
#pragma unroll
                    for (int kt = 0; kt < 2; ++kt) { d[kt] = mfma32(g[1][0][kt], R1 & mE, d[kt]); d[kt] = mfma32(g[1][1][kt], R1 & ~mE, d[kt]); }
                }
                f32x4 o;
#pragma unroll
                for (int rr = 0; rr < 4; ++rr) {
                    const float a = d[0][rr] + dppf<DPP_ROR8>(d[0][rr]), b2 = d[1][rr] + dppf<DPP_ROR8>(d[1][rr]);
                    o[rr] = (col < 8) ? a : b2;
                }
                *(f32x4*)(lds + L_REDX + w * 1024 + lane * 16) = o;
            }
            bar();
            if (w == 0 && s < NSEG - 1) {
                f32x4 t4 = *(const f32x4*)(lds + L_REDX + lane * 16);
#pragma unroll
                for (int pp = 1; pp < 4; ++pp) t4 += *(const f32x4*)(lds + L_REDX + pp * 1024 + lane * 16);
                float m = fmx(fmx(t4.x, t4.y), fmx(t4.z, t4.w));
                m = fmx(m, dppf<DPP_ROR8>(m));
                { float a, b2; swap16(m, a, b2); m = fmx(a, b2); swap32(m, a, b2); m = fmx(a, b2); }
                const unsigned mb = nopoison(__float_as_uint(m));
                u32x4 ms = {dppu<0x00>(mb), dppu<0x55>(mb), dppu<0xAA>(mb), dppu<0xFF>(mb)};
                const float nM = -m * SC2;
                float e[4];
#pragma unroll
                for (int rr = 0; rr < 4; ++rr) e[rr] = __builtin_amdgcn_exp2f(__builtin_fmaf(t4[rr], SC2, nM));
                const unsigned o0 = nopoison(pk2(e[0], e[1])), o1 = nopoison(pk2(e[2], e[3]));
                const unsigned o2 = dppu<DPP_ROR8>(o0), o3 = dppu<DPP_ROR8>(o1);
                const u32x4 o = {col < 8 ? o0 : ms.x, col < 8 ? o1 : ms.y, col < 8 ? o2 : ms.z, col < 8 ? o3 : ms.w};
                AST4(o, vo_e, ds, oM);
            }
        }
        bar();
    } else {
        const int hw = w - 4;
        u32x4 gb[7];
#pragma unroll
        for (int k = 0; k < 7; ++k) gb[k] = (u32x4){0, 0, 0, 0};
        const unsigned vo_fg = (unsigned)(OFF_FLAG + (FLAG_GX + h) * 4);
        const unsigned vo_g = (unsigned)(OFF_GX + ((size_t)h * 28 + hw * 7) * FR + lane * 16);
        auto issue = [&](int sg) {
#pragma unroll
            for (int k = 0; k < 7; ++k) ALD4R(gb[k], vo_g + (unsigned)k * 1024u, ds, (unsigned)sg * (16u * 28u * 1024u));
        };
        auto put = [&](int sg) {
            asm volatile("s_waitcnt vmcnt(0)" : "+v"(gb[0]), "+v"(gb[1]), "+v"(gb[2]), "+v"(gb[3]), "+v"(gb[4]), "+v"(gb[5]), "+v"(gb[6]) :: "memory");
            SCHEDB();
#pragma unroll
            for (int k = 0; k < 7; ++k) *(u32x4*)(lds + L_GX + (sg & 1) * 28672 + (hw * 7 + k) * 1024 + lane * 16) = gb[k];
            SCHEDB();
        };
        await_flag(ds, vo_fg, 2, dead);
        issue(0);
        (void)start_sync(p, ds, 112 + h, lds);
        put(0);
        await_flag(ds, vo_fg + 64u, 2, dead);
        issue(1);
        bar();
        _Pragma("nounroll") for (int s = 0; s < NSEG; ++s) {
            __builtin_amdgcn_s_sleep(XHD);
            put(s + 1);
            const int sn = (s + 2 < NSEG) ? s + 2 : NSEG - 1;
            await_flag(ds, vo_fg + (unsigned)sn * 64u, 2, dead);
            issue(__builtin_amdgcn_readfirstlane(sn));
            bar();
        }
        AWAIT(0);
        bar();
    }
}

DEVI void wait_prog(const Params& p, int s, bool& dead) {
    const int w = __builtin_amdgcn_readfirstlane(threadIdx.x >> 6), lane = threadIdx.x & 63;
    if (w == 0) {
        const unsigned t0 = __builtin_amdgcn_readfirstlane(ldflag(p, FLAG_T0));
        if (t0) {
            const unsigned target = t0 + (unsigned)((s <= NSEG - 3) ? (s + 2) * (O1T + C2H) + 10 : (NSEG - 1) * (O1T + C2H) + 90);
            int g = 0;
            _Pragma("nounroll") while ((int)(rt32() - target) < 0 && ++g < 20000) __builtin_amdgcn_s_sleep(8);
        }
        int k = dead ? SPIN_MAX : 0;
        _Pragma("nounroll") for (; k < SPIN_MAX; ++k) {
            unsigned v;
            if (s <= NSEG - 3) v = ldflag(p, FLAG_PROG + s * 32);
            else { const unsigned a = ldflag(p, FLAG_DONE + lane), b2 = ldflag(p, FLAG_DONE + (lane < 48 ? 64 + lane : 0)); v = a & b2; }
            if (!__any(v == 0u)) break;
            if (s <= NSEG - 4) __builtin_amdgcn_s_sleep(8); else __builtin_amdgcn_s_sleep(2);
        }
        if (k >= SPIN_MAX) dead = true;
    }
    __syncthreads();
}

DEVI void fin_unit(const Params& p, int s, int half, char* lds, bool& dead) {
    const int tid = threadIdx.x, lane = tid & 63, w = __builtin_amdgcn_readfirstlane(tid >> 6);
    const int h = 8 * half + w;
    const rsrc_t rs = mkrsrc(p.ws, 0x10000000u);
    __syncthreads();
    waitflag(p, FLAG_GX + s * 16 + h, 2, dead);
    u32x4 xs[4], g[28];
#pragma unroll
    for (int j = 0; j < 4; ++j) { const int f = (w + 8 * j < 28) ? w + 8 * j : w; xs[j] = *(const u32x4*)(p.ws + OFF_XF + ((size_t)s * 28 + f) * FR + lane * 16); }
#pragma unroll
    for (int f = 0; f < 28; ++f) g[f] = bld4(rs, (unsigned)(OFF_GX + (((size_t)s * 16 + h) * 28 + f) * FR + lane * 16), 0);
#pragma unroll
    for (int j = 0; j < 4; ++j) { const int f = w + 8 * j; if (f < 28) *(u32x4*)(lds + f * 1024 + lane * 16) = xs[j]; }
    __syncthreads();
    f32x4 xx[3] = {{0,0,0,0},{0,0,0,0},{0,0,0,0}};
#pragma unroll
    for (int t = 0; t < 14; ++t) {
        const u32x4 x0 = *(const u32x4*)(lds + t * 1024 + lane * 16), x1 = *(const u32x4*)(lds + (14 + t) * 1024 + lane * 16);
        xx[0] = mfma32(g[t], x0, xx[0]);
        xx[1] = mfma32(g[t], x1, xx[1]);
        xx[2] = mfma32(g[14 + t], x1, xx[2]);
    }
    wait_prog(p, s, dead);
    f32x4 sm = {0, 0, 0, 0};
    {
        u32x4 xm[7];
#pragma unroll
        for (int c = 0; c < 7; ++c) xm[c] = bld4(rs, (unsigned)(OFF_XM + (((size_t)s * 16 + h) * 7 + c) * 1024 + lane * 16), 0);
#pragma unroll
        for (int c = 0; c < 7; ++c) sm += __builtin_bit_cast(f32x4, xm[c]);
    }
    f32x4 sm1;
#pragma unroll
    for (int rr = 0; rr < 4; ++rr) { float lo, hi; swap32(sm[rr], lo, hi); sm[rr] = lo; sm1[rr] = hi; }
    x_finalize(rs, sm, xx[0], xx[0], s, h, 0, lane);
    x_finalize(rs, sm1, xx[1], xx[2], s, h, 1, lane);
    WAITVM0();
    __syncthreads();
    if (tid == 0) bst1(rs, 1u, (unsigned)(OFF_FLAG + (FLAG_P + s * 32 + half) * 4), 0);
}

DEVI void out_unit(const Params& p, int s, int bg, char* lds, bool& dead) {
    const int tid = threadIdx.x, lane = tid & 63, w = __builtin_amdgcn_readfirstlane(tid >> 6);
    const int col = lane & 15, q4 = lane >> 4;
    const rsrc_t rs = mkrsrc(p.ws, 0x10000000u);
    const int beta = 8 * bg + w;
    constexpr int O_PX = 0, O_PM = 32768, O_W = 40960;
    waitflag(p, FLAG_U + s * 16 + beta / 14, 8, dead);
    u32x4 uf[8];
#pragma unroll
    for (int hp = 0; hp < 8; ++hp) uf[hp] = bld4(rs, (unsigned)(OFF_UF + (((size_t)s * 56 + beta) * 8 + hp) * FR + lane * 16), 0);
    wait_prog(p, s, dead);
    u32x4 wl[2];
#pragma unroll
    for (int j = 0; j < 2; ++j) wl[j] = bld4(rs, (unsigned)(OFF_WC + ((size_t)s * 56 + 8 * bg) * 2048 + (2 * w + j) * 1024 + lane * 16), 0);
    waitall(p, FLAG_P + s * 32, 2, dead);
    u32x4 pl[4], pml;
#pragma unroll
    for (int j = 0; j < 4; ++j) pl[j] = bld4(rs, (unsigned)(OFF_PX + ((size_t)s * 32 + 4 * w + j) * FR + lane * 16), 0);
    pml = bld4(rs, (unsigned)(OFF_PM + (size_t)s * 8192 + w * 1024 + lane * 16), 0);
#pragma unroll
    for (int j = 0; j < 2; ++j) *(u32x4*)(lds + O_W + (2 * w + j) * 1024 + lane * 16) = wl[j];
#pragma unroll
    for (int j = 0; j < 4; ++j) *(u32x4*)(lds + O_PX + (4 * w + j) * 1024 + lane * 16) = pl[j];
    *(u32x4*)(lds + O_PM + w * 1024 + lane * 16) = pml;
    __syncthreads();
    const bool diag = (col >> 3) == (q4 >> 1);
    u32x2 wv[8];
#pragma unroll
    for (int hp = 0; hp < 8; ++hp) { const u32x2 t = *(const u32x2*)(lds + O_W + (w * 8 + hp) * 256 + col * 16 + (q4 & 1) * 8); wv[hp] = diag ? t : (u32x2){0u, 0u}; }
#pragma unroll
    for (int it = 0; it < 2; ++it) {
        f32x4 aa = {0,0,0,0}, ab = {0,0,0,0}, am = {0,0,0,0};
#pragma unroll
        for (int hp = 0; hp < 8; ++hp) {
            const u32x4 pa = *(const u32x4*)(lds + O_PX + ((2 * hp) * 2 + it) * 1024 + lane * 16), pb = *(const u32x4*)(lds + O_PX + ((2 * hp + 1) * 2 + it) * 1024 + lane * 16);
            const u32x2 pm = *(const u32x2*)(lds + O_PM + (hp * 2 + it) * 512 + lane * 8);
            aa = mfma32(uf[hp], pa, aa);
            ab = mfma32(uf[hp], pb, ab);
            am = mfma16(wv[hp], pm, am);
        }
        float v[4];
#pragma unroll
        for (int rr = 0; rr < 4; ++rr) { const float x = ((q4 >> 1) == 0 ? aa[rr] : ab[rr]) + am[rr]; float a, b2; swap32(x, a, b2); v[rr] = a + b2; }
        if (lane < 32) { const u32x2 o = {pk2(v[0], v[1]), pk2(v[2], v[3])};
            *(u32x2*)(p.out + (size_t)(32 * s + 16 * it + col) * OUTW + 8 * beta + 4 * q4) = o; }
    }
}

DEVI float phi(float t) { return (t > 0.f ? t + 1.f : __builtin_amdgcn_exp2f(t * 1.4426950408889634f)) + 1e-6f; }
DEVI float rbf(float x) { return bf_lo(pk2(x, 0.f)); }

DEVI void b1_unit(const Params& p, int rb, int hp, char* lds) {
    const int tid = threadIdx.x, lane = tid & 63, w = __builtin_amdgcn_readfirstlane(tid >> 6);
    const int col = lane & 15, q4 = lane >> 4;
    const rsrc_t rs = mkrsrc(p.ws, 0x10000000u);
    const int h = 2 * hp + (w & 1), rt0 = rb * 8 + (w >> 1) * 2;
    u32x4 xr[2][14], stg[11];
    auto wload = [&](int c) {
#pragma unroll
        for (int j = 0; j < 11; ++j) {
            const int i = (w + 8 * j < 84) ? w + 8 * j : w, tt = i % 7, g = i / 7, tile = g & 1, mat = (g >> 1) % 3, hh = g / 6;
            stg[j] = *(const u32x4*)(p.ws + OFF_BW + ((size_t)((mat * 16 + 2 * hp + hh) * 2 + tile) * 14 + 7 * c + tt) * FR + lane * 16);
        }
    };
    auto wput = [&]() {
#pragma unroll
        for (int j = 0; j < 11; ++j) if (w + 8 * j < 84) *(u32x4*)(lds + (w + 8 * j) * 1024 + lane * 16) = stg[j];
    };
    wload(0);
#pragma unroll
    for (int a = 0; a < 2; ++a)
#pragma unroll
        for (int t = 0; t < 14; ++t) xr[a][t] = *(const u32x4*)(p.ws + OFF_BX + ((size_t)(rt0 + a) * 14 + t) * FR + lane * 16);
    f32x4 aq[2][2], ak[2][2], av[2][2];
#pragma unroll
    for (int a = 0; a < 2; ++a)
#pragma unroll
        for (int c = 0; c < 2; ++c) { aq[a][c] = (f32x4){0,0,0,0}; ak[a][c] = (f32x4){0,0,0,0}; av[a][c] = (f32x4){0,0,0,0}; }
    __syncthreads();
    wput();
    wload(1);
    __syncthreads();
#pragma unroll
    for (int c2 = 0; c2 < 2; ++c2) {
#pragma unroll
        for (int tt = 0; tt < 7; ++tt) {
            const char* wb = lds + ((w & 1) * 42 + tt) * 1024 + lane * 16;
            u32x4 wq[2], wk[2], wv[2];
#pragma unroll
            for (int c = 0; c < 2; ++c) { wq[c] = *(const u32x4*)(wb + (0 + c) * 7168); wk[c] = *(const u32x4*)(wb + (2 + c) * 7168); wv[c] = *(const u32x4*)(wb + (4 + c) * 7168); }
#pragma unroll
            for (int a = 0; a < 2; ++a)
#pragma unroll
                for (int c = 0; c < 2; ++c) {
                    aq[a][c] = mfma32(wq[c], xr[a][7 * c2 + tt], aq[a][c]);
                    ak[a][c] = mfma32(xr[a][7 * c2 + tt], wk[c], ak[a][c]);
                    av[a][c] = mfma32(xr[a][7 * c2 + tt], wv[c], av[a][c]);
                }
        }
        if (c2 == 0) { __syncthreads(); wput(); __syncthreads(); }
    }
    __syncthreads();
#pragma unroll
    for (int a = 0; a < 2; ++a) {
        f32x4 p0, p1;
#pragma unroll
        for (int rr = 0; rr < 4; ++rr) {
            p0[rr] = phi(rbf(aq[a][0][rr]));
            p1[rr] = (8 * q4 + 4 + rr < 28) ? phi(rbf(aq[a][1][rr])) : 0.f;
        }
        bst4(rs, pack8(p0, p1), (unsigned)(OFF_BQ + ((size_t)(rt0 + a) * 16 + h) * FR + lane * 16), 0);
    }
    u32x4 pk[2], vv[2];
#pragma unroll
    for (int c = 0; c < 2; ++c) {
        f32x4 k0, k1, v0, v1;
        const int d = 16 * c + col;
#pragma unroll
        for (int rr = 0; rr < 4; ++rr) {
            k0[rr] = (d < 28) ? phi(rbf(ak[0][c][rr])) : 0.f; k1[rr] = (d < 28) ? phi(rbf(ak[1][c][rr])) : 0.f;
            v0[rr] = (d == 28) ? 1.f : av[0][c][rr]; v1[rr] = (d == 28) ? 1.f : av[1][c][rr];
        }
        pk[c] = pack8(k0, k1); vv[c] = pack8(v0, v1);
    }
    float* red = (float*)lds;
#pragma unroll
    for (int dc = 0; dc < 2; ++dc)
#pragma unroll
        for (int ec = 0; ec < 2; ++ec) {
            const f32x4 d = mfma32(pk[dc], vv[ec], (f32x4){0,0,0,0});
            *(f32x4*)(red + ((w * 4 + dc * 2 + ec) * 64 + lane) * 4) = d;
        }
    bar();
    {
        const int hh = w & 1, tile = w >> 1;
        f32x4 t4 = {0,0,0,0};
#pragma unroll
        for (int g = 0; g < 4; ++g) t4 += *(const f32x4*)(red + (((g * 2 + hh) * 4 + tile) * 64 + lane) * 4);
        bst4(rs, __builtin_bit_cast(u32x4, t4), (unsigned)(OFF_BKV + (((size_t)rb * 16 + 2 * hp + hh) * 4 + tile) * 1024 + lane * 16), 0);
    }
    WAITVM0();
    bar();
    if (tid == 0) signal(p, FLAG_B1);
}

DEVI void b2_unit(const Params& p, int h, char* lds, bool& dead) {
    const int tid = threadIdx.x, lane = tid & 63, w = __builtin_amdgcn_readfirstlane(tid >> 6);
    const int col = lane & 15, q4 = lane >> 4;
    const rsrc_t rs = mkrsrc(p.ws, 0x10000000u);
    float* kvs = (float*)lds;
    waitflag(p, FLAG_B1, 128, dead);
    if (w < 4) {
        f32x4 t4 = {0,0,0,0};
#pragma unroll
        for (int rb = 0; rb < 16; ++rb) t4 += __builtin_bit_cast(f32x4, bld4(rs, (unsigned)(OFF_BKV + (((size_t)rb * 16 + h) * 4 + w) * 1024 + lane * 16), 0));
        const int dc = w >> 1, ec = w & 1;
#pragma unroll
        for (int rr = 0; rr < 4; ++rr) kvs[(16 * dc + 4 * q4 + rr) * 33 + 16 * ec + col] = t4[rr];
    }
    bar();
    if (tid < 32) bst1(rs, __float_as_uint(kvs[tid * 33 + 28]), (unsigned)(OFF_BZ + (h * 32 + tid) * 4), 0);
    u32x4 ka[2];
#pragma unroll
    for (int bb = 0; bb < 2; ++bb) {
        const int d = 8 * (col >> 2) + 4 * bb + (col & 3);
        float e[8];
#pragma unroll
        for (int j = 0; j < 8; ++j) e[j] = (8 * q4 + j < 28) ? kvs[d * 33 + 8 * q4 + j] : 0.f;
        ka[bb] = (u32x4){pk2(e[0], e[1]), pk2(e[2], e[3]), pk2(e[4], e[5]), pk2(e[6], e[7])};
    }
    for (int ct = w; ct < 28; ct += 8) {
        u16 e[8];
#pragma unroll
        for (int j = 0; j < 8; ++j) e[j] = (8 * q4 + j < 28) ? p.bwo[(size_t)(28 * h + 8 * q4 + j) * DM + 32 * (ct >> 1) + 8 * (col >> 2) + 4 * (ct & 1) + (col & 3)] : (u16)0;
        const u32x4 wf = {e[0] | ((unsigned)e[1] << 16), e[2] | ((unsigned)e[3] << 16), e[4] | ((unsigned)e[5] << 16), e[6] | ((unsigned)e[7] << 16)};
        const f32x4 d0 = mfma32(ka[0], wf, (f32x4){0,0,0,0}), d1 = mfma32(ka[1], wf, (f32x4){0,0,0,0});
        bst4(rs, pack8(d0, d1), (unsigned)(OFF_BM + ((size_t)h * 28 + ct) * FR + lane * 16), 0);
    }
    WAITVM0();
    bar();
    if (tid == 0) signal(p, FLAG_B2);
}

DEVI void b3_unit(const Params& p, int u, char* lds, bool& dead) {
    const int tid = threadIdx.x, lane = tid & 63, w = __builtin_amdgcn_readfirstlane(tid >> 6);
    const int col = lane & 15, q4 = lane >> 4;
    const rsrc_t rs = mkrsrc(p.ws, 0x10000000u);
    waitflag(p, FLAG_B2, 16, dead);
    __syncthreads();
#pragma unroll
    for (int k = 0; k < 4; ++k) {
        const int h = 2 * w + (k >> 1), rt = 2 * u + (k & 1);
        const u32x4 pq = bld4(rs, (unsigned)(OFF_BQ + ((size_t)rt * 16 + h) * FR + lane * 16), 0);
        const f32x4 z0 = __builtin_bit_cast(f32x4, bld4(rs, (unsigned)(OFF_BZ + (h * 32 + 8 * q4) * 4), 0)), z1 = __builtin_bit_cast(f32x4, bld4(rs, (unsigned)(OFF_BZ + (h * 32 + 8 * q4 + 4) * 4), 0));
        float f[8] = {bf_lo(pq.x), bf_hi(pq.x), bf_lo(pq.y), bf_hi(pq.y), bf_lo(pq.z), bf_hi(pq.z), bf_lo(pq.w), bf_hi(pq.w)};
        float den = f[0] * z0.x + f[1] * z0.y + f[2] * z0.z + f[3] * z0.w + f[4] * z1.x + f[5] * z1.y + f[6] * z1.z + f[7] * z1.w;
        { float a, b2; swap16(den, a, b2); den = a + b2; swap32(den, a, b2); den = a + b2; }
        const float inv = 1.0f / (den + 1e-6f);
        const u32x4 pn = {pk2(f[0] * inv, f[1] * inv), pk2(f[2] * inv, f[3] * inv), pk2(f[4] * inv, f[5] * inv), pk2(f[6] * inv, f[7] * inv)};
        *(u32x4*)(lds + ((k & 1) * 16 + h) * 1024 + lane * 16) = pn;
    }
    u32x4 bm[2][16];
    auto bload = [&](int pr) {
#pragma unroll
        for (int a = 0; a < 2; ++a)
#pragma unroll
            for (int h = 0; h < 16; ++h) bm[a][h] = bld4(rs, (unsigned)(OFF_BM + ((size_t)h * 28 + 2 * pr + a) * FR + lane * 16), 0);
    };
    if (w < 7) bload(2 * w);
    __syncthreads();
    if (w < 7) {
#pragma unroll
        for (int jp = 0; jp < 2; ++jp) {
            const int pr = 2 * w + jp;
            f32x4 acc[2][2] = {{{0,0,0,0},{0,0,0,0}},{{0,0,0,0},{0,0,0,0}}};
#pragma unroll
            for (int h = 0; h < 16; ++h) {
                const u32x4 p0 = *(const u32x4*)(lds + h * 1024 + lane * 16), p1 = *(const u32x4*)(lds + (16 + h) * 1024 + lane * 16);
#pragma unroll
                for (int a = 0; a < 2; ++a) { acc[a][0] = mfma32(bm[a][h], p0, acc[a][0]); acc[a][1] = mfma32(bm[a][h], p1, acc[a][1]); }
            }
            if (jp == 0) bload(2 * w + 1);
#pragma unroll
            for (int rt = 0; rt < 2; ++rt)
                *(u32x4*)(p.out + (size_t)(32 * u + 16 * rt + col) * OUTW + 448 + 32 * pr + 8 * q4) = pack8(acc[0][rt], acc[1][rt]);
        }
    }
}

DEVI void worker(const Params& p, int u, char* lds) {
    bool dead = false;
    if (p.mode != 2) gemm_worker(p, u, lds);
    if (p.mode == 1) return;
    if (p.mode == 2 && (p.mode2 & 1)) return;
    b1_unit(p, u >> 3, u & 7, lds);
    if (u < 16) b2_unit(p, u, lds, dead);
    if (u >= 64) b3_unit(p, u - 64, lds, dead);
    _Pragma("nounroll") for (int j = u; j < 9 * NSEG; j += 128) {
        const int s = j / 9, k = j % 9;
        if (k < 2) fin_unit(p, s, k, lds, dead); else out_unit(p, s, k - 2, lds, dead);
    }
}

__global__ void __launch_bounds__(512, 2) k_main(Params p) {
    extern __shared__ __attribute__((aligned(16))) char lds[];
    const int b = blockIdx.x;
    const int idx = (b >> 3) * 4 + (b & 3);
    if ((b & 7) < 4) {
        if (p.mode == 1) return;
        if (idx < 112) chain_g(p, idx, lds); else chain_x(p, idx - 112, lds);
    } else {
        worker(p, idx, lds);
    }
}

extern "C" void kernel_launch(void* const* d_in, const int* in_sizes, int n_in, void* d_out, int out_size, void* d_ws, size_t ws_size, hipStream_t stream) {
    Params p;
    p.x = (const u16*)d_in[0]; p.mem0 = (const u16*)d_in[1]; p.Wq = (const u16*)d_in[2]; p.Wk = (const u16*)d_in[3];
    p.Wv = (const u16*)d_in[4]; p.Wo = (const u16*)d_in[5]; p.bx = (const u16*)d_in[7]; p.wqkv = (const u16*)d_in[8]; p.bwo = (const u16*)d_in[9];
    p.out = (u16*)d_out; p.ws = (char*)d_ws;
    const uintptr_t base = (uintptr_t)d_ws;
    const uintptr_t al = (base + OFF_COMM_RAW + ((2u << 20) - 1)) & ~(uintptr_t)((2u << 20) - 1);
    p.comm_off = (unsigned long long)(al - base);
    (void)hipFuncSetAttribute((const void*)k0_prep, hipFuncAttributeMaxDynamicSharedMemorySize, 136000);
    (void)hipFuncSetAttribute((const void*)k_main, hipFuncAttributeMaxDynamicSharedMemorySize, 160000);
    k0_prep<<<256, 512, 136000, stream>>>(p);
    p.mode = 0; p.mode2 = 0;
    k_main<<<256, 512, 160000, stream>>>(p);
}
```

```cpp
#include <hip/hip_runtime.h>
#include <hip/hip_bf16.h>
#include <stdint.h>

typedef __attribute__((ext_vector_type(8))) __bf16 bf16x8;
typedef __attribute__((ext_vector_type(4))) short s16x4;
typedef __attribute__((ext_vector_type(2))) __bf16 bf16x2;
typedef __attribute__((ext_vector_type(2))) float f32x2;
typedef __attribute__((ext_vector_type(4))) float f32x4;
typedef __attribute__((ext_vector_type(4))) unsigned u32x4;
typedef __attribute__((ext_vector_type(2))) unsigned u32x2;
typedef unsigned short u16;
#define LAS __attribute__((address_space(3)))
#define DEVI __device__ __forceinline__

constexpr int DM = 448, NH = 16, NSEG = 64, SEGL = 32, OUTW = 896;
constexpr size_t FR = 1024;
constexpr size_t OFF_FLAG = 0;
constexpr size_t SZ_FLAG = 69632;
constexpr size_t OFF_AF = OFF_FLAG + SZ_FLAG;
constexpr size_t SZ_AF = (size_t)16 * 28 * 14 * FR;
constexpr size_t OFF_BF = OFF_AF + SZ_AF;
constexpr size_t OFF_XF = OFF_BF + SZ_AF;
constexpr size_t SZ_XF = (size_t)64 * 28 * FR;
constexpr size_t OFF_GX = OFF_XF + SZ_XF;
constexpr size_t SZ_GX = (size_t)64 * 16 * 28 * FR;
constexpr size_t OFF_UF = OFF_GX + SZ_GX;
constexpr size_t SZ_UF = (size_t)64 * 56 * 8 * FR;
constexpr size_t OFF_XM = OFF_UF + SZ_UF;
constexpr size_t SZ_XM = (size_t)64 * 16 * 7 * 1024;
constexpr size_t OFF_WC = OFF_XM + SZ_XM;
constexpr size_t SZ_WC = (size_t)64 * 56 * 8 * 256;
constexpr size_t OFF_PX = OFF_WC + SZ_WC;
constexpr size_t SZ_PX = (size_t)64 * 16 * 2 * FR;
constexpr size_t OFF_PM = OFF_PX + SZ_PX;
constexpr size_t SZ_PM = (size_t)64 * 8 * 2 * 512;
constexpr size_t OFF_BQ = OFF_PM + SZ_PM;
constexpr size_t SZ_BQ = (size_t)128 * 16 * FR;
constexpr size_t OFF_BW = OFF_BQ + SZ_BQ;
constexpr size_t SZ_BW = (size_t)3 * 16 * 2 * 14 * FR;
constexpr size_t OFF_BX = OFF_BW + SZ_BW;
constexpr size_t SZ_BX = (size_t)128 * 14 * FR;
constexpr size_t OFF_BKV = OFF_BX + SZ_BX;
constexpr size_t SZ_BKV = (size_t)16 * 16 * 4 * 1024;
constexpr size_t OFF_BM = OFF_BKV + SZ_BKV;
constexpr size_t SZ_BM = (size_t)16 * 28 * FR;
constexpr size_t OFF_BZ = OFF_BM + SZ_BM;
constexpr size_t SZ_BZ = 16 * 32 * 4;
constexpr size_t OFF_COMM_RAW = OFF_BZ + SZ_BZ;
constexpr size_t COMM_BYTES = (size_t)6 << 20;
constexpr unsigned POISON = 0xFFFFFFFFu;
constexpr int FLAG_CFG = 0;
constexpr int FLAG_RDY = 12544;
constexpr int FLAG_B1 = 32;
constexpr int FLAG_B2 = 64;
constexpr int FLAG_PROG = 8192;
constexpr int FLAG_DONE = 12288;
constexpr int FLAG_T0 = 384;
constexpr int FLAG_GX = 2048;
constexpr int FLAG_U = 3072;
constexpr int FLAG_P = 4096;
constexpr int STATS_OFF = 7168;
constexpr int TS_OFF = 32768 + 448;

struct Params {
    const u16 *x, *mem0, *Wq, *Wk, *Wv, *Wo, *bx, *wqkv, *bwo;
    u16* out;
    char* ws;
    unsigned long long comm_off;
    unsigned long long mode;
    unsigned long long mode2;
};

DEVI unsigned pk2(float a, float b) { f32x2 v = {a, b}; bf16x2 r = __builtin_convertvector(v, bf16x2); return __builtin_bit_cast(unsigned, r); }
DEVI f32x4 mfma32(u32x4 a, u32x4 b, f32x4 c) { return __builtin_amdgcn_mfma_f32_16x16x32_bf16(__builtin_bit_cast(bf16x8, a), __builtin_bit_cast(bf16x8, b), c, 0, 0, 0); }
DEVI f32x4 mfma16(u32x2 a, u32x2 b, f32x4 c) { return __builtin_amdgcn_mfma_f32_16x16x16bf16_1k(__builtin_bit_cast(s16x4, a), __builtin_bit_cast(s16x4, b), c, 0, 0, 0); }
DEVI u32x4 pack8(f32x4 a, f32x4 b) { u32x4 r; r.x = pk2(a.x, a.y); r.y = pk2(a.z, a.w); r.z = pk2(b.x, b.y); r.w = pk2(b.z, b.w); return r; }
DEVI float fmx(float a, float b) { return __builtin_amdgcn_fmed3f(a, b, 3.4028235e38f); }
template <int CTRL> DEVI float dppf(float x) { return __uint_as_float((unsigned)__builtin_amdgcn_update_dpp(0, (int)__float_as_uint(x), CTRL, 0xf, 0xf, true)); }
constexpr int DPP_ROR8 = 0x128, DPP_ROR4 = 0x124, DPP_QP_1032 = 0xB1, DPP_QP_2301 = 0x4E;
DEVI void swap16(float x, float& a, float& b) { auto s = __builtin_amdgcn_permlane16_swap(__float_as_uint(x), __float_as_uint(x), false, false); a = __uint_as_float(s[0]); b = __uint_as_float(s[1]); }
DEVI void swap32(float x, float& a, float& b) { auto s = __builtin_amdgcn_permlane32_swap(__float_as_uint(x), __float_as_uint(x), false, false); a = __uint_as_float(s[0]); b = __uint_as_float(s[1]); }
DEVI void uswap16(unsigned x, unsigned& a, unsigned& b) { auto s = __builtin_amdgcn_permlane16_swap(x, x, false, false); a = s[0]; b = s[1]; }
DEVI void uswap32(unsigned x, unsigned& a, unsigned& b) { auto s = __builtin_amdgcn_permlane32_swap(x, x, false, false); a = s[0]; b = s[1]; }
template <int CTRL> DEVI unsigned dppu(unsigned x) { return (unsigned)__builtin_amdgcn_update_dpp(0, (int)x, CTRL, 0xf, 0xf, true); }
constexpr int DPP_SHL4 = 0x104, DPP_SHL8 = 0x108;
DEVI float bf_lo(unsigned v) { return __uint_as_float(v << 16); }
DEVI float bf_hi(unsigned v) { return __uint_as_float(v & 0xFFFF0000u); }
DEVI int kcol(int t, int q4) { return 64 * (t >> 1) + 16 * q4 + 8 * (t & 1); }
DEVI unsigned nopoison(unsigned v) { return v < 0xFFFFFFFEu ? v : 0xFFFFFFFEu; }
DEVI unsigned max4(u32x4 v) { unsigned a = v.x > v.y ? v.x : v.y, b = v.z > v.w ? v.z : v.w; return a > b ? a : b; }

typedef __amdgpu_buffer_rsrc_t rsrc_t;
DEVI rsrc_t mkrsrc(const void* p, unsigned bytes) { return __builtin_amdgcn_make_buffer_rsrc((void*)p, 0, bytes, 0x00020000); }
DEVI u32x4 bld4(rsrc_t r, unsigned v, unsigned s) { return __builtin_amdgcn_raw_buffer_load_b128(r, v, s, 16); }
DEVI u32x2 bld2(rsrc_t r, unsigned v, unsigned s) { return __builtin_amdgcn_raw_buffer_load_b64(r, v, s, 16); }
DEVI unsigned bld1(rsrc_t r, unsigned v, unsigned s) { return __builtin_amdgcn_raw_buffer_load_b32(r, v, s, 16); }
DEVI u32x4 bld4p(rsrc_t r, unsigned v, unsigned s) { return __builtin_amdgcn_raw_buffer_load_b128(r, v, s, 0); }
DEVI void bst4(rsrc_t r, u32x4 d, unsigned v, unsigned s) { __builtin_amdgcn_raw_buffer_store_b128(d, r, v, s, 16); }
DEVI void bst2(rsrc_t r, u32x2 d, unsigned v, unsigned s) { __builtin_amdgcn_raw_buffer_store_b64(d, r, v, s, 16); }
DEVI void bst1(rsrc_t r, unsigned d, unsigned v, unsigned s) { __builtin_amdgcn_raw_buffer_store_b32(d, r, v, s, 16); }
#define MEMFENCE() asm volatile("" ::: "memory")
#define WAITVM0() do { asm volatile("s_waitcnt vmcnt(0)" ::: "memory"); __builtin_amdgcn_s_waitcnt(0x0F70); } while (0)
DEVI void bar() { asm volatile("s_waitcnt lgkmcnt(0)\n\ts_barrier" ::: "memory"); }
constexpr unsigned OOB = 0xF0000000u;
DEVI u32x4 mkdesc(const void* p, unsigned bytes) {
    const unsigned long long a = (unsigned long long)p;
    u32x4 d; d.x = __builtin_amdgcn_readfirstlane((unsigned)a); d.y = __builtin_amdgcn_readfirstlane((unsigned)(a >> 32)) & 0xFFFFu; d.z = bytes; d.w = 0x00020000u; return d;
}
#define ALD4(dst, vo, ds, so) asm volatile("buffer_load_dwordx4 %0, %1, %2, %3 offen sc1" : "=v"(dst) : "v"(vo), "s"(ds), "s"(so) : "memory")
#define ALD4R(dst, vo, ds, so) asm volatile("buffer_load_dwordx4 %0, %1, %2, %3 offen sc1" : "+v"(dst) : "v"(vo), "s"(ds), "s"(so) : "memory")
#define ALD4PR(dst, vo, ds, so) asm volatile("buffer_load_dwordx4 %0, %1, %2, %3 offen" : "+v"(dst) : "v"(vo), "s"(ds), "s"(so) : "memory")
#define ALD1(dst, vo, ds, so) asm volatile("buffer_load_dword %0, %1, %2, %3 offen sc1" : "=v"(dst) : "v"(vo), "s"(ds), "s"(so) : "memory")
#define ALD1R(dst, vo, ds, so) asm volatile("buffer_load_dword %0, %1, %2, %3 offen sc1" : "+v"(dst) : "v"(vo), "s"(ds), "s"(so) : "memory")
#define AST4(dat, vo, ds, so) asm volatile("buffer_store_dwordx4 %0, %1, %2, %3 offen sc1\n\ts_nop 1" :: "v"(dat), "v"(vo), "s"(ds), "s"(so) : "memory")
#define AST2(dat, vo, ds, so) asm volatile("buffer_store_dwordx2 %0, %1, %2, %3 offen sc1\n\ts_nop 1" :: "v"(dat), "v"(vo), "s"(ds), "s"(so) : "memory")
#define AST1(dat, vo, ds, so) asm volatile("buffer_store_dword %0, %1, %2, %3 offen sc1" :: "v"(dat), "v"(vo), "s"(ds), "s"(so) : "memory")
#define AADD1(vo, ds) asm volatile("buffer_atomic_add %0, %1, %2, 0 offen" :: "v"(1u), "v"(vo), "s"(ds) : "memory")
#define SCHEDB() __builtin_amdgcn_sched_barrier(0)
#define PIN(x) asm volatile("" : "+v"(x))
#define AWAIT(n) asm volatile("s_waitcnt vmcnt(" #n ")" ::: "memory")

#define K0AUX 16
#define K0ST(v, off) __builtin_amdgcn_raw_buffer_store_b128(v, rs, off, 0, K0AUX)
__global__ void __launch_bounds__(512, 2) k0_prep(Params p) {
    extern __shared__ __attribute__((aligned(16))) char lds[];
    const rsrc_t rs = mkrsrc(p.ws, 0x10000000u);
    const int b = blockIdx.x, tid = threadIdx.x, lane = tid & 63, w = __builtin_amdgcn_readfirstlane(tid >> 6);
    const int l15 = lane & 15, q4 = lane >> 4;
    const int mat = b & 1, h = (b >> 1) & 15, sub = b >> 5, th = sub & 1, cg = sub >> 1;
    constexpr int K_SCR = 61440, K_XT = K_SCR + 7 * 8448;
    const int lr = lane >> 2, lp = lane & 3;
    u32x4 stg[7];
    if (mat == 0) {
#pragma unroll
        for (int i = 0; i < 7; ++i) {
            const int ct = 7 * cg + i;
            const int c = 64 * (ct >> 2) + 16 * (lr >> 2) + 4 * (ct & 3) + (lr & 3);
            stg[i] = *(const u32x4*)(p.Wq + ((size_t)c * 16 + h) * 256 + 32 * w + 8 * lp);
        }
    } else {
#pragma unroll
        for (int i = 0; i < 7; ++i) {
            const int n = tid + 512 * i, e = n / 14, ch = n % 14;
            stg[i] = *(const u32x4*)(p.Wo + ((size_t)h * 256 + e) * 448 + 112 * cg + 8 * ch);
        }
    }
    u32x4 rw[2][8];
    const int t = 7 * th + (w < 7 ? w : 0);
    {
        const u16* Wr = mat ? p.Wv : p.Wk;
#pragma unroll
        for (int bb = 0; bb < 2; ++bb) {
            const int d = 64 * (t >> 1) + 16 * (lr >> 2) + 8 * (t & 1) + 4 * bb + (lr & 3);
#pragma unroll
            for (int k = 0; k < 8; ++k) rw[bb][k] = *(const u32x4*)(Wr + ((size_t)d * 16 + h) * 256 + 32 * k + 8 * lp);
        }
    }
    const char* xsrc = (const char*)((b & 1) ? p.bx : p.x) + (size_t)(b >> 1) * 14336;
    const u32x4 xt0 = *(const u32x4*)(xsrc + tid * 16);
    const u32x4 xt1 = *(const u32x4*)(xsrc + 8192 + (tid < 384 ? tid : 0) * 16);
    const int fw = b * 8 + w;
    u16 ew[8];
    {
        const int f = (fw < 1344) ? fw : 0;
        const int tt = f % 14, tile = (f / 14) & 1, hh = (f / 28) & 15, t3 = f / 448;
        const int col = (t3 == 0) ? (8 * (l15 >> 2) + 4 * tile + (l15 & 3)) : (16 * tile + l15);
#pragma unroll
        for (int j = 0; j < 8; ++j) ew[j] = (col < 28) ? p.wqkv[(size_t)(kcol(tt, q4) + j) * 1344 + t3 * 448 + hh * 28 + col] : (u16)0;
    }
    if (b & 4) {
        const int q = (b >> 3) * 4 + (b & 3);
        const unsigned cb = (unsigned)p.comm_off + (unsigned)q * 32768u;
        const u32x4 pz = {POISON, POISON, POISON, POISON};
#pragma unroll
        for (int k = 0; k < 4; ++k) K0ST(pz, cb + k * 8192 + tid * 16);
        if (q == 0 && tid < 448) {
            const int beta = tid >> 3, i = tid & 7;
            const u32x4 m = *(const u32x4*)(p.mem0 + i * DM + 8 * beta);
            K0ST(m, cb + tid * 16);
            K0ST(m, cb + 8192 + tid * 16);
        }
    }
    if (b < 34 && tid < 128) K0ST(((u32x4){0, 0, 0, 0}), (unsigned)(OFF_FLAG + (size_t)b * 2048 + tid * 16));
    if (mat == 0) {
#pragma unroll
        for (int i = 0; i < 7; ++i) *(u32x4*)(lds + (i * 8 + w) * 1024 + (lp * 16 + lr) * 16) = stg[i];
    } else {
#pragma unroll
        for (int i = 0; i < 7; ++i) { const int n = tid + 512 * i, e = n / 14, ch = n % 14; *(u32x4*)(lds + e * 240 + ch * 16) = stg[i]; }
    }
    { const int o0 = tid * 16, o1 = 8192 + tid * 16;
      *(u32x4*)(lds + K_XT + (o0 / 896) * 912 + (o0 % 896)) = xt0;
      if (tid < 384) *(u32x4*)(lds + K_XT + (o1 / 896) * 912 + (o1 % 896)) = xt1; }
    if (fw < 1344) {
        const u32x4 o = {ew[0] | ((unsigned)ew[1] << 16), ew[2] | ((unsigned)ew[3] << 16), ew[4] | ((unsigned)ew[5] << 16), ew[6] | ((unsigned)ew[7] << 16)};
        K0ST(o, (unsigned)(OFF_BW + (size_t)fw * FR + lane * 16));
    }
    if (w < 7) {
        char* scr = lds + K_SCR + w * 8448;
#pragma unroll
        for (int bb = 0; bb < 2; ++bb) {
#pragma unroll
            for (int k = 0; k < 8; ++k) *(u32x4*)(scr + lr * 528 + 64 * k + 16 * lp) = rw[bb][k];
            asm volatile("s_waitcnt lgkmcnt(0)" ::: "memory");
#pragma unroll
            for (int k = 0; k < 8; ++k) rw[bb][k] = *(const u32x4*)(scr + l15 * 528 + 64 * k + 16 * q4);
            asm volatile("s_waitcnt lgkmcnt(0)" ::: "memory");
        }
    }
    __syncthreads();
#pragma unroll
    for (int j = 0; j < 2; ++j) {
        const int tt = w + 8 * j;
        if (tt < 14) {
            const u32x4 v = *(const u32x4*)(lds + K_XT + l15 * 912 + kcol(tt, q4) * 2);
            K0ST(v, (unsigned)(((b & 1) ? OFF_BX : OFF_XF) + ((size_t)(b >> 1) * 14 + tt) * FR + lane * 16));
        }
    }
    if (w < 7) {
        auto ldcf = [&](int i, u32x4 (&cf)[8]) {
#pragma unroll
            for (int k = 0; k < 8; ++k) {
                if (mat == 0) cf[k] = *(const u32x4*)(lds + (i * 8 + k) * 1024 + lane * 16);
                else {
                    const int qq = l15 >> 2, pp = l15 & 3;
                    const int row0 = 32 * k + 8 * q4 + qq;
                    typedef __attribute__((ext_vector_type(4))) short v4s;
                    v4s lo = __builtin_amdgcn_ds_read_tr16_b64_v4i16((LAS v4s*)(lds + row0 * 240 + (16 * i + 4 * pp) * 2));
                    v4s hi = __builtin_amdgcn_ds_read_tr16_b64_v4i16((LAS v4s*)(lds + (row0 + 4) * 240 + (16 * i + 4 * pp) * 2));
                    u32x2 l2 = __builtin_bit_cast(u32x2, lo), h2 = __builtin_bit_cast(u32x2, hi);
                    cf[k] = (u32x4){l2.x, l2.y, h2.x, h2.y};
                }
            }
        };
        u32x4 cfa[8], cfb[8];
        auto tile = [&](int i, const u32x4 (&cf)[8]) {
            f32x4 a0 = {0, 0, 0, 0}, a1 = {0, 0, 0, 0};
#pragma unroll
            for (int k = 0; k < 8; ++k) { a0 = mfma32(rw[0][k], cf[k], a0); a1 = mfma32(rw[1][k], cf[k], a1); }
            const u32x4 o = pack8(a0, a1);
            if (mat == 0) {
                const int ct = 7 * cg + i;
                K0ST(o, (unsigned)(OFF_AF + ((size_t)(h * 28 + ct) * 14 + t) * FR + lane * 16));
            } else {
                const int beta = 14 * cg + 2 * i + (l15 >> 3);
                K0ST(o, (unsigned)(OFF_BF + ((size_t)((h >> 1) * 56 + beta) * 14 + t) * FR + (16 * q4 + 8 * (h & 1) + (l15 & 7)) * 16));
            }
        };
        ldcf(0, cfa);
#pragma unroll
        for (int i = 0; i < 7; i += 2) {
            if (i + 1 < 7) ldcf(i + 1, cfb);
            tile(i, cfa);
            if (i + 2 < 7) ldcf(i + 2, cfa);
            if (i + 1 < 7) tile(i + 1, cfb);
        }
    }
}

constexpr int SPIN_MAX = 200000;
DEVI unsigned* flagp(const Params& p, int idx) { return (unsigned*)(p.ws + OFF_FLAG) + idx; }
DEVI void signal(const Params& p, int idx) { __hip_atomic_fetch_add(flagp(p, idx), 1u, __ATOMIC_RELAXED, __HIP_MEMORY_SCOPE_AGENT); }
DEVI unsigned ldflag(const Params& p, int idx) { return __hip_atomic_load(flagp(p, idx), __ATOMIC_RELAXED, __HIP_MEMORY_SCOPE_AGENT); }
DEVI void waitflag(const Params& p, int idx, unsigned target, bool& dead, int sleep = 4) {
    int k = dead ? SPIN_MAX : 0;
    _Pragma("nounroll") for (; k < SPIN_MAX; ++k) {
        const unsigned v = __builtin_amdgcn_readfirstlane(ldflag(p, idx));
        if (v >= target) break;
        __builtin_amdgcn_s_sleep(4);
    }
    if (k >= SPIN_MAX) dead = true;
    MEMFENCE();
}

DEVI void waitall(const Params& p, int idx, int n, bool& dead) {
    int k = dead ? SPIN_MAX : 0;
    const int _l = (threadIdx.x & 63) & (n - 1);
    _Pragma("nounroll") for (; k < SPIN_MAX; ++k) {
        const unsigned v = ldflag(p, idx + _l);
        if (!__any(v == 0u)) break;
        __builtin_amdgcn_s_sleep(3);
    }
    if (k >= SPIN_MAX) dead = true;
    MEMFENCE();
}

DEVI void gemm_worker(const Params& p, int u, char* lds) {
    const int tid = threadIdx.x, lane = tid & 63, w = __builtin_amdgcn_readfirstlane(tid >> 6);
    const u32x4 ds = mkdesc(p.ws, 0x10000000u);
    const int isU = u >> 6, par = (u >> 2) & 1, v = ((((u & 3) | (((u & 63) >> 3) << 2))) << 1) | par;
    constexpr unsigned SEG_STRIDE = 16 * 28 * 1024;
    const int flag_idx = isU ? FLAG_U + ((v >> 1) & 3) : FLAG_GX + (v >> 2);
    u32x4 x0[4];
#pragma unroll
    for (int j = 0; j < 4; ++j) { const int f = (w + 8 * j < 28) ? w + 8 * j : w; x0[j] = *(const u32x4*)(p.ws + OFF_XF + ((size_t)par * 28 + f) * FR + lane * 16); }
    if (w < 7) {
        u32x4 wt[2][14];
        unsigned vo_out[2];
        const char* base;
        if (!isU) {
            const int h = v >> 2, tp = 7 * ((v >> 1) & 1) + w; base = p.ws + OFF_AF + (size_t)(h * 28 + 2 * tp) * 14 * FR;
            vo_out[0] = (unsigned)(OFF_GX + ((size_t)(h * 2 + 0) * 14 + tp) * FR + lane * 16);
            vo_out[1] = (unsigned)(OFF_GX + ((size_t)(h * 2 + 1) * 14 + tp) * FR + lane * 16);
        } else {
            const int hp = v >> 3, b0 = 14 * ((v >> 1) & 3) + 2 * w; base = p.ws + OFF_BF + (size_t)(hp * 56 + b0) * 14 * FR;
            vo_out[0] = (unsigned)(OFF_UF + ((size_t)(b0 + 0) * 8 + hp) * FR + lane * 16);
            vo_out[1] = (unsigned)(OFF_UF + ((size_t)(b0 + 1) * 8 + hp) * FR + lane * 16);
        }
#pragma unroll
        for (int a = 0; a < 2; ++a)
#pragma unroll
            for (int t = 0; t < 14; ++t) wt[a][t] = *(const u32x4*)(base + (size_t)(a * 14 + t) * FR + lane * 16);
        WAITVM0();
#pragma unroll
        for (int j = 0; j < 4; ++j) { const int f = w + 8 * j; if (f < 28) *(u32x4*)(lds + f * 1024 + lane * 16) = x0[j]; }
        bar();
        int buf = 0;
        _Pragma("nounroll") for (int s = par; s < NSEG; s += 2) {
            f32x4 acc[2][2];
#pragma unroll
            for (int a = 0; a < 2; ++a)
#pragma unroll
                for (int kt = 0; kt < 2; ++kt) acc[a][kt] = (f32x4){0, 0, 0, 0};
            const char* xb = lds + buf * 28672 + lane * 16;
            u32x4 xf[28];
#pragma unroll
            for (int k = 0; k < 14; ++k) xf[k] = *(const u32x4*)(xb + ((k & 1) * 14 + (k >> 1)) * 1024);
            SCHEDB();
            if (!isU) {
#pragma unroll
                for (int k = 0; k < 28; ++k) {
                    if (k + 14 < 28) xf[k + 14] = *(const u32x4*)(xb + (((k + 14) & 1) * 14 + ((k + 14) >> 1)) * 1024);
                    acc[0][k & 1] = mfma32(wt[0][k >> 1], xf[k], acc[0][k & 1]); acc[1][k & 1] = mfma32(wt[1][k >> 1], xf[k], acc[1][k & 1]);
                    SCHEDB();
                }
            } else {
#pragma unroll
                for (int k = 0; k < 28; ++k) {
                    if (k + 14 < 28) xf[k + 14] = *(const u32x4*)(xb + (((k + 14) & 1) * 14 + ((k + 14) >> 1)) * 1024);
                    acc[0][k & 1] = mfma32(xf[k], wt[0][k >> 1], acc[0][k & 1]); acc[1][k & 1] = mfma32(xf[k], wt[1][k >> 1], acc[1][k & 1]);
                    SCHEDB();
                }
            }
            u32x4 o0, o1;
            if (!isU) { o0 = pack8(acc[0][0], acc[1][0]); o1 = pack8(acc[0][1], acc[1][1]); }
            else { o0 = pack8(acc[0][0], acc[0][1]); o1 = pack8(acc[1][0], acc[1][1]); }
            const unsigned so = (unsigned)s * SEG_STRIDE;
            AST4(o0, vo_out[0], ds, so);
            AST4(o1, vo_out[1], ds, so);
            if (s < 2) AWAIT(0); else AWAIT(2);
            bar();
            buf ^= 1;
        }
        AWAIT(0);
        bar();
    } else {
        u32x4 nx[28];
#pragma unroll
        for (int f = 0; f < 28; ++f) nx[f] = (u32x4){0, 0, 0, 0};
        {
            const size_t o = (isU ? OFF_UF : OFF_GX) + (size_t)(lane & 7) * (1u << 20);
            if (lane < 8) { const unsigned t = *(const volatile unsigned*)(p.ws + o); asm volatile("" :: "v"(t)); }
        }
        const unsigned vo_x = (unsigned)(OFF_XF + lane * 16);
        auto issue = [&](int sg) {
#pragma unroll
            for (int f = 0; f < 28; ++f) ALD4PR(nx[f], vo_x + (unsigned)f * 1024u, ds, (unsigned)sg * 28672u);
        };
        WAITVM0();
#pragma unroll
        for (int j = 0; j < 4; ++j) { const int f = w + 8 * j; if (f < 28) *(u32x4*)(lds + f * 1024 + lane * 16) = x0[j]; }
        issue(par + 2);
        bar();
        int buf = 0;
        _Pragma("nounroll") for (int s = par; s < NSEG; s += 2) {
            AWAIT(0);
#pragma unroll
            for (int f = 0; f < 28; ++f) asm volatile("" : "+v"(nx[f]));
            SCHEDB();
#pragma unroll
            for (int f = 0; f < 28; ++f) *(u32x4*)(lds + (buf ^ 1) * 28672 + f * 1024 + lane * 16) = nx[f];
            SCHEDB();
            const int sn = (s + 4 < NSEG) ? s + 4 : s;
            issue(__builtin_amdgcn_readfirstlane(sn));
            bar();
            if (lane == 0) {
                if (s < 2) signal(p, flag_idx + s * 16);
                else if (s >= 4) signal(p, flag_idx + (s - 2) * 16);
            }
            buf ^= 1;
        }
        AWAIT(0);
        bar();
        if (lane == 0) signal(p, flag_idx + (NSEG - 2 + par) * 16);
    }
}

constexpr float SC2 = 0.0625f * 1.4426950408889634f;
constexpr int L_R = 0;
constexpr int L_RED1 = 7168;
constexpr int L_RED2 = L_RED1 + 4352;
constexpr int L_REDX = L_RED2 + 2432;
constexpr int L_XX = L_REDX + 8192;
constexpr int L_CFG = L_XX + 73728;

DEVI unsigned chunk_off(const Params& p, unsigned cfg, int s, int j) {
    const int pg = s >> 5;
    return (unsigned)p.comm_off + (unsigned)pg * (2u << 20) + (unsigned)(2 * (4 * (s & 31) + j) + ((cfg >> pg) & 1)) * 8192u;
}

DEVI unsigned ts_off(const Params& p, unsigned cfg, int s) { return (unsigned)p.comm_off + (4u << 20) + (unsigned)(2 * (s >> 6) + ((cfg >> 2) & 1)) * 8192u + (unsigned)(s & 63) * 128u; }
DEVI unsigned rt32() { return (unsigned)__builtin_amdgcn_s_memrealtime(); }
DEVI void wait_until(unsigned target) {
    const unsigned now = rt32();
    const int d = (int)(target - now);
    if (d <= 0) return;
    if (d > 2000) target = now + 2000u;
    _Pragma("nounroll") while ((int)(rt32() - target) < 0) { }
}
#define C2T 110
#define O1T 98
#define C2BOOT 260
#define O1BOOT 200
#define C2H 88
#define GDELAY 100
#define WPOS 0
#define SDLY 3
#define TSOFF 10
#define XHD 6
#define BULKD 4
#define CUP 40
#define XOFFT 0
#define DEXTRA 0
#define STARTD 60
DEVI void probe_cfg(const Params& p, rsrc_t rs, int idx) {
    const int pg = idx >> 1, par = idx & 1;
    unsigned best = 1u << 20;
    for (int k = 0; k < 4; ++k) {
        const unsigned off = (unsigned)p.comm_off + pg * (2u << 20) + (unsigned)((pg ? 2 * (4 * 31 + 1) : 0) + par) * 8192u + 512u * k;
        const unsigned long long c0 = __builtin_amdgcn_s_memtime();
        unsigned vv = bld1(rs, off, 0);
        asm volatile("s_waitcnt vmcnt(0)" : "+v"(vv) :: "memory");
        const unsigned long long c1 = __builtin_amdgcn_s_memtime();
        const unsigned dt = (unsigned)(c1 - c0);
        if (k > 0 && dt < best) best = dt;
    }
    if ((threadIdx.x & 63) == 0) bst1(rs, best + 1u, OFF_FLAG + (FLAG_CFG + idx) * 4, 0);
}
DEVI unsigned get_cfg(const Params& p, rsrc_t rs, char* lds) {
    const int tid = threadIdx.x, lane = tid & 63, w = __builtin_amdgcn_readfirstlane(tid >> 6);
    if (w == 7) {
        unsigned v = 0;
        for (int it = 0; it < SPIN_MAX; ++it) { v = bld1(rs, OFF_FLAG + (FLAG_CFG + (lane & 3)) * 4, 0); MEMFENCE(); if (!__any(v == 0u)) break; __builtin_amdgcn_s_sleep(2); }
        const unsigned l0 = __builtin_amdgcn_readlane(v, 0), l1 = __builtin_amdgcn_readlane(v, 1), l2 = __builtin_amdgcn_readlane(v, 2), l3 = __builtin_amdgcn_readlane(v, 3);
        unsigned cfg = (l1 < l0 ? 1u : 0u) | (l3 < l2 ? 2u : 0u);
        if (lane == 0) *(unsigned*)(lds + L_CFG) = cfg;
    }
    __syncthreads();
    return __builtin_amdgcn_readfirstlane(*(unsigned*)(lds + L_CFG));
}
DEVI unsigned start_sync(const Params& p, u32x4 ds, int r, char* lds) {
    const int tid = threadIdx.x, lane = tid & 63, w = __builtin_amdgcn_readfirstlane(tid >> 6);
    bar();
    if (w == 3) {
        const unsigned base = rt32();
        if (lane == 0) AST1(base | 1u, (unsigned)(OFF_FLAG + FLAG_RDY * 4) + (unsigned)r * 128u, ds, 0u);
        unsigned t0;
        if (r == 0) {
            unsigned v0 = 0, v1 = 0;
            const unsigned vo = (unsigned)(OFF_FLAG + FLAG_RDY * 4) + (unsigned)lane * 128u;
            for (int it = 0; it < SPIN_MAX; ++it) {
                ALD1R(v0, vo, ds, 0u); ALD1R(v1, vo, ds, 8192u);
                asm volatile("s_waitcnt vmcnt(0)" : "+v"(v0), "+v"(v1) :: "memory");
                if (!__any(v0 == 0u || v1 == 0u)) break;
            }
            t0 = (rt32() + STARTD) | 1u;
            if (lane == 0) AST1(t0, (unsigned)(OFF_FLAG + FLAG_T0 * 4), ds, 0u);
        } else {
            unsigned v = 0;
            const unsigned vo = (unsigned)(OFF_FLAG + FLAG_T0 * 4) + (unsigned)(lane & 0);
            for (int it = 0; it < SPIN_MAX; ++it) {
                ALD1R(v, vo, ds, 0u);
                asm volatile("s_waitcnt vmcnt(0)" : "+v"(v) :: "memory");
                v = (unsigned)__builtin_amdgcn_readfirstlane(v);
                if (v) break;
                __builtin_amdgcn_s_sleep(1);
            }
            t0 = v;
        }
        if (lane == 0) *(unsigned*)(lds + L_CFG + 4) = t0;
    }
    bar();
    return __builtin_amdgcn_readfirstlane(*(unsigned*)(lds + L_CFG + 4));
}

#define D1 0
#define D2 0
template <int DELAY> DEVI void pause() { if (DELAY > 0) __builtin_amdgcn_s_sleep(DELAY); }

#define EV(k)
#define STAMP(k)

constexpr int L_TS = L_CFG + 64;
constexpr int L_RF = L_CFG + 128;
constexpr int L_PU = L_CFG + 256;
constexpr int L_PX = L_PU + 8192;
constexpr int L_GX = L_CFG + 256;
#define WSPLIT 5
#define MINH 30
#define MINM 30
DEVI unsigned tmax(unsigned a, unsigned b) { return ((int)(a - b) > 0) ? a : b; }

DEVI void await_flag(u32x4 ds, unsigned vo, unsigned target, bool& dead) {
    int spins = 0; const int lim = dead ? 1 : SPIN_MAX; bool bad;
    _Pragma("nounroll") do {
        unsigned v; ALD1(v, vo, ds, 0u);
        asm volatile("s_waitcnt vmcnt(0)" : "+v"(v) :: "memory");
        bad = (unsigned)__builtin_amdgcn_readfirstlane(v) < target;
        if (bad) __builtin_amdgcn_s_sleep(2);
    } while (bad && ++spins < lim);
    if (bad) dead = true;
}
DEVI void await_all(u32x4 ds, unsigned vo, bool& dead) {
    int spins = 0; const int lim = dead ? 1 : SPIN_MAX; bool bad;
    _Pragma("nounroll") do {
        unsigned v; ALD1(v, vo, ds, 0u);
        asm volatile("s_waitcnt vmcnt(0)" : "+v"(v) :: "memory");
        bad = __any(v == 0u);
        if (bad) __builtin_amdgcn_s_sleep(2);
    } while (bad && ++spins < lim);
    if (bad) dead = true;
}
#define LAT0()
#define LAT1(k)
#define WT(k)
#define TA0(v)
#define TA1(v, k, cond)

#define PTA()
#define PTB(k)
template <bool V> struct BoolC { static constexpr bool value = V; };

DEVI void chain_gw(const Params& p, int r, char* lds, u32x4 ds, unsigned t_entry) {
    const rsrc_t rs = mkrsrc(p.ws, 0x10000000u);
    const int tid = threadIdx.x, lane = tid & 63, w = __builtin_amdgcn_readfirstlane(tid >> 6);
    const int col = lane & 15, q4 = lane >> 4;
    const int h = r / 7, c7 = r % 7, beta = r >> 1, rh = r & 1;
    u32x4 aw[2][2][4];
    const int nq = (w < 3) ? 2 : 1;
    const int qf0 = (c7 + 2 * w) % 7, qf1 = (c7 + 2 * w + nq - 1) % 7;
#pragma unroll
    for (int qi = 0; qi < 2; ++qi)
#pragma unroll
        for (int e = 0; e < 2; ++e)
#pragma unroll
            for (int tau = 0; tau < 4; ++tau) {
                const int q = qi ? qf1 : qf0;
                aw[qi][e][tau] = *(const u32x4*)(p.ws + OFF_AF + ((size_t)(h * 28 + 4 * c7 + tau) * 14 + 2 * q + e) * FR + lane * 16);
                if (qi == 1 && nq == 1) aw[qi][e][tau] = (u32x4){0, 0, 0, 0};
            }
    unsigned mE = (col < 8) ? 0xFFFFFFFFu : 0u, nE = ~mE;
    asm volatile("" : "+v"(mE), "+v"(nE));
    const unsigned cfg = get_cfg(p, rs, lds);
    WAITVM0();
    bool dead = false;
    const unsigned vo_m0 = (unsigned)(qf0 * 1024 + lane * 16), vo_m1 = (unsigned)(qf1 * 1024 + lane * 16);
    const unsigned vo_pp = (lane >= 32) ? (unsigned)(TS_OFF + (lane & 1) * 4) : (unsigned)((((lane >> 4) & 1) ? 16384 : 32768) + h * 512 + c7 * 64 + (lane & 15) * 4);
    const int npub = (r == 0) ? 34 : 32;
    const unsigned vo_mp = (unsigned)(beta * 128 + rh * 64 + (lane & 15) * 4);
    const unsigned vo_xm = (unsigned)(OFF_XM + ((size_t)h * 7 + c7) * 1024 + lane * 16);
    const unsigned vo_ts = (unsigned)(TS_OFF + (lane & 1) * 4);
    u32x4 R0 = {0,0,0,0}, R1 = {0,0,0,0};
    unsigned ts1 = 0;
    unsigned tm = 0, th = 0;
    volatile LAS unsigned* ldist = (volatile LAS unsigned*)((LAS char*)lds + L_RF + 4);
    bool dh_prev = false;
    LAS char* lbase = (LAS char*)lds;
    LAS char* lr0 = lbase + L_R + vo_m0; LAS char* lr1 = lbase + L_R + vo_m1; LAS char* lrc = lbase + L_R + c7 * 1024 + lane * 16;
    volatile LAS unsigned* rflag = (volatile LAS unsigned*)(lbase + L_RF + (lane & 0));
    volatile LAS unsigned* rflagw = (volatile LAS unsigned*)(lbase + (w == 0 ? L_RF : L_RF + 16 + 4 * (w & 3)) + (lane & 0));
    asm volatile("" : "+v"(lr0), "+v"(lr1), "+v"(lrc), "+v"(rflag), "+v"(rflagw));
    if (tid == 0) { *rflag = 0u; ldist[0] = 0u; ldist[1] = 0u; }
    tm = start_sync(p, ds, r, lds);
    __builtin_amdgcn_s_setprio(2);
    th = tm + O1T;
    auto step = [&](const int s, auto LASTC) {
        constexpr bool LAST = decltype(LASTC)::value;
        const unsigned oM = (unsigned)__builtin_amdgcn_readfirstlane((int)chunk_off(p, cfg, s, 0)), oMn = (unsigned)__builtin_amdgcn_readfirstlane((int)chunk_off(p, cfg, s + 1, 0));
        u32x4 gE, gO;
        unsigned sv1 = (unsigned)(s + 1); PIN(sv1);
        wait_until(tm);
        if (w == 0) EV(4);
        f32x4 acc[4];
        u32x4 Re1, Rc;
        unsigned mx, fl;
        auto ghalf1 = [&]() {
            ALD4R(R0, vo_m0, ds, oM);
            ALD4R(R1, vo_m1, ds, oM);
            asm volatile("s_waitcnt vmcnt(1)" : "+v"(R0) :: "memory");
            PTB(10);
            SCHEDB();
            u32x4 Re = R0 & mE;
            PIN(Re);
            SCHEDB();
            acc[0] = mfma32(aw[0][0][0], Re, (f32x4){0, 0, 0, 0});
            *(volatile LAS u32x4*)(lr0) = R0;
            SCHEDB();
            u32x4 Ro;
            acc[1] = mfma32(aw[0][0][1], Re, (f32x4){0, 0, 0, 0});
            Ro.x = R0.x & nE; Ro.y = R0.y & nE;
            SCHEDB();
            acc[2] = mfma32(aw[0][0][2], Re, (f32x4){0, 0, 0, 0});
            Ro.z = R0.z & nE; Ro.w = R0.w & nE;
            PIN(Ro);
            SCHEDB();
            acc[3] = mfma32(aw[0][0][3], Re, (f32x4){0, 0, 0, 0});
            mx = max(max(R0.x, R0.y), R0.z); mx = max(mx, R0.w);
            PIN(mx);
            SCHEDB();
            acc[0] = mfma32(aw[0][1][0], Ro, acc[0]);
            if (__builtin_expect(!__any(mx == POISON), 1)) *rflagw = sv1;
            SCHEDB();
            asm volatile("s_waitcnt vmcnt(0)" : "+v"(R1) :: "memory");
            PTB(11);
            SCHEDB();
            *(volatile LAS u32x4*)(lr1) = R1;
            SCHEDB();
            acc[1] = mfma32(aw[0][1][1], Ro, acc[1]);
            mx = max(max(mx, R1.x), R1.y); mx = max(max(mx, R1.z), R1.w);
            PIN(mx);
            SCHEDB();
            acc[2] = mfma32(aw[0][1][2], Ro, acc[2]);
            Re1.x = R1.x & mE; Re1.y = R1.y & mE;
            fl = *rflag;
            Rc = *(volatile LAS u32x4*)(lrc);
            SCHEDB();
            acc[3] = mfma32(aw[0][1][3], Ro, acc[3]);
            Re1.z = R1.z & mE; Re1.w = R1.w & mE;
            PIN(Re1);
            SCHEDB();
        };
        ghalf1();
        if (__builtin_expect(__any(mx == POISON), 0)) {
            int spins = 0; const int lim = dead ? 1 : SPIN_MAX; bool bad;
            ldist[0] = sv1;
            _Pragma("nounroll") do { ghalf1(); bad = __any(mx == POISON); } while (bad && ++spins < lim);
            if (bad) dead = true;
        }
        if (w == 0) EV(1);
        SCHEDB();
        u32x4 Rce, Rco, Ro1;
        acc[0] = mfma32(aw[1][0][0], Re1, acc[0]);
        Ro1.x = R1.x & nE; Ro1.y = R1.y & nE;
        SCHEDB();
        acc[1] = mfma32(aw[1][0][1], Re1, acc[1]);
        Ro1.z = R1.z & nE; Ro1.w = R1.w & nE;
        PIN(Ro1);
        SCHEDB();
        acc[2] = mfma32(aw[1][0][2], Re1, acc[2]);
        SCHEDB();
        acc[3] = mfma32(aw[1][0][3], Re1, acc[3]);
        SCHEDB();
        if (__builtin_expect((unsigned)__builtin_amdgcn_readfirstlane(fl) != (unsigned)(s + 1), 0)) {
            int g = 0;
            _Pragma("nounroll") do { fl = *rflag; Rc = *(volatile LAS u32x4*)(lrc); }
            while ((unsigned)__builtin_amdgcn_readfirstlane(fl) != (unsigned)(s + 1) && ++g < (1 << 22));
        }
        SCHEDB();
        acc[0] = mfma32(aw[1][1][0], Ro1, acc[0]);
        Rce.x = Rc.x & mE; Rce.y = Rc.y & mE;
        SCHEDB();
        acc[1] = mfma32(aw[1][1][1], Ro1, acc[1]);
        Rce.z = Rc.z & mE; Rce.w = Rc.w & mE;
        PIN(Rce);
        SCHEDB();
        acc[2] = mfma32(aw[1][1][2], Ro1, acc[2]);
        Rco.x = dppu<DPP_ROR8>(Rc.x) & mE; Rco.y = dppu<DPP_ROR8>(Rc.y) & mE;
        SCHEDB();
        acc[3] = mfma32(aw[1][1][3], Ro1, acc[3]);
        Rco.z = dppu<DPP_ROR8>(Rc.z) & mE; Rco.w = dppu<DPP_ROR8>(Rc.w) & mE;
        PIN(Rco);
        SCHEDB();
        gE = pack8(acc[0], acc[1]);
        f32x4 d2 = mfma32(gE, Rce, (f32x4){0, 0, 0, 0});
        gO = pack8(acc[2], acc[3]);
        d2 = mfma32(gO, Rco, d2);
        if (col < 8) {
            float* red = (float*)(lds + L_RED1) + (col * 4 + 2 * (q4 & 1)) * 20 + w * 4 + (q4 >> 1) * 2;
            *(f32x2*)(red) = (f32x2){d2[0], d2[1]};
            *(f32x2*)(red + 20) = (f32x2){d2[2], d2[3]};
        }
        unsigned tpre = 0;
        if (!LAST && r == 0 && w == 0) tpre = rt32();
        PTB(12);
        bar();
        PTB(13);
        if (!LAST && w == 0) {
            const float* red = (const float*)(lds + L_RED1) + (lane & 31) * 20;
            const f32x4 a = *(const f32x4*)(red), b2 = *(const f32x4*)(red + 4), c = *(const f32x4*)(red + 8), d = *(const f32x4*)(red + 12);
            const f32x4 t4 = (a + b2) + (c + d);
            unsigned o = nopoison(pk2(t4.x + t4.z, t4.y + t4.w));
            if (r == 0) {
                const bool dist0 = dh_prev || (unsigned)__builtin_amdgcn_readfirstlane(ldist[0]) == (unsigned)(s + 1);
                const unsigned tv = nopoison(tmax(th, tpre + 3u + MINH) + (unsigned)(C2H + (dist0 ? CUP : 0)) + ((lane & 1) ? (unsigned)O1T : 0u));
                o = (lane >= 32) ? tv : o;
            }
            if (lane < npub) AST1(o, vo_pp, ds, oM);
            PTB(14);
            EV(2);
            SCHEDB();
        }
        unsigned tb1 = 0, the = 0;
        if (!LAST && w == 1) { tb1 = rt32(); the = tmax(th, tb1 + MINH); }
#pragma unroll
        for (int it = 0; it < 2; ++it) {
            const char* pxb = lds + L_PX + lane * 16;
            const u32x4 x0 = *(const u32x4*)(pxb + (it * 2 + 0) * 1024), x1 = *(const u32x4*)(pxb + (it * 2 + 1) * 1024);
            f32x4 dx = mfma32(gE, x0, (f32x4){0, 0, 0, 0});
            dx = mfma32(gO, x1, dx);
            *(f32x4*)(lds + L_REDX + (2 * w + (q4 >> 1)) * 1024 + ((it * 2 + (q4 & 1)) * 16 + col) * 16) = dx;
        }
        if (!LAST && w == 1) {
            wait_until(the);
            ALD1R(ts1, vo_ts, ds, oM);
            asm volatile("s_waitcnt vmcnt(0)" : "+v"(ts1) :: "memory");
            if (__builtin_expect(__any(ts1 == POISON), 0)) {
                int spins = 0; const int lim = dead ? 1 : SPIN_MAX; bool bad;
                ldist[1] = sv1;
                _Pragma("nounroll") do {
                    ALD1R(ts1, vo_ts, ds, oM);
                    asm volatile("s_waitcnt vmcnt(0)" : "+v"(ts1) :: "memory");
                    bad = __any(ts1 == POISON);
                } while (bad && ++spins < lim);
                if (bad) dead = true;
            }
            if (lane < 2) *(unsigned*)(lds + L_TS + lane * 4) = ts1;
        }
        bar();
        if (!LAST && w == 3) {
            const float* red = (const float*)(lds + L_RED2) + (lane & 15) * 36 + ((lane >> 4) & 1) * 16;
            const f32x4 a = *(const f32x4*)(red), b2 = *(const f32x4*)(red + 4), c = *(const f32x4*)(red + 8), d = *(const f32x4*)(red + 12);
            const f32x4 t4 = (a + b2) + (c + d);
            float v0 = t4.x + t4.z, v1 = t4.y + t4.w;
            { float x0, x1; swap16(v0, x0, x1); v0 = x0 + x1; swap16(v1, x0, x1); v1 = x0 + x1; }
            const unsigned o = nopoison(pk2(v0, v1));
            if (lane < 16) AST1(o, vo_mp, ds, oMn);
            EV(0);
            SCHEDB();
        }
        const unsigned tb2 = rt32() - (w == 3 ? 9u : 0u);
        if (w == 1) EV(5);
        if (w == 2) {
            f32x4 t4 = *(const f32x4*)(lds + L_REDX + lane * 16);
#pragma unroll
            for (int pp = 1; pp < 8; ++pp) t4 += *(const f32x4*)(lds + L_REDX + pp * 1024 + lane * 16);
            const u32x4 xm = __builtin_bit_cast(u32x4, t4);
            AST4(xm, vo_xm, ds, (unsigned)s * (16u * 7u * 1024u));
        }
        if (!LAST) {
          const u32x2 t2 = *(const u32x2*)(lds + L_TS);
          dh_prev = (unsigned)__builtin_amdgcn_readfirstlane(ldist[1]) == (unsigned)(s + 1);
          tm = tmax((unsigned)__builtin_amdgcn_readfirstlane(t2.x), tb2 + MINM); th = (unsigned)__builtin_amdgcn_readfirstlane(t2.y);
          if (r == 0 && w == 1 && s >= 1) {
              __builtin_amdgcn_s_sleep(5);
              if (lane == 0) AST1(1u, (unsigned)(OFF_FLAG + FLAG_PROG * 4), ds, (unsigned)(s - 1) * 128u);
          }
        }
    };
    _Pragma("nounroll") for (int s = 0; s < NSEG - 1; ++s) step(s, BoolC<false>{});
    step(NSEG - 1, BoolC<true>{});
    AWAIT(0);
    bar();
    if (w == 2 && lane == 0) AST1(1u, (unsigned)(OFF_FLAG + (FLAG_DONE + r) * 4), ds, 0u);
}

DEVI void chain_bw(const Params& p, int r, char* lds, u32x4 ds) {
    const rsrc_t rs = mkrsrc(p.ws, 0x10000000u);
    const int tid = threadIdx.x, lane = tid & 63, w = __builtin_amdgcn_readfirstlane(tid >> 6), hw = w & 3;
    const int col = lane & 15, q4 = lane >> 4;
    const int c7 = r % 7, beta = r >> 1, rh = r & 1;
    u32x4 bw[2][14];
#pragma unroll
    for (int a = 0; a < 2; ++a)
#pragma unroll
        for (int t = 0; t < 14; ++t) bw[a][t] = *(const u32x4*)(p.ws + OFF_BF + ((size_t)((2 * hw + a) * 56 + beta) * 14 + t) * FR + lane * 16);
    u32x4 sel;
    { const int j = col & 7; const unsigned one = (j & 1) ? 0x3F800000u : 0x00003F80u; sel.x = (j >> 1) == 0 ? one : 0; sel.y = (j >> 1) == 1 ? one : 0; sel.z = (j >> 1) == 2 ? one : 0; sel.w = (j >> 1) == 3 ? one : 0; }
    const unsigned cfg = get_cfg(p, rs, lds);
    WAITVM0();
    bool dead = false;
    const int hh = col >> 2, i2 = col & 3, hB = 4 * hw + hh;
    const unsigned vo_pa = (unsigned)((rh ? 16384 : 32768) + hB * 512 + q4 * 64 + i2 * 16);
    const unsigned vo_pb = (q4 == 3) ? OOB : vo_pa + 256u;
    const unsigned vo_stat = (unsigned)(STATS_OFF + rh * 512 + (hB * 4 + i2) * 4);
    const unsigned vo_ex = (unsigned)(49152 + rh * 4096 + hB * 256 + q4 * 64 + i2 * 16);
    const unsigned vo_u0 = (unsigned)(OFF_UF + ((size_t)beta * 8 + 2 * hw) * FR + lane * 16), vo_u1 = vo_u0 + 1024;
    const unsigned vo_x = (unsigned)(OFF_XF + (size_t)((hw >> 1) * 14 + 2 * c7 + (hw & 1)) * FR + lane * 16);
    const unsigned vo_fu = (unsigned)(OFF_FLAG + (FLAG_U + beta / 14) * 4);
    const unsigned vo_wc = (rh == 0 && lane < 16) ? (unsigned)(OFF_WC + ((size_t)beta * 8 + 2 * hw) * 256 + col * 16) : OOB;
    const bool diag = (col >> 3) == (q4 >> 1);
    const bool lowh = hh < 2, outv = (hh & 1) == (q4 >> 1);
    u32x4 un0 = {0,0,0,0}, un1 = {0,0,0,0}, px = {0,0,0,0};
    unsigned fu = 0;
    u32x4 pa = {0,0,0,0}, pb = {0,0,0,0}, ex = {0,0,0,0}; unsigned st = 0;
    unsigned th = 0;
    volatile LAS unsigned* ldist = (volatile LAS unsigned*)((LAS char*)lds + L_RF + 4);
    await_flag(ds, vo_fu, 8, dead);
    ALD4R(un0, vo_u0, ds, 0u); ALD4R(un1, vo_u1, ds, 0u); ALD4PR(px, vo_x, ds, 0u);
    ALD1R(fu, vo_fu, ds, 64u);
    th = start_sync(p, ds, r, lds) + O1T;
    auto step = [&](const int s, auto LASTC) {
        constexpr bool LAST = decltype(LASTC)::value;
        const unsigned oM = chunk_off(p, cfg, s, 0);
        unsigned sv1 = (unsigned)(s + 1); PIN(sv1);
        asm volatile("s_waitcnt vmcnt(0)" : "+v"(un0), "+v"(un1), "+v"(px), "+v"(fu) :: "memory");
        SCHEDB();
        u32x4 uf0 = un0, uf1 = un1;
        asm volatile("" : "+v"(uf0), "+v"(uf1));
        const unsigned fu_s = (unsigned)__builtin_amdgcn_readfirstlane(fu);
        *(u32x4*)(lds + L_PX + hw * 1024 + lane * 16) = px;
        if (s + 1 < NSEG - 1 && fu_s < 8u) await_flag(ds, vo_fu + (unsigned)(s + 1) * 64u, 8, dead);
        bar();
        const unsigned tb1 = rt32();
        SCHEDB();
        u32x4 Rq[7];
#pragma unroll
        for (int q = 0; q < 7; ++q) Rq[q] = *(const u32x4*)(lds + L_R + q * 1024 + lane * 16);
        SCHEDB();
        const unsigned the = tmax(th, tb1 + MINH);
        f32x4 ae[2] = {{0,0,0,0},{0,0,0,0}}, ao[2] = {{0,0,0,0},{0,0,0,0}};
#pragma unroll
        for (int q = 0; q < WSPLIT; ++q)
#pragma unroll
            for (int a = 0; a < 2; ++a) { ae[a] = mfma32(Rq[q], bw[a][2 * q], ae[a]); ao[a] = mfma32(Rq[q], bw[a][2 * q + 1], ao[a]); }
        SCHEDB();
        if (!LAST) {
            wait_until(the);
            if (w == 6) EV(6);
            ALD4R(pa, vo_pa, ds, oM);
            ALD4R(pb, vo_pb, ds, oM);
            ALD1R(st, vo_stat, ds, oM);
            ALD4R(ex, vo_ex, ds, oM);
            SCHEDB();
        }
#pragma unroll
        for (int q = WSPLIT; q < 7; ++q)
#pragma unroll
            for (int a = 0; a < 2; ++a) { ae[a] = mfma32(Rq[q], bw[a][2 * q], ae[a]); ao[a] = mfma32(Rq[q], bw[a][2 * q + 1], ao[a]); }
        u32x2 wop[2]; u32x4 wst[2];
#pragma unroll
        for (int a = 0; a < 2; ++a) {
            float wv[4];
#pragma unroll
            for (int rr = 0; rr < 4; ++rr) { const float x = (lane < 32) ? ae[a][rr] : ao[a][rr]; float u, v; swap32(x, u, v); wv[rr] = u + v; }
            const u32x2 wp = {pk2(wv[0], wv[1]), pk2(wv[2], wv[3])};
            wop[a].x = diag ? wp.x : 0u; wop[a].y = diag ? wp.y : 0u;
            unsigned a0, b0, a1, b1; uswap16(wp.x, a0, b0); uswap16(wp.y, a1, b1); wst[a] = (u32x4){a0, a1, b0, b1};
        }
        asm volatile("" : "+v"(wop[0]), "+v"(wop[1]), "+v"(wst[0]), "+v"(wst[1]));
        SCHEDB();
        if (!LAST) {
            f32x4 ro;
            auto bstage = [&]() {
                f32x4 sm = mfma32(sel, pa, (f32x4){0, 0, 0, 0});
                sm = mfma32(sel, pb, sm);
                PIN(sm);
                SCHEDB();
                f32x4 dx0 = mfma32(uf0, ex, (f32x4){0, 0, 0, 0}), dx1 = mfma32(uf1, ex, (f32x4){0, 0, 0, 0});
                f32x4 sx4 = mfma32((u32x4){0x3F803F80u, 0x3F803F80u, 0x3F803F80u, 0x3F803F80u}, ex, (f32x4){0, 0, 0, 0});
                PIN(dx0); PIN(dx1); PIN(sx4);
                const float mxs = __uint_as_float(st), sxs = sx4[0];
                SCHEDB();
                float ml = fmx(fmx(sm[0], sm[1]), fmx(sm[2], sm[3]));
                { float a, b2; swap16(ml, a, b2); ml = fmx(fmx(a, b2), mxs); }
                const float nM = -ml * SC2;
                float e[4];
#pragma unroll
                for (int rr = 0; rr < 4; ++rr) e[rr] = __builtin_amdgcn_exp2f(__builtin_fmaf(sm[rr], SC2, nM));
                float al = __builtin_amdgcn_exp2f(__builtin_fmaf(mxs, SC2, nM));
                PIN(al);
                const u32x2 pp = {pk2(e[0], e[1]), pk2(e[2], e[3])};
                const f32x4 r0 = mfma16(wop[0], pp, dx0 * al), r1 = mfma16(wop[1], pp, dx1 * al);
                float ls = (e[0] + e[1]) + (e[2] + e[3]);
                { float a, b2; swap16(ls, a, b2); ls = a + b2; }
                const float inv = __builtin_amdgcn_rcpf(__builtin_fmaf(al, sxs, ls));
#pragma unroll
                for (int rr = 0; rr < 4; ++rr) ro[rr] = (lowh ? r0[rr] : r1[rr]) * inv;
                if (outv) {
                    float* red = (float*)(lds + L_RED2) + (i2 * 4 + 2 * (q4 & 1)) * 36 + hB * 2;
                    *(f32x2*)(red) = (f32x2){ro[0], ro[1]};
                    *(f32x2*)(red + 36) = (f32x2){ro[2], ro[3]};
                }
                SCHEDB();
            };
            {
                const unsigned sn = (unsigned)(s + 1);
                ALD4R(un0, vo_u0, ds, sn * (56u * 8u * 1024u));
                ALD4R(un1, vo_u1, ds, sn * (56u * 8u * 1024u));
                ALD4PR(px, vo_x, ds, sn * 28672u);
                ALD1R(fu, vo_fu, ds, (sn + 1u) * 64u);
            }
            PTB(0);
            asm volatile("s_waitcnt vmcnt(4)" : "+v"(pa), "+v"(pb), "+v"(st), "+v"(ex) :: "memory");
            PTB(1);
            SCHEDB();
            bstage();
            if (__builtin_expect(__any(max(max(max4(pa), max4(pb)), max(max4(ex), st)) == POISON), 0)) {
              int spins = 0; const int lim = dead ? 1 : SPIN_MAX; bool bad;
              ldist[1] = sv1;
              _Pragma("nounroll") do {
                ALD4R(pa, vo_pa, ds, oM);
                ALD4R(pb, vo_pb, ds, oM);
                ALD1R(st, vo_stat, ds, oM);
                ALD4R(ex, vo_ex, ds, oM);
                asm volatile("s_waitcnt vmcnt(0)" : "+v"(pa), "+v"(pb), "+v"(st), "+v"(ex) :: "memory");
                bad = __any(max(max(max4(pa), max4(pb)), max(max4(ex), st)) == POISON);
              } while (bad && ++spins < lim);
              if (bad) dead = true;
              bstage();
            }
            if (w == 7) EV(3);
            PTB(3);
        }
        bar();
        if (!LAST) PTB(4);
        if (LAST || WPOS != 1) { AST4(wst[0], vo_wc, ds, (unsigned)s * (56u * 8u * 256u)); AST4(wst[1], vo_wc + 256u, ds, (unsigned)s * (56u * 8u * 256u)); }
        if (!LAST) th = (unsigned)__builtin_amdgcn_readfirstlane(*(const unsigned*)(lds + L_TS + 4));
    };
    _Pragma("nounroll") for (int s = 0; s < NSEG - 1; ++s) step(s, BoolC<false>{});
    step(NSEG - 1, BoolC<true>{});
    AWAIT(0);
    bar();
}

DEVI void pretouch(const Params& p) {
    const int lane = threadIdx.x & 63;
    const unsigned off = (unsigned)lane * (2u << 20);
    if (off < (unsigned)p.comm_off + (4u << 20)) { const unsigned v = *(const volatile unsigned*)(p.ws + off); asm volatile("" :: "v"(v)); }
}
DEVI void chain_g(const Params& p, int r, char* lds) {
    const int w = __builtin_amdgcn_readfirstlane(threadIdx.x >> 6);
    const u32x4 ds = mkdesc(p.ws, 0x10000000u);
    const unsigned t_entry = rt32();
    if (w == 3) pretouch(p);
    __builtin_amdgcn_s_sleep(GDELAY);
    if (w < 4) chain_gw(p, r, lds, ds, t_entry); else chain_bw(p, r, lds, ds);
}

DEVI void x_finalize(rsrc_t rs, f32x4 sm, f32x4 s0, f32x4 s1, int s, int h, int it, int lane) {
    const int col = lane & 15, q4 = lane >> 4;
    const float NEG = -3.0e38f;
    float v0[4], v1[4], vm[4];
#pragma unroll
    for (int rr = 0; rr < 4; ++rr) {
        const bool causal = (4 * q4 + rr) <= col;
        v0[rr] = (it == 1 || causal) ? s0[rr] : NEG;
        v1[rr] = (it == 1 && causal) ? s1[rr] : NEG;
        vm[rr] = (q4 < 2) ? sm[rr] : NEG;
    }
    float m = NEG;
#pragma unroll
    for (int rr = 0; rr < 4; ++rr) m = fmaxf(m, fmaxf(fmaxf(v0[rr], v1[rr]), vm[rr]));
    { float a, b2; swap16(m, a, b2); m = fmaxf(a, b2); swap32(m, a, b2); m = fmaxf(a, b2); }
    const float nM = -m * SC2;
    float l = 0;
#pragma unroll
    for (int rr = 0; rr < 4; ++rr) {
        v0[rr] = (v0[rr] == NEG) ? 0.f : __builtin_amdgcn_exp2f(__builtin_fmaf(v0[rr], SC2, nM));
        v1[rr] = (v1[rr] == NEG) ? 0.f : __builtin_amdgcn_exp2f(__builtin_fmaf(v1[rr], SC2, nM));
        vm[rr] = (vm[rr] == NEG) ? 0.f : __builtin_amdgcn_exp2f(__builtin_fmaf(vm[rr], SC2, nM));
        l += v0[rr] + v1[rr] + vm[rr];
    }
    { float a, b2; swap16(l, a, b2); l = a + b2; swap32(l, a, b2); l = a + b2; }
    const float inv = __builtin_amdgcn_rcpf(l);
    const u32x4 px = {pk2(v0[0] * inv, v0[1] * inv), pk2(v0[2] * inv, v0[3] * inv), pk2(v1[0] * inv, v1[1] * inv), pk2(v1[2] * inv, v1[3] * inv)};
    bst4(rs, px, (unsigned)(OFF_PX + (((size_t)s * 16 + h) * 2 + it) * FR + lane * 16), 0);
    if (q4 < 2) { const u32x2 pm = {pk2(vm[0] * inv, vm[1] * inv), pk2(vm[2] * inv, vm[3] * inv)};
        bst2(rs, pm, (unsigned)(OFF_PM + (((size_t)s * 8 + (h >> 1)) * 2 + it) * 512 + (col + 16 * (2 * (h & 1) + q4)) * 8), 0); }
}

DEVI void chain_x(const Params& p, int h, char* lds) {
    const int tid = threadIdx.x, lane = tid & 63, w = __builtin_amdgcn_readfirstlane(tid >> 6);
    const int col = lane & 15, q4 = lane >> 4;
    const rsrc_t rs = mkrsrc(p.ws, 0x10000000u);
    const u32x4 ds = mkdesc(p.ws, 0x10000000u);
    if (w == 0 && (h & 3) == 0) probe_cfg(p, rs, h >> 2);
    const unsigned cfg = get_cfg(p, rs, lds);
    WAITVM0();
    bool dead = false;
    if (w < 4) {
        const unsigned mE = (col < 8) ? 0xFFFFFFFFu : 0u;
        const int nq = (w < 3) ? 2 : 1;
        const unsigned vo_m0 = (unsigned)(2 * w * 1024 + lane * 16), vo_m1 = (unsigned)((2 * w + nq - 1) * 1024 + lane * 16);
        const unsigned vo_e = (col < 8) ? (unsigned)(49152 + ((col >> 2) & 1) * 4096 + h * 256 + q4 * 64 + (col & 3) * 16)
                            : ((lane == 8 || lane == 12) ? (unsigned)(STATS_OFF + ((lane >> 2) & 1) * 512 + h * 16) : OOB);
        u32x4 R0 = {0,0,0,0}, R1 = {0,0,0,0};
        unsigned tmx = start_sync(p, ds, 112 + h, lds);
        unsigned thx = tmx + O1T;
        __builtin_amdgcn_s_setprio(3);
        bar();
        for (int s = 0; s < NSEG; ++s) {
            const unsigned oM = chunk_off(p, cfg, s, 0), oE = oM + 3u * 16384u;
            if (s < NSEG - 1) {
                u32x4 g[2][2][2];
#pragma unroll
                for (int qi = 0; qi < 2; ++qi)
#pragma unroll
                    for (int e = 0; e < 2; ++e)
#pragma unroll
                        for (int k = 0; k < 2; ++k) g[qi][e][k] = *(const u32x4*)(lds + L_GX + (s & 1) * 28672 + (k * 14 + 2 * ((w < 3) ? 2 * w + qi : 2 * w) + e) * 1024 + lane * 16);
                if (s > 0) {
                    wait_until(thx);
                    unsigned t1 = 0; int spins = 0; const int lim = dead ? 1 : SPIN_MAX; bool bad;
                    const unsigned oT = chunk_off(p, cfg, s - 1, 0); const unsigned vts = (unsigned)(TS_OFF + (lane & 1) * 4);
                    _Pragma("nounroll") do {
                        ALD1R(t1, vts, ds, oT);
                        asm volatile("s_waitcnt vmcnt(0)" : "+v"(t1) :: "memory");
                        bad = __any(t1 == POISON);
                        if (bad) __builtin_amdgcn_s_sleep(4);
                    } while (bad && ++spins < lim);
                    if (bad) dead = true;
                    thx = (unsigned)__builtin_amdgcn_readlane(t1, 1);
                    tmx = (unsigned)__builtin_amdgcn_readlane(t1, 0);
                }
                wait_until(tmx + XOFFT);
                { int spins = 0; const int lim = dead ? 1 : SPIN_MAX; bool bad;
                  _Pragma("nounroll") do {
                    ALD4R(R0, vo_m0, ds, oM);
                    ALD4R(R1, vo_m1, ds, oM);
                    asm volatile("s_waitcnt vmcnt(0)" : "+v"(R0), "+v"(R1) :: "memory");
                    bad = __any(max(max4(R0), max4(R1)) == POISON);
                  } while (bad && ++spins < lim);
                  if (bad) dead = true; }
                f32x4 d[2] = {{0,0,0,0},{0,0,0,0}};
#pragma unroll
                for (int kt = 0; kt < 2; ++kt) { d[kt] = mfma32(g[0][0][kt], R0 & mE, d[kt]); d[kt] = mfma32(g[0][1][kt], R0 & ~mE, d[kt]); }
                if (nq == 2) {
#pragma unroll
                    for (int kt = 0; kt < 2; ++kt) { d[kt] = mfma32(g[1][0][kt], R1 & mE, d[kt]); d[kt] = mfma32(g[1][1][kt], R1 & ~mE, d[kt]); }
                }
                f32x4 o;
#pragma unroll
                for (int rr = 0; rr < 4; ++rr) {
                    const float a = d[0][rr] + dppf<DPP_ROR8>(d[0][rr]), b2 = d[1][rr] + dppf<DPP_ROR8>(d[1][rr]);
                    o[rr] = (col < 8) ? a : b2;
                }
                *(f32x4*)(lds + L_REDX + w * 1024 + lane * 16) = o;
            }
            bar();
            if (w == 0 && s < NSEG - 1) {
                f32x4 t4 = *(const f32x4*)(lds + L_REDX + lane * 16);
#pragma unroll
                for (int pp = 1; pp < 4; ++pp) t4 += *(const f32x4*)(lds + L_REDX + pp * 1024 + lane * 16);
                float m = fmx(fmx(t4.x, t4.y), fmx(t4.z, t4.w));
                m = fmx(m, dppf<DPP_ROR8>(m));
                { float a, b2; swap16(m, a, b2); m = fmx(a, b2); swap32(m, a, b2); m = fmx(a, b2); }
                const unsigned mb = nopoison(__float_as_uint(m));
                u32x4 ms = {dppu<0x00>(mb), dppu<0x55>(mb), dppu<0xAA>(mb), dppu<0xFF>(mb)};
                const float nM = -m * SC2;
                float e[4];
#pragma unroll
                for (int rr = 0; rr < 4; ++rr) e[rr] = __builtin_amdgcn_exp2f(__builtin_fmaf(t4[rr], SC2, nM));
                const unsigned o0 = nopoison(pk2(e[0], e[1])), o1 = nopoison(pk2(e[2], e[3]));
                const unsigned o2 = dppu<DPP_ROR8>(o0), o3 = dppu<DPP_ROR8>(o1);
                const u32x4 o = {col < 8 ? o0 : ms.x, col < 8 ? o1 : ms.y, col < 8 ? o2 : ms.z, col < 8 ? o3 : ms.w};
                AST4(o, vo_e, ds, oM);
            }
        }
        bar();
    } else {
        const int hw = w - 4;
        u32x4 gb[7];
#pragma unroll
        for (int k = 0; k < 7; ++k) gb[k] = (u32x4){0, 0, 0, 0};
        const unsigned vo_fg = (unsigned)(OFF_FLAG + (FLAG_GX + h) * 4);
        const unsigned vo_g = (unsigned)(OFF_GX + ((size_t)h * 28 + hw * 7) * FR + lane * 16);
        auto issue = [&](int sg) {
#pragma unroll
            for (int k = 0; k < 7; ++k) ALD4R(gb[k], vo_g + (unsigned)k * 1024u, ds, (unsigned)sg * (16u * 28u * 1024u));
        };
        auto put = [&](int sg) {
            asm volatile("s_waitcnt vmcnt(0)" : "+v"(gb[0]), "+v"(gb[1]), "+v"(gb[2]), "+v"(gb[3]), "+v"(gb[4]), "+v"(gb[5]), "+v"(gb[6]) :: "memory");
            SCHEDB();
#pragma unroll
            for (int k = 0; k < 7; ++k) *(u32x4*)(lds + L_GX + (sg & 1) * 28672 + (hw * 7 + k) * 1024 + lane * 16) = gb[k];
            SCHEDB();
        };
        await_flag(ds, vo_fg, 2, dead);
        issue(0);
        (void)start_sync(p, ds, 112 + h, lds);
        put(0);
        await_flag(ds, vo_fg + 64u, 2, dead);
        issue(1);
        bar();
        _Pragma("nounroll") for (int s = 0; s < NSEG; ++s) {
            __builtin_amdgcn_s_sleep(XHD);
            put(s + 1);
            const int sn = (s + 2 < NSEG) ? s + 2 : NSEG - 1;
            await_flag(ds, vo_fg + (unsigned)sn * 64u, 2, dead);
            issue(__builtin_amdgcn_readfirstlane(sn));
            bar();
        }
        AWAIT(0);
        bar();
    }
}

DEVI void wait_prog(const Params& p, int s, bool& dead) {
    const int w = __builtin_amdgcn_readfirstlane(threadIdx.x >> 6), lane = threadIdx.x & 63;
    if (w == 0) {
        const unsigned t0 = __builtin_amdgcn_readfirstlane(ldflag(p, FLAG_T0));
        if (t0) {
            const unsigned target = t0 + (unsigned)((s <= NSEG - 3) ? (s + 2) * (O1T + C2H) + 10 : (NSEG - 1) * (O1T + C2H) + 90);
            int g = 0;
            _Pragma("nounroll") while ((int)(rt32() - target) < 0 && ++g < 20000) __builtin_amdgcn_s_sleep(8);
        }
        int k = dead ? SPIN_MAX : 0;
        _Pragma("nounroll") for (; k < SPIN_MAX; ++k) {
            unsigned v;
            if (s <= NSEG - 3) v = ldflag(p, FLAG_PROG + s * 32);
            else { const unsigned a = ldflag(p, FLAG_DONE + lane), b2 = ldflag(p, FLAG_DONE + (lane < 48 ? 64 + lane : 0)); v = a & b2; }
            if (!__any(v == 0u)) break;
            if (s <= NSEG - 4) __builtin_amdgcn_s_sleep(8); else __builtin_amdgcn_s_sleep(2);
        }
        if (k >= SPIN_MAX) dead = true;
    }
    __syncthreads();
}

DEVI void fin_unit(const Params& p, int s, int half, char* lds, bool& dead) {
    const int tid = threadIdx.x, lane = tid & 63, w = __builtin_amdgcn_readfirstlane(tid >> 6);
    const int h = 8 * half + w;
    const rsrc_t rs = mkrsrc(p.ws, 0x10000000u);
    __syncthreads();
    waitflag(p, FLAG_GX + s * 16 + h, 2, dead);
    u32x4 xs[4], g[28];
#pragma unroll
    for (int j = 0; j < 4; ++j) { const int f = (w + 8 * j < 28) ? w + 8 * j : w; xs[j] = *(const u32x4*)(p.ws + OFF_XF + ((size_t)s * 28 + f) * FR + lane * 16); }
#pragma unroll
    for (int f = 0; f < 28; ++f) g[f] = bld4(rs, (unsigned)(OFF_GX + (((size_t)s * 16 + h) * 28 + f) * FR + lane * 16), 0);
#pragma unroll
    for (int j = 0; j < 4; ++j) { const int f = w + 8 * j; if (f < 28) *(u32x4*)(lds + f * 1024 + lane * 16) = xs[j]; }
    __syncthreads();
    f32x4 xx[3] = {{0,0,0,0},{0,0,0,0},{0,0,0,0}};
#pragma unroll
    for (int t = 0; t < 14; ++t) {
        const u32x4 x0 = *(const u32x4*)(lds + t * 1024 + lane * 16), x1 = *(const u32x4*)(lds + (14 + t) * 1024 + lane * 16);
        xx[0] = mfma32(g[t], x0, xx[0]);
        xx[1] = mfma32(g[t], x1, xx[1]);
        xx[2] = mfma32(g[14 + t], x1, xx[2]);
    }
    wait_prog(p, s, dead);
    f32x4 sm = {0, 0, 0, 0};
    {
        u32x4 xm[7];
#pragma unroll
        for (int c = 0; c < 7; ++c) xm[c] = bld4(rs, (unsigned)(OFF_XM + (((size_t)s * 16 + h) * 7 + c) * 1024 + lane * 16), 0);
#pragma unroll
        for (int c = 0; c < 7; ++c) sm += __builtin_bit_cast(f32x4, xm[c]);
    }
    f32x4 sm1;
#pragma unroll
    for (int rr = 0; rr < 4; ++rr) { float lo, hi; swap32(sm[rr], lo, hi); sm[rr] = lo; sm1[rr] = hi; }
    x_finalize(rs, sm, xx[0], xx[0], s, h, 0, lane);
    x_finalize(rs, sm1, xx[1], xx[2], s, h, 1, lane);
    WAITVM0();
    __syncthreads();
    if (tid == 0) bst1(rs, 1u, (unsigned)(OFF_FLAG + (FLAG_P + s * 32 + half) * 4), 0);
}

DEVI void out_unit(const Params& p, int s, int bg, char* lds, bool& dead) {
    const int tid = threadIdx.x, lane = tid & 63, w = __builtin_amdgcn_readfirstlane(tid >> 6);
    const int col = lane & 15, q4 = lane >> 4;
    const rsrc_t rs = mkrsrc(p.ws, 0x10000000u);
    const int beta = 8 * bg + w;
    constexpr int O_PX = 0, O_PM = 32768, O_W = 40960;
    waitflag(p, FLAG_U + s * 16 + beta / 14, 8, dead);
    u32x4 uf[8];
#pragma unroll
    for (int hp = 0; hp < 8; ++hp) uf[hp] = bld4(rs, (unsigned)(OFF_UF + (((size_t)s * 56 + beta) * 8 + hp) * FR + lane * 16), 0);
    wait_prog(p, s, dead);
    u32x4 wl[2];
#pragma unroll
    for (int j = 0; j < 2; ++j) wl[j] = bld4(rs, (unsigned)(OFF_WC + ((size_t)s * 56 + 8 * bg) * 2048 + (2 * w + j) * 1024 + lane * 16), 0);
    waitall(p, FLAG_P + s * 32, 2, dead);
    u32x4 pl[4], pml;
#pragma unroll
    for (int j = 0; j < 4; ++j) pl[j] = bld4(rs, (unsigned)(OFF_PX + ((size_t)s * 32 + 4 * w + j) * FR + lane * 16), 0);
    pml = bld4(rs, (unsigned)(OFF_PM + (size_t)s * 8192 + w * 1024 + lane * 16), 0);
#pragma unroll
    for (int j = 0; j < 2; ++j) *(u32x4*)(lds + O_W + (2 * w + j) * 1024 + lane * 16) = wl[j];
#pragma unroll
    for (int j = 0; j < 4; ++j) *(u32x4*)(lds + O_PX + (4 * w + j) * 1024 + lane * 16) = pl[j];
    *(u32x4*)(lds + O_PM + w * 1024 + lane * 16) = pml;
    __syncthreads();
    const bool diag = (col >> 3) == (q4 >> 1);
    u32x2 wv[8];
#pragma unroll
    for (int hp = 0; hp < 8; ++hp) { const u32x2 t = *(const u32x2*)(lds + O_W + (w * 8 + hp) * 256 + col * 16 + (q4 & 1) * 8); wv[hp] = diag ? t : (u32x2){0u, 0u}; }
#pragma unroll
    for (int it = 0; it < 2; ++it) {
        f32x4 aa = {0,0,0,0}, ab = {0,0,0,0}, am = {0,0,0,0};
#pragma unroll
        for (int hp = 0; hp < 8; ++hp) {
            const u32x4 pa = *(const u32x4*)(lds + O_PX + ((2 * hp) * 2 + it) * 1024 + lane * 16), pb = *(const u32x4*)(lds + O_PX + ((2 * hp + 1) * 2 + it) * 1024 + lane * 16);
            const u32x2 pm = *(const u32x2*)(lds + O_PM + (hp * 2 + it) * 512 + lane * 8);
            aa = mfma32(uf[hp], pa, aa);
            ab = mfma32(uf[hp], pb, ab);
            am = mfma16(wv[hp], pm, am);
        }
        float v[4];
#pragma unroll
        for (int rr = 0; rr < 4; ++rr) { const float x = ((q4 >> 1) == 0 ? aa[rr] : ab[rr]) + am[rr]; float a, b2; swap32(x, a, b2); v[rr] = a + b2; }
        if (lane < 32) { const u32x2 o = {pk2(v[0], v[1]), pk2(v[2], v[3])};
            *(u32x2*)(p.out + (size_t)(32 * s + 16 * it + col) * OUTW + 8 * beta + 4 * q4) = o; }
    }
}

DEVI float phi(float t) { return (t > 0.f ? t + 1.f : __builtin_amdgcn_exp2f(t * 1.4426950408889634f)) + 1e-6f; }
DEVI float rbf(float x) { return bf_lo(pk2(x, 0.f)); }

DEVI void b1_unit(const Params& p, int rb, int hp, char* lds) {
    const int tid = threadIdx.x, lane = tid & 63, w = __builtin_amdgcn_readfirstlane(tid >> 6);
    const int col = lane & 15, q4 = lane >> 4;
    const rsrc_t rs = mkrsrc(p.ws, 0x10000000u);
    const int h = 2 * hp + (w & 1), rt0 = rb * 8 + (w >> 1) * 2;
    u32x4 xr[2][14], stg[11];
    auto wload = [&](int c) {
#pragma unroll
        for (int j = 0; j < 11; ++j) {
            const int i = (w + 8 * j < 84) ? w + 8 * j : w, tt = i % 7, g = i / 7, tile = g & 1, mat = (g >> 1) % 3, hh = g / 6;
            stg[j] = *(const u32x4*)(p.ws + OFF_BW + ((size_t)((mat * 16 + 2 * hp + hh) * 2 + tile) * 14 + 7 * c + tt) * FR + lane * 16);
        }
    };
    auto wput = [&]() {
#pragma unroll
        for (int j = 0; j < 11; ++j) if (w + 8 * j < 84) *(u32x4*)(lds + (w + 8 * j) * 1024 + lane * 16) = stg[j];
    };
    wload(0);
#pragma unroll
    for (int a = 0; a < 2; ++a)
#pragma unroll
        for (int t = 0; t < 14; ++t) xr[a][t] = *(const u32x4*)(p.ws + OFF_BX + ((size_t)(rt0 + a) * 14 + t) * FR + lane * 16);
    f32x4 aq[2][2], ak[2][2], av[2][2];
#pragma unroll
    for (int a = 0; a < 2; ++a)
#pragma unroll
        for (int c = 0; c < 2; ++c) { aq[a][c] = (f32x4){0,0,0,0}; ak[a][c] = (f32x4){0,0,0,0}; av[a][c] = (f32x4){0,0,0,0}; }
    __syncthreads();
    wput();
    wload(1);
    __syncthreads();
#pragma unroll
    for (int c2 = 0; c2 < 2; ++c2) {
#pragma unroll
        for (int tt = 0; tt < 7; ++tt) {
            const char* wb = lds + ((w & 1) * 42 + tt) * 1024 + lane * 16;
            u32x4 wq[2], wk[2], wv[2];
#pragma unroll
            for (int c = 0; c < 2; ++c) { wq[c] = *(const u32x4*)(wb + (0 + c) * 7168); wk[c] = *(const u32x4*)(wb + (2 + c) * 7168); wv[c] = *(const u32x4*)(wb + (4 + c) * 7168); }
#pragma unroll
            for (int a = 0; a < 2; ++a)
#pragma unroll
                for (int c = 0; c < 2; ++c) {
                    aq[a][c] = mfma32(wq[c], xr[a][7 * c2 + tt], aq[a][c]);
                    ak[a][c] = mfma32(xr[a][7 * c2 + tt], wk[c], ak[a][c]);
                    av[a][c] = mfma32(xr[a][7 * c2 + tt], wv[c], av[a][c]);
                }
        }
        if (c2 == 0) { __syncthreads(); wput(); __syncthreads(); }
    }
    __syncthreads();
#pragma unroll
    for (int a = 0; a < 2; ++a) {
        f32x4 p0, p1;
#pragma unroll
        for (int rr = 0; rr < 4; ++rr) {
            p0[rr] = phi(rbf(aq[a][0][rr]));
            p1[rr] = (8 * q4 + 4 + rr < 28) ? phi(rbf(aq[a][1][rr])) : 0.f;
        }
        bst4(rs, pack8(p0, p1), (unsigned)(OFF_BQ + ((size_t)(rt0 + a) * 16 + h) * FR + lane * 16), 0);
    }
    u32x4 pk[2], vv[2];
#pragma unroll
    for (int c = 0; c < 2; ++c) {
        f32x4 k0, k1, v0, v1;
        const int d = 16 * c + col;
#pragma unroll
        for (int rr = 0; rr < 4; ++rr) {
            k0[rr] = (d < 28) ? phi(rbf(ak[0][c][rr])) : 0.f; k1[rr] = (d < 28) ? phi(rbf(ak[1][c][rr])) : 0.f;
            v0[rr] = (d == 28) ? 1.f : av[0][c][rr]; v1[rr] = (d == 28) ? 1.f : av[1][c][rr];
        }
        pk[c] = pack8(k0, k1); vv[c] = pack8(v0, v1);
    }
    float* red = (float*)lds;
#pragma unroll
    for (int dc = 0; dc < 2; ++dc)
#pragma unroll
        for (int ec = 0; ec < 2; ++ec) {
            const f32x4 d = mfma32(pk[dc], vv[ec], (f32x4){0,0,0,0});
            *(f32x4*)(red + ((w * 4 + dc * 2 + ec) * 64 + lane) * 4) = d;
        }
    bar();
    {
        const int hh = w & 1, tile = w >> 1;
        f32x4 t4 = {0,0,0,0};
#pragma unroll
        for (int g = 0; g < 4; ++g) t4 += *(const f32x4*)(red + (((g * 2 + hh) * 4 + tile) * 64 + lane) * 4);
        bst4(rs, __builtin_bit_cast(u32x4, t4), (unsigned)(OFF_BKV + (((size_t)rb * 16 + 2 * hp + hh) * 4 + tile) * 1024 + lane * 16), 0);
    }
    WAITVM0();
    bar();
    if (tid == 0) signal(p, FLAG_B1);
}

DEVI void b2_unit(const Params& p, int h, char* lds, bool& dead) {
    const int tid = threadIdx.x, lane = tid & 63, w = __builtin_amdgcn_readfirstlane(tid >> 6);
    const int col = lane & 15, q4 = lane >> 4;
    const rsrc_t rs = mkrsrc(p.ws, 0x10000000u);
    float* kvs = (float*)lds;
    waitflag(p, FLAG_B1, 128, dead);
    if (w < 4) {
        f32x4 t4 = {0,0,0,0};
#pragma unroll
        for (int rb = 0; rb < 16; ++rb) t4 += __builtin_bit_cast(f32x4, bld4(rs, (unsigned)(OFF_BKV + (((size_t)rb * 16 + h) * 4 + w) * 1024 + lane * 16), 0));
        const int dc = w >> 1, ec = w & 1;
#pragma unroll
        for (int rr = 0; rr < 4; ++rr) kvs[(16 * dc + 4 * q4 + rr) * 33 + 16 * ec + col] = t4[rr];
    }
    bar();
    if (tid < 32) bst1(rs, __float_as_uint(kvs[tid * 33 + 28]), (unsigned)(OFF_BZ + (h * 32 + tid) * 4), 0);
    u32x4 ka[2];
#pragma unroll
    for (int bb = 0; bb < 2; ++bb) {
        const int d = 8 * (col >> 2) + 4 * bb + (col & 3);
        float e[8];
#pragma unroll
        for (int j = 0; j < 8; ++j) e[j] = (8 * q4 + j < 28) ? kvs[d * 33 + 8 * q4 + j] : 0.f;
        ka[bb] = (u32x4){pk2(e[0], e[1]), pk2(e[2], e[3]), pk2(e[4], e[5]), pk2(e[6], e[7])};
    }
    for (int ct = w; ct < 28; ct += 8) {
        u16 e[8];
#pragma unroll
        for (int j = 0; j < 8; ++j) e[j] = (8 * q4 + j < 28) ? p.bwo[(size_t)(28 * h + 8 * q4 + j) * DM + 32 * (ct >> 1) + 8 * (col >> 2) + 4 * (ct & 1) + (col & 3)] : (u16)0;
        const u32x4 wf = {e[0] | ((unsigned)e[1] << 16), e[2] | ((unsigned)e[3] << 16), e[4] | ((unsigned)e[5] << 16), e[6] | ((unsigned)e[7] << 16)};
        const f32x4 d0 = mfma32(ka[0], wf, (f32x4){0,0,0,0}), d1 = mfma32(ka[1], wf, (f32x4){0,0,0,0});
        bst4(rs, pack8(d0, d1), (unsigned)(OFF_BM + ((size_t)h * 28 + ct) * FR + lane * 16), 0);
    }
    WAITVM0();
    bar();
    if (tid == 0) signal(p, FLAG_B2);
}

DEVI void b3_unit(const Params& p, int u, char* lds, bool& dead) {
    const int tid = threadIdx.x, lane = tid & 63, w = __builtin_amdgcn_readfirstlane(tid >> 6);
    const int col = lane & 15, q4 = lane >> 4;
    const rsrc_t rs = mkrsrc(p.ws, 0x10000000u);
    waitflag(p, FLAG_B2, 16, dead);
    __syncthreads();
#pragma unroll
    for (int k = 0; k < 4; ++k) {
        const int h = 2 * w + (k >> 1), rt = 2 * u + (k & 1);
        const u32x4 pq = bld4(rs, (unsigned)(OFF_BQ + ((size_t)rt * 16 + h) * FR + lane * 16), 0);
        const f32x4 z0 = __builtin_bit_cast(f32x4, bld4(rs, (unsigned)(OFF_BZ + (h * 32 + 8 * q4) * 4), 0)), z1 = __builtin_bit_cast(f32x4, bld4(rs, (unsigned)(OFF_BZ + (h * 32 + 8 * q4 + 4) * 4), 0));
        float f[8] = {bf_lo(pq.x), bf_hi(pq.x), bf_lo(pq.y), bf_hi(pq.y), bf_lo(pq.z), bf_hi(pq.z), bf_lo(pq.w), bf_hi(pq.w)};
        float den = f[0] * z0.x + f[1] * z0.y + f[2] * z0.z + f[3] * z0.w + f[4] * z1.x + f[5] * z1.y + f[6] * z1.z + f[7] * z1.w;
        { float a, b2; swap16(den, a, b2); den = a + b2; swap32(den, a, b2); den = a + b2; }
        const float inv = 1.0f / (den + 1e-6f);
        const u32x4 pn = {pk2(f[0] * inv, f[1] * inv), pk2(f[2] * inv, f[3] * inv), pk2(f[4] * inv, f[5] * inv), pk2(f[6] * inv, f[7] * inv)};
        *(u32x4*)(lds + ((k & 1) * 16 + h) * 1024 + lane * 16) = pn;
    }
    u32x4 bm[2][16];
    auto bload = [&](int pr) {
#pragma unroll
        for (int a = 0; a < 2; ++a)
#pragma unroll
            for (int h = 0; h < 16; ++h) bm[a][h] = bld4(rs, (unsigned)(OFF_BM + ((size_t)h * 28 + 2 * pr + a) * FR + lane * 16), 0);
    };
    if (w < 7) bload(2 * w);
    __syncthreads();
    if (w < 7) {
#pragma unroll
        for (int jp = 0; jp < 2; ++jp) {
            const int pr = 2 * w + jp;
            f32x4 acc[2][2] = {{{0,0,0,0},{0,0,0,0}},{{0,0,0,0},{0,0,0,0}}};
#pragma unroll
            for (int h = 0; h < 16; ++h) {
                const u32x4 p0 = *(const u32x4*)(lds + h * 1024 + lane * 16), p1 = *(const u32x4*)(lds + (16 + h) * 1024 + lane * 16);
#pragma unroll
                for (int a = 0; a < 2; ++a) { acc[a][0] = mfma32(bm[a][h], p0, acc[a][0]); acc[a][1] = mfma32(bm[a][h], p1, acc[a][1]); }
            }
            if (jp == 0) bload(2 * w + 1);
#pragma unroll
            for (int rt = 0; rt < 2; ++rt)
                *(u32x4*)(p.out + (size_t)(32 * u + 16 * rt + col) * OUTW + 448 + 32 * pr + 8 * q4) = pack8(acc[0][rt], acc[1][rt]);
        }
    }
}

DEVI void worker(const Params& p, int u, char* lds) {
    bool dead = false;
    if (p.mode != 2) gemm_worker(p, u, lds);
    if (p.mode == 1) return;
    if (p.mode == 2 && (p.mode2 & 1)) return;
    b1_unit(p, u >> 3, u & 7, lds);
    if (u < 16) b2_unit(p, u, lds, dead);
    if (u >= 64) b3_unit(p, u - 64, lds, dead);
    _Pragma("nounroll") for (int j = u; j < 9 * NSEG; j += 128) {
        const int s = j / 9, k = j % 9;
        if (k < 2) fin_unit(p, s, k, lds, dead); else out_unit(p, s, k - 2, lds, dead);
    }
}

__global__ void __launch_bounds__(512, 2) k_main(Params p) {
    extern __shared__ __attribute__((aligned(16))) char lds[];
    const int b = blockIdx.x;
    const int idx = (b >> 3) * 4 + (b & 3);
    if ((b & 7) < 4) {
        if (p.mode == 1) return;
        if (idx < 112) chain_g(p, idx, lds); else chain_x(p, idx - 112, lds);
    } else {
        worker(p, idx, lds);
    }
}

extern "C" void kernel_launch(void* const* d_in, const int* in_sizes, int n_in, void* d_out, int out_size, void* d_ws, size_t ws_size, hipStream_t stream) {
    Params p;
    p.x = (const u16*)d_in[0]; p.mem0 = (const u16*)d_in[1]; p.Wq = (const u16*)d_in[2]; p.Wk = (const u16*)d_in[3];
    p.Wv = (const u16*)d_in[4]; p.Wo = (const u16*)d_in[5]; p.bx = (const u16*)d_in[7]; p.wqkv = (const u16*)d_in[8]; p.bwo = (const u16*)d_in[9];
    p.out = (u16*)d_out; p.ws = (char*)d_ws;
    const uintptr_t base = (uintptr_t)d_ws;
    const uintptr_t al = (base + OFF_COMM_RAW + ((2u << 20) - 1)) & ~(uintptr_t)((2u << 20) - 1);
    p.comm_off = (unsigned long long)(al - base);
    (void)hipFuncSetAttribute((const void*)k0_prep, hipFuncAttributeMaxDynamicSharedMemorySize, 136000);
    (void)hipFuncSetAttribute((const void*)k_main, hipFuncAttributeMaxDynamicSharedMemorySize, 160000);
    k0_prep<<<256, 512, 136000, stream>>>(p);
    p.mode = 0; p.mode2 = 0;
    k_main<<<256, 512, 160000, stream>>>(p);
}
```

```cpp
#include <hip/hip_runtime.h>
#include <hip/hip_bf16.h>
#include <stdint.h>

typedef __attribute__((ext_vector_type(8))) __bf16 bf16x8;
typedef __attribute__((ext_vector_type(4))) short s16x4;
typedef __attribute__((ext_vector_type(2))) __bf16 bf16x2;
typedef __attribute__((ext_vector_type(2))) float f32x2;
typedef __attribute__((ext_vector_type(4))) float f32x4;
typedef __attribute__((ext_vector_type(4))) unsigned u32x4;
typedef __attribute__((ext_vector_type(2))) unsigned u32x2;
typedef unsigned short u16;
#define LAS __attribute__((address_space(3)))
#define DEVI __device__ __forceinline__

constexpr int DM = 448, NH = 16, NSEG = 64, SEGL = 32, OUTW = 896;
constexpr size_t FR = 1024;
constexpr size_t OFF_FLAG = 0;
constexpr size_t SZ_FLAG = 69632;
constexpr size_t OFF_AF = OFF_FLAG + SZ_FLAG;
constexpr size_t SZ_AF = (size_t)16 * 28 * 14 * FR;
constexpr size_t OFF_BF = OFF_AF + SZ_AF;
constexpr size_t OFF_XF = OFF_BF + SZ_AF;
constexpr size_t SZ_XF = (size_t)64 * 28 * FR;
constexpr size_t OFF_GX = OFF_XF + SZ_XF;
constexpr size_t SZ_GX = (size_t)64 * 16 * 28 * FR;
constexpr size_t OFF_UF = OFF_GX + SZ_GX;
constexpr size_t SZ_UF = (size_t)64 * 56 * 8 * FR;
constexpr size_t OFF_XM = OFF_UF + SZ_UF;
constexpr size_t SZ_XM = (size_t)64 * 16 * 7 * 1024;
constexpr size_t OFF_WC = OFF_XM + SZ_XM;
constexpr size_t SZ_WC = (size_t)64 * 56 * 8 * 256;
constexpr size_t OFF_PX = OFF_WC + SZ_WC;
constexpr size_t SZ_PX = (size_t)64 * 16 * 2 * FR;
constexpr size_t OFF_PM = OFF_PX + SZ_PX;
constexpr size_t SZ_PM = (size_t)64 * 8 * 2 * 512;
constexpr size_t OFF_BQ = OFF_PM + SZ_PM;
constexpr size_t SZ_BQ = (size_t)128 * 16 * FR;
constexpr size_t OFF_BW = OFF_BQ + SZ_BQ;
constexpr size_t SZ_BW = (size_t)3 * 16 * 2 * 14 * FR;
constexpr size_t OFF_BX = OFF_BW + SZ_BW;
constexpr size_t SZ_BX = (size_t)128 * 14 * FR;
constexpr size_t OFF_BKV = OFF_BX + SZ_BX;
constexpr size_t SZ_BKV = (size_t)16 * 16 * 4 * 1024;
constexpr size_t OFF_BM = OFF_BKV + SZ_BKV;
constexpr size_t SZ_BM = (size_t)16 * 28 * FR;
constexpr size_t OFF_BZ = OFF_BM + SZ_BM;
constexpr size_t SZ_BZ = 16 * 32 * 4;
constexpr size_t OFF_COMM_RAW = OFF_BZ + SZ_BZ;
constexpr size_t COMM_BYTES = (size_t)6 << 20;
constexpr unsigned POISON = 0xFFFFFFFFu;
constexpr int FLAG_CFG = 0;
constexpr int FLAG_RDY = 12544;
constexpr int FLAG_B1 = 32;
constexpr int FLAG_B2 = 64;
constexpr int FLAG_PZ = 96;
constexpr int FLAG_BT = 128;
constexpr int FLAG_PROG = 8192;
constexpr int FLAG_DONE = 12288;
constexpr int FLAG_T0 = 384;
constexpr int FLAG_GX = 2048;
constexpr int FLAG_U = 3072;
constexpr int FLAG_P = 4096;
#define ALTV 0u
constexpr unsigned ALT = ALTV;
constexpr int STATS_OFF = 7168;
constexpr int TS_OFF = 7680;
constexpr unsigned TSALT = 0u;

struct Params {
    const u16 *x, *mem0, *Wq, *Wk, *Wv, *Wo, *bx, *wqkv, *bwo;
    u16* out;
    char* ws;
    unsigned long long comm_off;
    unsigned long long mode;
    unsigned long long mode2;
};

#define SLOTSH 1
DEVI int stat_off(int h) { return ((h >> 1) & 3) * 128 + ((h & 1) + 2 * (h >> 3)) * 16; }
DEVI unsigned pk2(float a, float b) { f32x2 v = {a, b}; bf16x2 r = __builtin_convertvector(v, bf16x2); return __builtin_bit_cast(unsigned, r); }
DEVI f32x4 mfma32(u32x4 a, u32x4 b, f32x4 c) { return __builtin_amdgcn_mfma_f32_16x16x32_bf16(__builtin_bit_cast(bf16x8, a), __builtin_bit_cast(bf16x8, b), c, 0, 0, 0); }
DEVI f32x4 mfma16(u32x2 a, u32x2 b, f32x4 c) { return __builtin_amdgcn_mfma_f32_16x16x16bf16_1k(__builtin_bit_cast(s16x4, a), __builtin_bit_cast(s16x4, b), c, 0, 0, 0); }
DEVI u32x4 pack8(f32x4 a, f32x4 b) { u32x4 r; r.x = pk2(a.x, a.y); r.y = pk2(a.z, a.w); r.z = pk2(b.x, b.y); r.w = pk2(b.z, b.w); return r; }
DEVI float fmx(float a, float b) { return __builtin_amdgcn_fmed3f(a, b, 3.4028235e38f); }
template <int CTRL> DEVI float dppf(float x) { return __uint_as_float((unsigned)__builtin_amdgcn_update_dpp(0, (int)__float_as_uint(x), CTRL, 0xf, 0xf, true)); }
constexpr int DPP_ROR8 = 0x128, DPP_ROR4 = 0x124, DPP_QP_1032 = 0xB1, DPP_QP_2301 = 0x4E;
DEVI void swap16(float x, float& a, float& b) { auto s = __builtin_amdgcn_permlane16_swap(__float_as_uint(x), __float_as_uint(x), false, false); a = __uint_as_float(s[0]); b = __uint_as_float(s[1]); }
DEVI void swap32(float x, float& a, float& b) { auto s = __builtin_amdgcn_permlane32_swap(__float_as_uint(x), __float_as_uint(x), false, false); a = __uint_as_float(s[0]); b = __uint_as_float(s[1]); }
DEVI void uswap16(unsigned x, unsigned& a, unsigned& b) { auto s = __builtin_amdgcn_permlane16_swap(x, x, false, false); a = s[0]; b = s[1]; }
DEVI void uswap32(unsigned x, unsigned& a, unsigned& b) { auto s = __builtin_amdgcn_permlane32_swap(x, x, false, false); a = s[0]; b = s[1]; }
template <int CTRL> DEVI unsigned dppu(unsigned x) { return (unsigned)__builtin_amdgcn_update_dpp(0, (int)x, CTRL, 0xf, 0xf, true); }
constexpr int DPP_SHL4 = 0x104, DPP_SHL8 = 0x108;
DEVI float bf_lo(unsigned v) { return __uint_as_float(v << 16); }
DEVI float bf_hi(unsigned v) { return __uint_as_float(v & 0xFFFF0000u); }
DEVI int kcol(int t, int q4) { return 64 * (t >> 1) + 16 * q4 + 8 * (t & 1); }
DEVI unsigned nopoison(unsigned v) { return v < 0xFFFFFFFEu ? v : 0xFFFFFFFEu; }
DEVI unsigned max4(u32x4 v) { unsigned a = v.x > v.y ? v.x : v.y, b = v.z > v.w ? v.z : v.w; return a > b ? a : b; }

typedef __amdgpu_buffer_rsrc_t rsrc_t;
DEVI rsrc_t mkrsrc(const void* p, unsigned bytes) { return __builtin_amdgcn_make_buffer_rsrc((void*)p, 0, bytes, 0x00020000); }
DEVI u32x4 bld4(rsrc_t r, unsigned v, unsigned s) { return __builtin_amdgcn_raw_buffer_load_b128(r, v, s, 16); }
DEVI u32x2 bld2(rsrc_t r, unsigned v, unsigned s) { return __builtin_amdgcn_raw_buffer_load_b64(r, v, s, 16); }
DEVI unsigned bld1(rsrc_t r, unsigned v, unsigned s) { return __builtin_amdgcn_raw_buffer_load_b32(r, v, s, 16); }
DEVI u32x4 bld4p(rsrc_t r, unsigned v, unsigned s) { return __builtin_amdgcn_raw_buffer_load_b128(r, v, s, 0); }
DEVI void bst4(rsrc_t r, u32x4 d, unsigned v, unsigned s) { __builtin_amdgcn_raw_buffer_store_b128(d, r, v, s, 16); }
DEVI void bst2(rsrc_t r, u32x2 d, unsigned v, unsigned s) { __builtin_amdgcn_raw_buffer_store_b64(d, r, v, s, 16); }
DEVI void bst1(rsrc_t r, unsigned d, unsigned v, unsigned s) { __builtin_amdgcn_raw_buffer_store_b32(d, r, v, s, 16); }
#define MEMFENCE() asm volatile("" ::: "memory")
#define WAITVM0() do { asm volatile("s_waitcnt vmcnt(0)" ::: "memory"); __builtin_amdgcn_s_waitcnt(0x0F70); } while (0)
DEVI void bar() { asm volatile("s_waitcnt lgkmcnt(0)\n\ts_barrier" ::: "memory"); }
constexpr unsigned OOB = 0xF0000000u;
DEVI u32x4 mkdesc(const void* p, unsigned bytes) {
    const unsigned long long a = (unsigned long long)p;
    u32x4 d; d.x = __builtin_amdgcn_readfirstlane((unsigned)a); d.y = __builtin_amdgcn_readfirstlane((unsigned)(a >> 32)) & 0xFFFFu; d.z = bytes; d.w = 0x00020000u; return d;
}
#define ALD4(dst, vo, ds, so) asm volatile("buffer_load_dwordx4 %0, %1, %2, %3 offen sc1" : "=v"(dst) : "v"(vo), "s"(ds), "s"(so) : "memory")
#define ALD4R(dst, vo, ds, so) asm volatile("buffer_load_dwordx4 %0, %1, %2, %3 offen sc1" : "+v"(dst) : "v"(vo), "s"(ds), "s"(so) : "memory")
#define ALD4PR(dst, vo, ds, so) asm volatile("buffer_load_dwordx4 %0, %1, %2, %3 offen" : "+v"(dst) : "v"(vo), "s"(ds), "s"(so) : "memory")
#define ALDU16R(dst, vo, ds, so) asm volatile("buffer_load_ushort %0, %1, %2, %3 offen" : "+v"(dst) : "v"(vo), "s"(ds), "s"(so) : "memory")
#define ALD1(dst, vo, ds, so) asm volatile("buffer_load_dword %0, %1, %2, %3 offen sc1" : "=v"(dst) : "v"(vo), "s"(ds), "s"(so) : "memory")
#define ALD1R(dst, vo, ds, so) asm volatile("buffer_load_dword %0, %1, %2, %3 offen sc1" : "+v"(dst) : "v"(vo), "s"(ds), "s"(so) : "memory")
#define AST4(dat, vo, ds, so) asm volatile("buffer_store_dwordx4 %0, %1, %2, %3 offen sc1\n\ts_nop 1" :: "v"(dat), "v"(vo), "s"(ds), "s"(so) : "memory")
#define AST2(dat, vo, ds, so) asm volatile("buffer_store_dwordx2 %0, %1, %2, %3 offen sc1\n\ts_nop 1" :: "v"(dat), "v"(vo), "s"(ds), "s"(so) : "memory")
#define AST1(dat, vo, ds, so) asm volatile("buffer_store_dword %0, %1, %2, %3 offen sc1" :: "v"(dat), "v"(vo), "s"(ds), "s"(so) : "memory")
#define AADD1(vo, ds) asm volatile("buffer_atomic_add %0, %1, %2, 0 offen" :: "v"(1u), "v"(vo), "s"(ds) : "memory")
#define SCHEDB() __builtin_amdgcn_sched_barrier(0)
#define PIN(x) asm volatile("" : "+v"(x))
#define AWAIT(n) asm volatile("s_waitcnt vmcnt(" #n ")" ::: "memory")

#define K0AUX 16
#define K0ST(v, off) __builtin_amdgcn_raw_buffer_store_b128(v, rs, off, 0, K0AUX)
__global__ void __launch_bounds__(512, 2) k0_prep(Params p) {
    extern __shared__ __attribute__((aligned(16))) char lds[];
    const rsrc_t rs = mkrsrc(p.ws, 0x10000000u);
    const int b = blockIdx.x, tid = threadIdx.x, lane = tid & 63, w = __builtin_amdgcn_readfirstlane(tid >> 6);
    const int l15 = lane & 15, q4 = lane >> 4;
    const int mat = b & 1, h = (b >> 1) & 15, sub = b >> 5, th = sub & 1, cg = sub >> 1;
    constexpr int K_SCR = 61440, K_XT = K_SCR + 7 * 8448;
    const int lr = lane >> 2, lp = lane & 3;
    u32x4 stg[7];
    if (mat == 0) {
#pragma unroll
        for (int i = 0; i < 7; ++i) {
            const int ct = 7 * cg + i;
            const int c = 64 * (ct >> 2) + 16 * (lr >> 2) + 4 * (ct & 3) + (lr & 3);
            stg[i] = *(const u32x4*)(p.Wq + ((size_t)c * 16 + h) * 256 + 32 * w + 8 * lp);
        }
    } else {
#pragma unroll
        for (int i = 0; i < 7; ++i) {
            const int n = tid + 512 * i, e = n / 14, ch = n % 14;
            stg[i] = *(const u32x4*)(p.Wo + ((size_t)h * 256 + e) * 448 + 112 * cg + 8 * ch);
        }
    }
    u32x4 rw[2][8];
    const int t = 7 * th + (w < 7 ? w : 0);
    {
        const u16* Wr = mat ? p.Wv : p.Wk;
#pragma unroll
        for (int bb = 0; bb < 2; ++bb) {
            const int d = 64 * (t >> 1) + 16 * (lr >> 2) + 8 * (t & 1) + 4 * bb + (lr & 3);
#pragma unroll
            for (int k = 0; k < 8; ++k) rw[bb][k] = *(const u32x4*)(Wr + ((size_t)d * 16 + h) * 256 + 32 * k + 8 * lp);
        }
    }
    const char* xsrc = (const char*)p.x + (size_t)(b >> 1) * 14336;
    u32x4 xt0 = {0, 0, 0, 0}, xt1 = {0, 0, 0, 0};
    if (!(b & 1)) { xt0 = *(const u32x4*)(xsrc + tid * 16); xt1 = *(const u32x4*)(xsrc + 8192 + (tid < 384 ? tid : 0) * 16); }
    if (b == 5 && tid < 448) {
        const int beta = tid >> 3, i = tid & 7;
        const u32x4 m = *(const u32x4*)(p.mem0 + i * DM + 8 * beta);
        K0ST(m, (unsigned)p.comm_off + tid * 16);
        K0ST(m, (unsigned)p.comm_off + 8192 + tid * 16);
    }
    if (b < 34 && tid < 128) K0ST(((u32x4){0, 0, 0, 0}), (unsigned)(OFF_FLAG + (size_t)b * 2048 + tid * 16));
    if (mat == 0) {
#pragma unroll
        for (int i = 0; i < 7; ++i) *(u32x4*)(lds + (i * 8 + w) * 1024 + (lp * 16 + lr) * 16) = stg[i];
    } else {
#pragma unroll
        for (int i = 0; i < 7; ++i) { const int n = tid + 512 * i, e = n / 14, ch = n % 14; *(u32x4*)(lds + e * 240 + ch * 16) = stg[i]; }
    }
    if (!(b & 1)) { const int o0 = tid * 16, o1 = 8192 + tid * 16;
      *(u32x4*)(lds + K_XT + (o0 / 896) * 912 + (o0 % 896)) = xt0;
      if (tid < 384) *(u32x4*)(lds + K_XT + (o1 / 896) * 912 + (o1 % 896)) = xt1; }
    if (w < 7) {
        char* scr = lds + K_SCR + w * 8448;
#pragma unroll
        for (int bb = 0; bb < 2; ++bb) {
#pragma unroll
            for (int k = 0; k < 8; ++k) *(u32x4*)(scr + lr * 528 + 64 * k + 16 * lp) = rw[bb][k];
            asm volatile("s_waitcnt lgkmcnt(0)" ::: "memory");
#pragma unroll
            for (int k = 0; k < 8; ++k) rw[bb][k] = *(const u32x4*)(scr + l15 * 528 + 64 * k + 16 * q4);
            asm volatile("s_waitcnt lgkmcnt(0)" ::: "memory");
        }
    }
    __syncthreads();
#pragma unroll
    for (int j = 0; j < 2; ++j) {
        const int tt = w + 8 * j;
        if (tt < 14 && !(b & 1)) {
            const u32x4 v = *(const u32x4*)(lds + K_XT + l15 * 912 + kcol(tt, q4) * 2);
            K0ST(v, (unsigned)(OFF_XF + ((size_t)(b >> 1) * 14 + tt) * FR + lane * 16));
        }
    }
    if (w < 7) {
        auto ldcf = [&](int i, u32x4 (&cf)[8]) {
#pragma unroll
            for (int k = 0; k < 8; ++k) {
                if (mat == 0) cf[k] = *(const u32x4*)(lds + (i * 8 + k) * 1024 + lane * 16);
                else {
                    const int qq = l15 >> 2, pp = l15 & 3;
                    const int row0 = 32 * k + 8 * q4 + qq;
                    typedef __attribute__((ext_vector_type(4))) short v4s;
                    v4s lo = __builtin_amdgcn_ds_read_tr16_b64_v4i16((LAS v4s*)(lds + row0 * 240 + (16 * i + 4 * pp) * 2));
                    v4s hi = __builtin_amdgcn_ds_read_tr16_b64_v4i16((LAS v4s*)(lds + (row0 + 4) * 240 + (16 * i + 4 * pp) * 2));
                    u32x2 l2 = __builtin_bit_cast(u32x2, lo), h2 = __builtin_bit_cast(u32x2, hi);
                    cf[k] = (u32x4){l2.x, l2.y, h2.x, h2.y};
                }
            }
        };
        u32x4 cfa[8], cfb[8];
        auto tile = [&](int i, const u32x4 (&cf)[8]) {
            f32x4 a0 = {0, 0, 0, 0}, a1 = {0, 0, 0, 0};
#pragma unroll
            for (int k = 0; k < 8; ++k) { a0 = mfma32(rw[0][k], cf[k], a0); a1 = mfma32(rw[1][k], cf[k], a1); }
            const u32x4 o = pack8(a0, a1);
            if (mat == 0) {
                const int ct = 7 * cg + i;
                K0ST(o, (unsigned)(OFF_AF + ((size_t)(h * 28 + ct) * 14 + t) * FR + lane * 16));
            } else {
                const int beta = 14 * cg + 2 * i + (l15 >> 3);
                K0ST(o, (unsigned)(OFF_BF + ((size_t)((h >> 1) * 56 + beta) * 14 + t) * FR + (16 * q4 + 8 * (h & 1) + (l15 & 7)) * 16));
            }
        };
        ldcf(0, cfa);
#pragma unroll
        for (int i = 0; i < 7; i += 2) {
            if (i + 1 < 7) ldcf(i + 1, cfb);
            tile(i, cfa);
            if (i + 2 < 7) ldcf(i + 2, cfa);
            if (i + 1 < 7) tile(i + 1, cfb);
        }
    }
}

#define STAG 0
constexpr int SPIN_MAX = 200000;
DEVI unsigned* flagp(const Params& p, int idx) { return (unsigned*)(p.ws + OFF_FLAG) + idx; }
DEVI void signal(const Params& p, int idx) { __hip_atomic_fetch_add(flagp(p, idx), 1u, __ATOMIC_RELAXED, __HIP_MEMORY_SCOPE_AGENT); }
DEVI unsigned ldflag(const Params& p, int idx) { return __hip_atomic_load(flagp(p, idx), __ATOMIC_RELAXED, __HIP_MEMORY_SCOPE_AGENT); }
DEVI void waitflag(const Params& p, int idx, unsigned target, bool& dead, int sleep = 4) {
    int k = dead ? SPIN_MAX : 0;
    _Pragma("nounroll") for (; k < SPIN_MAX; ++k) {
        const unsigned v = __builtin_amdgcn_readfirstlane(ldflag(p, idx));
        if (v >= target) break;
        __builtin_amdgcn_s_sleep(4);
    }
    if (k >= SPIN_MAX) dead = true;
    MEMFENCE();
}

DEVI void waitall(const Params& p, int idx, int n, bool& dead) {
    int k = dead ? SPIN_MAX : 0;
    const int _l = (threadIdx.x & 63) & (n - 1);
    _Pragma("nounroll") for (; k < SPIN_MAX; ++k) {
        const unsigned v = ldflag(p, idx + _l);
        if (!__any(v == 0u)) break;
        __builtin_amdgcn_s_sleep(3);
    }
    if (k >= SPIN_MAX) dead = true;
    MEMFENCE();
}

DEVI void gemm_worker(const Params& p, int u, char* lds) {
    const int tid = threadIdx.x, lane = tid & 63, w = __builtin_amdgcn_readfirstlane(tid >> 6);
    const u32x4 ds = mkdesc(p.ws, 0x10000000u);
    const int isU = u >> 6, par = (u >> 2) & 1, v = ((((u & 3) | (((u & 63) >> 3) << 2))) << 1) | par;
    constexpr unsigned SEG_STRIDE = 16 * 28 * 1024;
    const int flag_idx = isU ? FLAG_U + ((v >> 1) & 3) : FLAG_GX + (v >> 2);
    {
        const u32x4 pz = {POISON, POISON, POISON, POISON};
#pragma unroll
        for (int k = 0; k < 4; ++k) {
            const unsigned o = (unsigned)(w * 4 + k) * 1024u;
            if (!(u == 0 && (o < 7168u || (o >= 8192u && o < 15360u)))) AST4(pz, (unsigned)lane * 16u + o, ds, (unsigned)p.comm_off + (unsigned)u * 32768u);
        }
    }
    u32x4 x0[4];
#pragma unroll
    for (int j = 0; j < 4; ++j) { const int f = (w + 8 * j < 28) ? w + 8 * j : w; x0[j] = *(const u32x4*)(p.ws + OFF_XF + ((size_t)par * 28 + f) * FR + lane * 16); }
    if (w < 7) {
        u32x4 wt[2][14];
        unsigned vo_out[2];
        const char* base;
        if (!isU) {
            const int h = v >> 2, tp = 7 * ((v >> 1) & 1) + w; base = p.ws + OFF_AF + (size_t)(h * 28 + 2 * tp) * 14 * FR;
            vo_out[0] = (unsigned)(OFF_GX + ((size_t)(h * 2 + 0) * 14 + tp) * FR + lane * 16);
            vo_out[1] = (unsigned)(OFF_GX + ((size_t)(h * 2 + 1) * 14 + tp) * FR + lane * 16);
        } else {
            const int hp = v >> 3, b0 = 14 * ((v >> 1) & 3) + 2 * w; base = p.ws + OFF_BF + (size_t)(hp * 56 + b0) * 14 * FR;
            vo_out[0] = (unsigned)(OFF_UF + ((size_t)(b0 + 0) * 8 + hp) * FR + lane * 16);
            vo_out[1] = (unsigned)(OFF_UF + ((size_t)(b0 + 1) * 8 + hp) * FR + lane * 16);
        }
#pragma unroll
        for (int a = 0; a < 2; ++a)
#pragma unroll
            for (int t = 0; t < 14; ++t) wt[a][t] = *(const u32x4*)(base + (size_t)(a * 14 + t) * FR + lane * 16);
        WAITVM0();
#pragma unroll
        for (int j = 0; j < 4; ++j) { const int f = w + 8 * j; if (f < 28) *(u32x4*)(lds + f * 1024 + lane * 16) = x0[j]; }
        bar();
        int buf = 0;
        _Pragma("nounroll") for (int s = par; s < NSEG; s += 2) {
            f32x4 acc[2][2];
#pragma unroll
            for (int a = 0; a < 2; ++a)
#pragma unroll
                for (int kt = 0; kt < 2; ++kt) acc[a][kt] = (f32x4){0, 0, 0, 0};
            const char* xb = lds + buf * 28672 + lane * 16;
            u32x4 xf[28];
#pragma unroll
            for (int k = 0; k < 14; ++k) xf[k] = *(const u32x4*)(xb + ((k & 1) * 14 + (k >> 1)) * 1024);
            SCHEDB();
            if (!isU) {
#pragma unroll
                for (int k = 0; k < 28; ++k) {
                    if (k + 14 < 28) xf[k + 14] = *(const u32x4*)(xb + (((k + 14) & 1) * 14 + ((k + 14) >> 1)) * 1024);
                    acc[0][k & 1] = mfma32(wt[0][k >> 1], xf[k], acc[0][k & 1]); acc[1][k & 1] = mfma32(wt[1][k >> 1], xf[k], acc[1][k & 1]);
                    SCHEDB();
                }
            } else {
#pragma unroll
                for (int k = 0; k < 28; ++k) {
                    if (k + 14 < 28) xf[k + 14] = *(const u32x4*)(xb + (((k + 14) & 1) * 14 + ((k + 14) >> 1)) * 1024);
                    acc[0][k & 1] = mfma32(xf[k], wt[0][k >> 1], acc[0][k & 1]); acc[1][k & 1] = mfma32(xf[k], wt[1][k >> 1], acc[1][k & 1]);
                    SCHEDB();
                }
            }
            u32x4 o0, o1;
            if (!isU) { o0 = pack8(acc[0][0], acc[1][0]); o1 = pack8(acc[0][1], acc[1][1]); }
            else { o0 = pack8(acc[0][0], acc[0][1]); o1 = pack8(acc[1][0], acc[1][1]); }
            const unsigned so = (unsigned)s * SEG_STRIDE;
            AST4(o0, vo_out[0], ds, so);
            AST4(o1, vo_out[1], ds, so);
            if (s < 2) AWAIT(0); else AWAIT(2);
            bar();
            buf ^= 1;
        }
        AWAIT(0);
        bar();
    } else {
        u32x4 nx[28];
#pragma unroll
        for (int f = 0; f < 28; ++f) nx[f] = (u32x4){0, 0, 0, 0};
        {
            const size_t o = (isU ? OFF_UF : OFF_GX) + (size_t)(lane & 7) * (1u << 20);
            if (lane < 8) { const unsigned t = *(const volatile unsigned*)(p.ws + o); asm volatile("" :: "v"(t)); }
        }
        const unsigned vo_x = (unsigned)(OFF_XF + lane * 16);
        auto issue = [&](int sg) {
#pragma unroll
            for (int f = 0; f < 28; ++f) ALD4PR(nx[f], vo_x + (unsigned)f * 1024u, ds, (unsigned)sg * 28672u);
        };
        WAITVM0();
#pragma unroll
        for (int j = 0; j < 4; ++j) { const int f = w + 8 * j; if (f < 28) *(u32x4*)(lds + f * 1024 + lane * 16) = x0[j]; }
        issue(par + 2);
        bar();
        if (lane == 0) signal(p, FLAG_PZ);
        int buf = 0;
        const u32x4 dbx = mkdesc(p.bx, 2048u * 448u * 2u), dwq = mkdesc(p.wqkv, 448u * 1344u * 2u);
        const int l15 = lane & 15, q4 = lane >> 4;
        u32x4 tq = {0, 0, 0, 0};
        unsigned tw[8] = {0, 0, 0, 0, 0, 0, 0, 0};
        _Pragma("nounroll") for (int s = par; s < NSEG; s += 2) {
            const int it = s >> 1;
            AWAIT(0);
#pragma unroll
            for (int f = 0; f < 28; ++f) asm volatile("" : "+v"(nx[f]));
            asm volatile("" : "+v"(tq), "+v"(tw[0]), "+v"(tw[1]), "+v"(tw[2]), "+v"(tw[3]), "+v"(tw[4]), "+v"(tw[5]), "+v"(tw[6]), "+v"(tw[7]));
            SCHEDB();
#pragma unroll
            for (int f = 0; f < 28; ++f) *(u32x4*)(lds + (buf ^ 1) * 28672 + f * 1024 + lane * 16) = nx[f];
            SCHEDB();
            {
                const unsigned so1 = (it >= 1 && it <= 14) ? (unsigned)(OFF_BX + (size_t)((u * 14 + it - 1) * 1024)) + lane * 16 : OOB;
                const unsigned so2 = (it >= 15 && it <= 25 && u + 128 * (it - 15) < 1344) ? (unsigned)(OFF_BW + (size_t)((u + 128 * (it - 15)) * 1024)) + lane * 16 : OOB;
                const u32x4 o = {tw[0] | (tw[1] << 16), tw[2] | (tw[3] << 16), tw[4] | (tw[5] << 16), tw[6] | (tw[7] << 16)};
                AST4(tq, so1, ds, 0u);
                AST4(o, so2, ds, 0u);
            }
            const int sn = (s + 4 < NSEG) ? s + 4 : s;
            issue(__builtin_amdgcn_readfirstlane(sn));
            {
                const unsigned vo1 = (it < 14) ? (unsigned)(((16 * u + l15) * 448 + kcol(it, q4)) * 2) : OOB;
                const int f = u + 128 * (it - 14), tt = f % 14, tile = (f / 14) & 1, hh = (f / 28) & 15, t3 = f / 448;
                const int col = (t3 == 0) ? (8 * (l15 >> 2) + 4 * tile + (l15 & 3)) : (16 * tile + l15);
                const unsigned vo2 = (it >= 14 && it < 25 && f < 1344 && col < 28) ? (unsigned)((kcol(tt, q4) * 1344 + t3 * 448 + hh * 28 + col) * 2) : OOB;
                ALD4PR(tq, vo1, dbx, 0u);
#pragma unroll
                for (int j = 0; j < 8; ++j) ALDU16R(tw[j], vo2, dwq, (unsigned)j * 2688u);
            }
            bar();
            if (it == 26 && lane == 0) signal(p, FLAG_BT);
            if (lane == 0) {
                if (s < 2) signal(p, flag_idx + s * 16);
                else if (s >= 4) signal(p, flag_idx + (s - 2) * 16);
            }
            if (STAG > 0 && s < 2) {
                const int n = ((u * 13) & 31) * STAG;
                _Pragma("nounroll") for (int k = 0; k < n; ++k) __builtin_amdgcn_s_sleep(1);
            }
            buf ^= 1;
        }
        AWAIT(0);
        bar();
        if (lane == 0) signal(p, flag_idx + (NSEG - 2 + par) * 16);
    }
}

constexpr float SC2 = 0.0625f * 1.4426950408889634f;
constexpr int L_R = 0;
constexpr int L_RED1 = 7168;
constexpr int L_RED2 = L_RED1 + 4352;
constexpr int L_REDX = L_RED2 + 2432;
constexpr int L_XX = L_REDX + 8192;
constexpr int L_CFG = L_XX + 73728;

DEVI unsigned chunk_off(const Params& p, unsigned cfg, int s, int j) {
    const int pg = s >> 5;
    return (unsigned)p.comm_off + (unsigned)pg * (2u << 20) + (unsigned)(2 * (4 * (s & 31) + j) + ((cfg >> pg) & 1)) * 8192u;
}

DEVI unsigned ts_off(const Params& p, unsigned cfg, int s) { return (unsigned)p.comm_off + (4u << 20) + (unsigned)(2 * (s >> 6) + ((cfg >> 2) & 1)) * 8192u + (unsigned)(s & 63) * 128u; }
DEVI unsigned rt32() { return (unsigned)__builtin_amdgcn_s_memrealtime(); }
DEVI void wait_until(unsigned target) {
    const unsigned now = rt32();
    const int d = (int)(target - now);
    if (d <= 0) return;
    if (d > 2000) target = now + 2000u;
    _Pragma("nounroll") while ((int)(rt32() - target) < 0) { }
}
#define NOP4 "s_nop 0\n\ts_nop 0\n\ts_nop 0\n\ts_nop 0\n\t"
#define FINEK 44
DEVI void wait_fine(unsigned target) {
    unsigned now = rt32();
    int d = (int)(target - now);
    if (d > 2000) { target = now + 2000u; d = 2000; }
    _Pragma("nounroll") while (d > 5) d = (int)(target - rt32());
    if (d >= 2) {
        const unsigned off = 12u + 80u - ((unsigned)(d * 23 - FINEK) & 0x7Cu);
        asm volatile("s_getpc_b64 vcc\n\ts_add_u32 vcc_lo, vcc_lo, %0\n\ts_addc_u32 vcc_hi, vcc_hi, 0\n\ts_setpc_b64 vcc\n\t" NOP4 NOP4 NOP4 NOP4 NOP4 :: "s"(off) : "vcc", "scc", "memory");
    }
}
#define C2T 110
#define O1T 84
#define C2BOOT 260
#define O1BOOT 200
#define C2H 86
#define GDELAY 30
#define WPOS 0
#define SDLY 3
#define TSOFF 10
#define XHD2 10
#define XHD 0
#define BULKD 4
#define CUP 80
#define XOFFT 0
#define DEXTRA 0
#define DX 0
#define GMARG 29
#define BMARG 32
#define EINC 0
#define EMAX 30
#define EDEC 4
#define MINHM 24
#define TSDLY 1
#define TSEARLY 0
#define PACE0 32
#define PACEA 48
#define BDLY 2
#define STARTD 60
#define NPROBE 10
DEVI void probe_cfg(const Params& p, rsrc_t rs, int idx) {
    const int pg = idx >> 1, par = idx & 1;
    unsigned best = 1u << 20;
    for (int k = 0; k < NPROBE; ++k) {
        const unsigned off = (unsigned)p.comm_off + pg * (2u << 20) + (unsigned)((pg ? 2 * (4 * 31 + 1) : 0) + par) * 8192u + 512u * k;
        const unsigned long long c0 = __builtin_amdgcn_s_memtime();
        unsigned vv = bld1(rs, off, 0);
        asm volatile("s_waitcnt vmcnt(0)" : "+v"(vv) :: "memory");
        const unsigned long long c1 = __builtin_amdgcn_s_memtime();
        const unsigned dt = (unsigned)(c1 - c0);
        if (k > 0 && dt < best) best = dt;
    }
    if ((threadIdx.x & 63) == 0) bst1(rs, best + 1u, OFF_FLAG + (FLAG_CFG + idx) * 4, 0);
}
DEVI unsigned get_cfg(const Params& p, rsrc_t rs, char* lds) {
    const int tid = threadIdx.x, lane = tid & 63, w = __builtin_amdgcn_readfirstlane(tid >> 6);
    if (w == 7) {
        unsigned v = 0;
        for (int it = 0; it < SPIN_MAX; ++it) { v = bld1(rs, OFF_FLAG + (FLAG_CFG + (lane & 3)) * 4, 0); MEMFENCE(); if (!__any(v == 0u)) break; __builtin_amdgcn_s_sleep(2); }
        const unsigned l0 = __builtin_amdgcn_readlane(v, 0), l1 = __builtin_amdgcn_readlane(v, 1), l2 = __builtin_amdgcn_readlane(v, 2), l3 = __builtin_amdgcn_readlane(v, 3);
        unsigned cfg = (l1 < l0 ? 1u : 0u) | (l3 < l2 ? 2u : 0u);
        if (lane == 0) *(unsigned*)(lds + L_CFG) = cfg;
    }
    __syncthreads();
    return __builtin_amdgcn_readfirstlane(*(unsigned*)(lds + L_CFG));
}
DEVI unsigned start_sync(const Params& p, u32x4 ds, int r, char* lds) {
    const int tid = threadIdx.x, lane = tid & 63, w = __builtin_amdgcn_readfirstlane(tid >> 6);
    bar();
    if (w == 3) {
        const unsigned base = rt32();
        if (lane == 0) AST1(base | 1u, (unsigned)(OFF_FLAG + FLAG_RDY * 4) + (unsigned)r * 128u, ds, 0u);
        unsigned t0;
        if (r == 0) {
            unsigned v0 = 0, v1 = 0, v2 = 0;
            const unsigned vo = (unsigned)(OFF_FLAG + FLAG_RDY * 4) + (unsigned)lane * 128u, vz = (unsigned)(OFF_FLAG + FLAG_PZ * 4) + (unsigned)(lane & 0);
            for (int it = 0; it < SPIN_MAX; ++it) {
                ALD1R(v0, vo, ds, 0u); ALD1R(v1, vo, ds, 8192u); ALD1R(v2, vz, ds, 0u);
                asm volatile("s_waitcnt vmcnt(0)" : "+v"(v0), "+v"(v1), "+v"(v2) :: "memory");
                if (!__any(v0 == 0u || v1 == 0u || v2 < 128u)) break;
            }
            t0 = (rt32() + STARTD) | 1u;
            if (lane == 0) AST1(t0, (unsigned)(OFF_FLAG + FLAG_T0 * 4), ds, 0u);
        } else {
            unsigned v = 0;
            const unsigned vo = (unsigned)(OFF_FLAG + FLAG_T0 * 4) + (unsigned)(lane & 0);
            for (int it = 0; it < SPIN_MAX; ++it) {
                ALD1R(v, vo, ds, 0u);
                asm volatile("s_waitcnt vmcnt(0)" : "+v"(v) :: "memory");
                v = (unsigned)__builtin_amdgcn_readfirstlane(v);
                if (v) break;
                __builtin_amdgcn_s_sleep(1);
            }
            t0 = v;
        }
        if (lane == 0) *(unsigned*)(lds + L_CFG + 4) = t0;
    }
    bar();
    return __builtin_amdgcn_readfirstlane(*(unsigned*)(lds + L_CFG + 4));
}

#define D1 0
#define D2 0
template <int DELAY> DEVI void pause() { if (DELAY > 0) __builtin_amdgcn_s_sleep(DELAY); }

#define EV(k)
#define STAMP(k)

constexpr int L_TS = L_CFG + 64;
constexpr int L_RF = L_CFG + 128;
constexpr int L_PU = L_CFG + 256;
constexpr int L_PX = L_PU + 8192;
constexpr int L_GX = L_CFG + 256;
#define WSPLIT 4
#define MINH 28
#define MINM 24
DEVI unsigned tmax(unsigned a, unsigned b) { return ((int)(a - b) > 0) ? a : b; }

DEVI void await_flag(u32x4 ds, unsigned vo, unsigned target, bool& dead) {
    int spins = 0; const int lim = dead ? 1 : SPIN_MAX; bool bad;
    _Pragma("nounroll") do {
        unsigned v; ALD1(v, vo, ds, 0u);
        asm volatile("s_waitcnt vmcnt(0)" : "+v"(v) :: "memory");
        bad = (unsigned)__builtin_amdgcn_readfirstlane(v) < target;
        if (bad) __builtin_amdgcn_s_sleep(2);
    } while (bad && ++spins < lim);
    if (bad) dead = true;
}
DEVI void await_all(u32x4 ds, unsigned vo, bool& dead) {
    int spins = 0; const int lim = dead ? 1 : SPIN_MAX; bool bad;
    _Pragma("nounroll") do {
        unsigned v; ALD1(v, vo, ds, 0u);
        asm volatile("s_waitcnt vmcnt(0)" : "+v"(v) :: "memory");
        bad = __any(v == 0u);
        if (bad) __builtin_amdgcn_s_sleep(2);
    } while (bad && ++spins < lim);
    if (bad) dead = true;
}
#define LAT0()
#define LAT1(k)
#define WT(k)
#define TA0(v)
#define TA1(v, k, cond)

#define PTA()
#define PTB(k)
#define GTS(k)
#define GTDECL()
#define GTOUT(n, slot)
template <bool V> struct BoolC { static constexpr bool value = V; };

DEVI void chain_gw(const Params& p, int r, char* lds, u32x4 ds, unsigned t_entry) {
    const rsrc_t rs = mkrsrc(p.ws, 0x10000000u);
    const int tid = threadIdx.x, lane = tid & 63, w = __builtin_amdgcn_readfirstlane(tid >> 6);
    const int col = lane & 15, q4 = lane >> 4;
    const int h = r / 7, c7 = r % 7, beta = r >> 1, rh = r & 1;
    u32x4 aw[2][2][4];
    const int nq = (w < 3) ? 2 : 1;
    const int qf0 = (c7 + 2 * w) % 7, qf1 = (c7 + 2 * w + nq - 1) % 7;
#pragma unroll
    for (int qi = 0; qi < 2; ++qi)
#pragma unroll
        for (int e = 0; e < 2; ++e)
#pragma unroll
            for (int tau = 0; tau < 4; ++tau) {
                const int q = qi ? qf1 : qf0;
                aw[qi][e][tau] = *(const u32x4*)(p.ws + OFF_AF + ((size_t)(h * 28 + 4 * c7 + tau) * 14 + 2 * q + e) * FR + lane * 16);
                if (qi == 1 && nq == 1) aw[qi][e][tau] = (u32x4){0, 0, 0, 0};
            }
    unsigned mE = (col < 8) ? 0xFFFFFFFFu : 0u;
    asm volatile("" : "+v"(mE));
    const unsigned cfg = get_cfg(p, rs, lds);
    WAITVM0();
    bool dead = false;
    const unsigned vo_m0 = (unsigned)(qf0 * 1024 + lane * 16), vo_m1 = (nq == 2) ? (unsigned)(qf1 * 1024 + lane * 16) : OOB, vo_mc = (unsigned)(c7 * 1024 + lane * 16);
    const unsigned vo_pp = (lane >= 32) ? OOB : (unsigned)((((lane >> 4) & 1) ? 16384 : 32768) + h * 512 + (c7 + (h & SLOTSH)) * 64 + (lane & 15) * 4);
    const unsigned vo_mp = (lane >= 16) ? OOB : (unsigned)(beta * 128 + rh * 64 + (lane & 15) * 4);
    const unsigned vo_xm = (unsigned)(OFF_XM + ((size_t)h * 7 + c7) * 1024 + lane * 16);
    const unsigned vo_ts = (r == 0 && w == 2 && lane >= 4) ? OOB : (unsigned)(TS_OFF + (lane & 3) * 4);
    unsigned ts1 = 0;
    unsigned tm = 0, th = 0;
    volatile LAS unsigned* ldist = (volatile LAS unsigned*)((LAS char*)lds + L_RF + 4);
    bool dh_prev = false, dm_prev = false;
    unsigned e1 = 0, e2 = 0;
    int a1 = 0, a2 = 0, acnt = 0, g8 = (O1T - GMARG) * 8, b8 = (C2H - BMARG) * 8;
    LAS char* lbase = (LAS char*)lds;
    LAS char* red1 = lbase + L_RED1 + ((col < 8 && q4 < 2) ? w * 256 + (q4 * 8 + col) * 16 : 1024 + w * 768 + (q4 < 2 ? q4 * 8 + (col & 7) : 16 + (q4 - 2) * 16 + col) * 16);
    LAS char* redr = lbase + L_RED1 + ((((lane & 3) >> 1) * 8 + ((lane >> 2) & 7)) * 16 + (lane & 1) * 8);
    LAS char* red2r = lbase + L_RED2 + (lane & 15) * 144 + (lane >> 4) * 32;
    asm volatile("" : "+v"(red1), "+v"(redr), "+v"(red2r));
    if (tid == 0) { ldist[0] = 0u; ldist[1] = 0u; }
    tm = start_sync(p, ds, r, lds);
    __builtin_amdgcn_s_setprio(2);
    th = tm + O1T;
    auto step = [&](const int s, auto LASTC) {
        constexpr bool LAST = decltype(LASTC)::value;
        const unsigned oM = (unsigned)__builtin_amdgcn_readfirstlane((int)chunk_off(p, cfg, s, 0)), oMn = (unsigned)__builtin_amdgcn_readfirstlane((int)chunk_off(p, cfg, s + 1, 0));
        unsigned sv1 = (unsigned)(s + 1); PIN(sv1);
        wait_fine(tm);
        GTDECL();
        GTS(0);
        if (w == 0) EV(4);
        u32x4 gE, gO;
        unsigned long long bad64;
#define GTGV 0
#define GDLY "s_sleep 1\n\t"
#define MF "v_mfma_f32_16x16x32_bf16 "
#define DPP8 " row_ror:8 row_mask:0xf bank_mask:0xf bound_ctrl:1\n\t"
#define GS1(k)
#define GOUT1()
#define GS0(k)
#define GOUT0()
#define GSTAGE(GS, GOUT, OFF) \
            asm volatile( \
                "buffer_load_dwordx4 v[200:203], %[m0], %[ds], %[oM] offen sc1\n\t" \
                GDLY \
                "buffer_load_dwordx4 v[204:207], %[m1], %[ds], %[oM] offen sc1\n\t" \
                "buffer_load_dwordx4 v[208:211], %[mc], %[ds], %[oM] offen sc1\n\t" \
                "s_waitcnt vmcnt(2)\n\t" GS(1) \
                "v_and_b32 v212, v200, %[mE]\n\tv_and_b32 v213, v201, %[mE]\n\tv_and_b32 v214, v202, %[mE]\n\tv_and_b32 v215, v203, %[mE]\n\t" \
                "s_nop 1\n\t" \
                MF "v[236:239], %[a000], v[212:215], 0\n\t" \
                "v_and_b32_dpp v216, v200, %[mE]" DPP8 "v_and_b32_dpp v217, v201, %[mE]" DPP8 \
                MF "v[240:243], %[a001], v[212:215], 0\n\t" \
                "v_and_b32_dpp v218, v202, %[mE]" DPP8 "v_and_b32_dpp v219, v203, %[mE]" DPP8 \
                MF "v[244:247], %[a002], v[212:215], 0\n\t" \
                "v_max3_u32 v196, v200, v201, v202\n\t" \
                MF "v[248:251], %[a003], v[212:215], 0\n\t" \
                "v_max_u32 v196, v196, v203\n\t" \
                MF "v[236:239], %[a010], v[216:219], v[236:239]\n\t" \
                MF "v[240:243], %[a011], v[216:219], v[240:243]\n\t" \
                "s_waitcnt vmcnt(1)\n\t" GS(2) \
                "v_and_b32 v220, v204, %[mE]\n\tv_and_b32 v221, v205, %[mE]\n\t" \
                MF "v[244:247], %[a012], v[216:219], v[244:247]\n\t" \
                "v_and_b32 v222, v206, %[mE]\n\tv_and_b32 v223, v207, %[mE]\n\t" \
                MF "v[248:251], %[a013], v[216:219], v[248:251]\n\t" \
                "v_and_b32_dpp v224, v204, %[mE]" DPP8 "v_and_b32_dpp v225, v205, %[mE]" DPP8 \
                MF "v[236:239], %[a100], v[220:223], v[236:239]\n\t" \
                "v_and_b32_dpp v226, v206, %[mE]" DPP8 "v_and_b32_dpp v227, v207, %[mE]" DPP8 \
                MF "v[240:243], %[a101], v[220:223], v[240:243]\n\t" \
                "v_max3_u32 v196, v196, v204, v205\n\tv_max3_u32 v196, v196, v206, v207\n\t" \
                MF "v[236:239], %[a110], v[224:227], v[236:239]\n\t" \
                "s_waitcnt vmcnt(0)\n\t" GS(3) \
                "v_and_b32 v228, v208, %[mE]\n\tv_and_b32 v229, v209, %[mE]\n\t" \
                MF "v[240:243], %[a111], v[224:227], v[240:243]\n\t" \
                "v_and_b32 v230, v210, %[mE]\n\tv_and_b32 v231, v211, %[mE]\n\t" \
                MF "v[244:247], %[a102], v[220:223], v[244:247]\n\t" \
                "v_and_b32_dpp v232, v208, %[mE]" DPP8 "v_and_b32_dpp v233, v209, %[mE]" DPP8 \
                MF "v[248:251], %[a103], v[220:223], v[248:251]\n\t" \
                "v_and_b32_dpp v234, v210, %[mE]" DPP8 "v_and_b32_dpp v235, v211, %[mE]" DPP8 \
                MF "v[244:247], %[a112], v[224:227], v[244:247]\n\t" \
                "v_cvt_pk_bf16_f32 v184, v236, v237\n\tv_cvt_pk_bf16_f32 v185, v238, v239\n\t" \
                MF "v[248:251], %[a113], v[224:227], v[248:251]\n\t" \
                "v_cvt_pk_bf16_f32 v186, v240, v241\n\tv_cvt_pk_bf16_f32 v187, v242, v243\n\t" \
                "v_max3_u32 v196, v196, v208, v209\n\tv_max3_u32 v196, v196, v210, v211\n\t" \
                MF "v[192:195], v[184:187], v[228:231], 0\n\t" GS(4) \
                "v_cvt_pk_bf16_f32 v188, v244, v245\n\tv_cvt_pk_bf16_f32 v189, v246, v247\n\t" \
                "s_nop 0\n\t" \
                "v_cvt_pk_bf16_f32 v190, v248, v249\n\tv_cvt_pk_bf16_f32 v191, v250, v251\n\t" \
                "v_cmp_eq_u32_e64 %[bad], -1, v196\n\t" \
                "s_nop 0\n\t" \
                MF "v[192:195], v[188:191], v[232:235], v[192:195]\n\t" GS(5) \
                "s_nop 7\n\t" \
                "ds_write_b128 %[red], v[192:195]\n\t" \
                : "={v[184:187]}"(gE), "={v[188:191]}"(gO), [bad] "=s"(bad64) GOUT() \
                : [gm] "n"(GTGV), [m0] "v"(vo_m0), [m1] "v"(vo_m1), [mc] "v"(vo_mc), [ds] "s"(ds), [oM] "s"(OFF), [mE] "v"(mE), [red] "v"(red1), \
                  [a000] "v"(aw[0][0][0]), [a001] "v"(aw[0][0][1]), [a002] "v"(aw[0][0][2]), [a003] "v"(aw[0][0][3]), \
                  [a010] "v"(aw[0][1][0]), [a011] "v"(aw[0][1][1]), [a012] "v"(aw[0][1][2]), [a013] "v"(aw[0][1][3]), \
                  [a100] "v"(aw[1][0][0]), [a101] "v"(aw[1][0][1]), [a102] "v"(aw[1][0][2]), [a103] "v"(aw[1][0][3]), \
                  [a110] "v"(aw[1][1][0]), [a111] "v"(aw[1][1][1]), [a112] "v"(aw[1][1][2]), [a113] "v"(aw[1][1][3]) \
                : "memory", "v192", "v193", "v194", "v195", "v196", "v200", "v201", "v202", "v203", "v204", "v205", "v206", "v207", "v208", "v209", "v210", "v211", \
                  "v212", "v213", "v214", "v215", "v216", "v217", "v218", "v219", "v220", "v221", "v222", "v223", "v224", "v225", "v226", "v227", "v228", "v229", "v230", "v231", \
                  "v232", "v233", "v234", "v235", "v236", "v237", "v238", "v239", "v240", "v241", "v242", "v243", "v244", "v245", "v246", "v247", "v248", "v249", "v250", "v251")
        GSTAGE(GS1, GOUT1, oM);
        if (__builtin_expect(bad64 != 0ull, 0)) {
            int spins = 0; const int lim = dead ? 1 : SPIN_MAX; bool bad;
            ldist[0] = sv1;
            unsigned oR = oM ^ ALT;
            _Pragma("nounroll") do { GSTAGE(GS0, GOUT0, oR); oR ^= ALT; bad = bad64 != 0ull; } while (bad && ++spins < lim);
            if (bad) dead = true;
        }
        if (w == 0) EV(1);
        PTB(12);
        GTS(6);
        bar();
        GTS(7);
        PTB(13);
        if (!LAST && w == 0) {
            const f32x2 a = *(const LAS f32x2*)(redr), b2 = *(const LAS f32x2*)(redr + 256), c = *(const LAS f32x2*)(redr + 512), d = *(const LAS f32x2*)(redr + 768);
            const f32x2 t2 = (a + b2) + (c + d);
            const unsigned o = nopoison(pk2(t2.x, t2.y));
            AST1(o, vo_pp, ds, oM);
            PTB(14);
            EV(2);
            SCHEDB();
        }
        GTS(8);
        GTOUT(8, w);
        unsigned tb1 = 0, the = 0;
        if (!LAST && (w == 1 || w == 2)) {
            tb1 = rt32();
            the = tmax(th, tb1 + MINH + e1);
        }
        if (__builtin_expect(!LAST && r == 0 && w == 2, 0)) {
            const bool mret = (unsigned)__builtin_amdgcn_readfirstlane(ldist[0]) == (unsigned)(s + 1);
            if (mret && !dh_prev && !dm_prev && s > 1) a2 = min(a2 + EINC, EMAX);
            if (dh_prev && !dm_prev && s > 2) a1 = min(a1 + EINC, EMAX);
            if (!mret && !dh_prev) { if (++acnt >= EDEC) { acnt = 0; a1 = max(a1 - 1, 0); a2 = max(a2 - 1, 0); } } else acnt = 0;
            if (!mret) g8 += (min((int)(tb1 - tm), 120) * 8 - g8) >> 2;
            const unsigned n1 = (unsigned)(max((g8 >> 3) + GMARG - O1T, 0) + a1), n2 = (unsigned)(max((b8 >> 3) + BMARG - C2H, 0) + a2);
            const unsigned tmn = tmax(th, tb1 + (unsigned)MINHM + e1) + (unsigned)(C2H + ((dh_prev || mret) ? CUP : 0)) + n2;
            const unsigned tv = nopoison(lane == 0 ? tmn : (lane == 1 ? tmn + (unsigned)O1T + n1 : (lane == 2 ? n1 : n2)));
            dm_prev = mret;
            __builtin_amdgcn_s_sleep(TSDLY);
            AST1(tv, vo_ts, ds, oM ^ TSALT);
        }
#pragma unroll
        for (int it = 0; it < 2; ++it) {
            const char* pxb = lds + L_PX + lane * 16;
            const u32x4 x0 = *(const u32x4*)(pxb + (it * 2 + 0) * 1024), x1 = *(const u32x4*)(pxb + (it * 2 + 1) * 1024);
            f32x4 dx = mfma32(gE, x0, (f32x4){0, 0, 0, 0});
            dx = mfma32(gO, x1, dx);
            *(f32x4*)(lds + L_REDX + (2 * w + (q4 >> 1)) * 1024 + ((it * 2 + (q4 & 1)) * 16 + col) * 16) = dx;
        }
        if (!LAST && w == 1) {
            wait_until(the - TSEARLY);
            ALD1R(ts1, vo_ts, ds, oM ^ TSALT);
            asm volatile("s_waitcnt vmcnt(0)" : "+v"(ts1) :: "memory");
            if (__builtin_expect(__any(ts1 == POISON), 0)) {
                int spins = 0; const int lim = dead ? 1 : SPIN_MAX; bool bad;
                ldist[1] = sv1;
                _Pragma("nounroll") do {
                    ALD1R(ts1, vo_ts, ds, oM ^ TSALT);
                    asm volatile("s_waitcnt vmcnt(0)" : "+v"(ts1) :: "memory");
                    bad = __any(ts1 == POISON);
                } while (bad && ++spins < lim);
                if (bad) dead = true;
            }
            if (lane < 4) *(unsigned*)(lds + L_TS + lane * 4) = ts1;
        }
        bar();
        if (!LAST && w == 3) {
            asm volatile(
                "ds_read_b128 v[200:203], %[red]\n\t"
                "ds_read_b128 v[204:207], %[red] offset:16\n\t"
                "s_waitcnt lgkmcnt(1)\n\t"
                "v_add_f32 v200, v200, v202\n\tv_add_f32 v201, v201, v203\n\t"
                "s_waitcnt lgkmcnt(0)\n\t"
                "v_add_f32 v204, v204, v206\n\tv_add_f32 v205, v205, v207\n\t"
                "v_add_f32 v200, v200, v204\n\tv_add_f32 v201, v201, v205\n\t"
                "v_mov_b32 v202, v200\n\tv_mov_b32 v203, v201\n\t"
                "s_nop 0\n\t"
                "v_permlane16_swap_b32 v200, v202\n\tv_permlane16_swap_b32 v201, v203\n\t"
                "v_add_f32 v200, v200, v202\n\tv_add_f32 v201, v201, v203\n\t"
                "v_mov_b32 v202, v200\n\tv_mov_b32 v203, v201\n\t"
                "s_nop 0\n\t"
                "v_permlane32_swap_b32 v200, v202\n\tv_permlane32_swap_b32 v201, v203\n\t"
                "v_add_f32 v200, v200, v202\n\tv_add_f32 v201, v201, v203\n\t"
                "v_cvt_pk_bf16_f32 v200, v200, v201\n\t"
                "v_min_u32 v200, -2, v200\n\t"
                "buffer_store_dword v200, %[vo], %[ds], %[oM] offen sc1\n\t"
                :: [red] "v"(red2r), [vo] "v"(vo_mp), [ds] "s"(ds), [oM] "s"(oMn)
                : "memory", "v200", "v201", "v202", "v203", "v204", "v205", "v206", "v207");
            EV(0);
            SCHEDB();
        }
        const unsigned tb2 = rt32() - (w == 3 ? 9u : 0u);
        if (w == 1) EV(5);
        if (w == 2) {
            f32x4 t4 = *(const f32x4*)(lds + L_REDX + lane * 16);
#pragma unroll
            for (int pp = 1; pp < 4; ++pp) t4 += *(const f32x4*)(lds + L_REDX + 2 * pp * 1024 + lane * 16);
            const u32x4 xm = __builtin_bit_cast(u32x4, t4);
            AST4(xm, vo_xm, ds, (unsigned)s * (16u * 7u * 1024u));
        }
        if (!LAST) {
          const u32x4 t2 = *(const u32x4*)(lds + L_TS);
          dh_prev = (unsigned)__builtin_amdgcn_readfirstlane(ldist[1]) == (unsigned)(s + 1);
          if (r == 0 && w == 2 && !dh_prev) b8 += (min((int)(tb2 - the), 120) * 8 - b8) >> 2;
          e1 = (unsigned)__builtin_amdgcn_readfirstlane(t2.z); e2 = (unsigned)__builtin_amdgcn_readfirstlane(t2.w);
          tm = tmax((unsigned)__builtin_amdgcn_readfirstlane(t2.x), tb2 + MINM + e2); th = (unsigned)__builtin_amdgcn_readfirstlane(t2.y);
        }
    };
    _Pragma("nounroll") for (int s = 0; s < NSEG - 1; ++s) step(s, BoolC<false>{});
    step(NSEG - 1, BoolC<true>{});
    AWAIT(0);
    bar();
    if (w == 2 && lane == 0) AST1(1u, (unsigned)(OFF_FLAG + (FLAG_DONE + r) * 4), ds, 0u);
}

DEVI void chain_bw(const Params& p, int r, char* lds, u32x4 ds) {
    const rsrc_t rs = mkrsrc(p.ws, 0x10000000u);
    const int tid = threadIdx.x, lane = tid & 63, w = __builtin_amdgcn_readfirstlane(tid >> 6), hw = w & 3;
    const int col = lane & 15, q4 = lane >> 4;
    const int c7 = r % 7, beta = r >> 1, rh = r & 1;
    u32x4 bw[2][14];
#pragma unroll
    for (int a = 0; a < 2; ++a)
#pragma unroll
        for (int t = 0; t < 14; ++t) bw[a][t] = *(const u32x4*)(p.ws + OFF_BF + ((size_t)((2 * hw + a) * 56 + beta) * 14 + t) * FR + lane * 16);
    u32x4 sel;
    { const int j = col & 7; const unsigned one = (j & 1) ? 0x3F800000u : 0x00003F80u; sel.x = (j >> 1) == 0 ? one : 0; sel.y = (j >> 1) == 1 ? one : 0; sel.z = (j >> 1) == 2 ? one : 0; sel.w = (j >> 1) == 3 ? one : 0; }
    const unsigned cfg = get_cfg(p, rs, lds);
    WAITVM0();
    bool dead = false;
    const int hh = col >> 2, i2 = col & 3, hB = 4 * hw + hh;
    const unsigned vo_p0 = (unsigned)((rh ? 16384 : 32768) + hB * 512 + q4 * 64 + i2 * 16);
    const unsigned vo_pa = ((hB & SLOTSH) && q4 == 0) ? OOB : vo_p0;
    const unsigned vo_pb = (!(hB & SLOTSH) && q4 == 3) ? OOB : vo_p0 + 256u;
    const unsigned vo_stat = (unsigned)(STATS_OFF + stat_off(hB) + rh * 64 + i2 * 4);
    const unsigned vo_ex = (unsigned)(49152 + rh * 4096 + hB * 256 + q4 * 64 + i2 * 16);
    const unsigned vo_u0 = (unsigned)(OFF_UF + ((size_t)beta * 8 + 2 * hw) * FR + lane * 16), vo_u1 = vo_u0 + 1024;
    const unsigned vo_x = (unsigned)(OFF_XF + (size_t)((hw >> 1) * 14 + 2 * c7 + (hw & 1)) * FR + lane * 16);
    const unsigned vo_fu = (unsigned)(OFF_FLAG + (FLAG_U + beta / 14) * 4);
    const unsigned vo_wc = (rh == 0 && lane < 16) ? (unsigned)(OFF_WC + ((size_t)beta * 8 + 2 * hw) * 256 + col * 16) : OOB;
    const bool diag = (col >> 3) == (q4 >> 1);
    const bool outv = (hh & 1) == (q4 >> 1);
    const unsigned long long lomask = 0x00FF00FF00FF00FFull;
    unsigned mlo = (hh < 2) ? 0xFFFFFFFFu : 0u, mhi = ~mlo;
    u32x4 ones = {0x3F803F80u, 0x3F803F80u, 0x3F803F80u, 0x3F803F80u};
    LAS char* red2 = (LAS char*)lds + (outv ? L_RED2 + ((i2 * 4 + 2 * (q4 & 1)) * 36 + hB * 2) * 4 : L_XX + 2048 + hw * 1024 + lane * 8);
    asm volatile("" : "+v"(mlo), "+v"(mhi), "+v"(ones), "+v"(red2));
    u32x4 un0 = {0,0,0,0}, un1 = {0,0,0,0}, px = {0,0,0,0};
    unsigned fu = 0;
    u32x4 pa = {0,0,0,0}, pb = {0,0,0,0}, ex = {0,0,0,0}; unsigned st = 0;
    unsigned th = 0;
    volatile LAS unsigned* ldist = (volatile LAS unsigned*)((LAS char*)lds + L_RF + 4);
    await_flag(ds, vo_fu, 8, dead);
    ALD4R(un0, vo_u0, ds, 0u); ALD4R(un1, vo_u1, ds, 0u); ALD4PR(px, vo_x, ds, 0u);
    ALD1R(fu, vo_fu, ds, 64u);
    const unsigned vo_s0 = (unsigned)(2 * hw * 1024 + lane * 16), vo_s1 = (hw < 3) ? vo_s0 + 1024u : OOB;
    LAS char* ls0 = (LAS char*)lds + L_R + vo_s0; LAS char* ls1 = (LAS char*)lds + L_R + ((hw < 3) ? vo_s0 + 1024u : (unsigned)(L_XX + lane * 16));
    asm volatile("" : "+v"(ls0), "+v"(ls1));
    const unsigned vo_f = (unsigned)(lane * 16);
    unsigned tm = start_sync(p, ds, r, lds), e1 = 0;
    th = tm + O1T;
    auto step = [&](const int s, auto LASTC) {
        constexpr bool LAST = decltype(LASTC)::value;
        const unsigned oM = (unsigned)__builtin_amdgcn_readfirstlane((int)chunk_off(p, cfg, s, 0));
        unsigned sv1 = (unsigned)(s + 1); PIN(sv1);
        wait_fine(tm);
        GTDECL();
        GTS(0);
        __builtin_amdgcn_s_sleep(BDLY);
        u32x4 S0, S1;
        ALD4(S0, vo_s0, ds, oM);
        ALD4(S1, vo_s1, ds, oM);
        asm volatile("s_waitcnt vmcnt(2)" : "+v"(un0), "+v"(un1), "+v"(px), "+v"(fu) :: "memory");
        SCHEDB();
        u32x4 uf0 = un0, uf1 = un1;
        asm volatile("" : "+v"(uf0), "+v"(uf1));
        const unsigned fu_s = (unsigned)__builtin_amdgcn_readfirstlane(fu);
        *(u32x4*)(lds + L_PX + hw * 1024 + lane * 16) = px;
        GTS(1);
        asm volatile("s_waitcnt vmcnt(0)" : "+v"(S0), "+v"(S1) :: "memory");
        GTS(2);
        SCHEDB();
        *(volatile LAS u32x4*)(ls0) = S0;
        *(volatile LAS u32x4*)(ls1) = S1;
        GTS(3);
        bar();
        GTS(4);
        const unsigned tb1 = rt32();
        SCHEDB();
        u32x4 Rq[7];
#pragma unroll
        for (int q = 0; q < 7; ++q) Rq[q] = *(const u32x4*)(lds + L_R + q * 1024 + lane * 16);
        SCHEDB();
        f32x4 ae[2] = {{0,0,0,0},{0,0,0,0}}, ao[2] = {{0,0,0,0},{0,0,0,0}};
        const unsigned the = tmax(th, tb1 + MINH + e1);
#pragma unroll
        for (int q = 0; q < WSPLIT; ++q)
#pragma unroll
            for (int a = 0; a < 2; ++a) { ae[a] = mfma32(Rq[q], bw[a][2 * q], ae[a]); ao[a] = mfma32(Rq[q], bw[a][2 * q + 1], ao[a]); }
        {
            unsigned mq = max4(Rq[0]);
#pragma unroll
            for (int q = 1; q < 7; ++q) mq = max(mq, max4(Rq[q]));
            if (__builtin_expect(__any(mq == POISON), 0)) {
                int spins = 0; const int lim = dead ? 1 : SPIN_MAX; bool bad;
                ldist[0] = sv1;
                unsigned oR = oM ^ ALT;
                _Pragma("nounroll") do {
#pragma unroll
                    for (int q = 0; q < 7; ++q) ALD4(Rq[q], vo_f + (unsigned)q * 1024u, ds, oR);
                    oR ^= ALT;
                    asm volatile("s_waitcnt vmcnt(0)" : "+v"(Rq[0]), "+v"(Rq[1]), "+v"(Rq[2]), "+v"(Rq[3]), "+v"(Rq[4]), "+v"(Rq[5]), "+v"(Rq[6]) :: "memory");
                    mq = max4(Rq[0]);
#pragma unroll
                    for (int q = 1; q < 7; ++q) mq = max(mq, max4(Rq[q]));
                    bad = __any(mq == POISON);
                } while (bad && ++spins < lim);
                if (bad) dead = true;
#pragma unroll
                for (int a = 0; a < 2; ++a) { ae[a] = (f32x4){0, 0, 0, 0}; ao[a] = (f32x4){0, 0, 0, 0}; }
#pragma unroll
                for (int q = 0; q < WSPLIT; ++q)
#pragma unroll
                    for (int a = 0; a < 2; ++a) { ae[a] = mfma32(Rq[q], bw[a][2 * q], ae[a]); ao[a] = mfma32(Rq[q], bw[a][2 * q + 1], ao[a]); }
            }
        }
        SCHEDB();
        if (!LAST) {
            wait_fine(the);
            if (w == 6) EV(6);
            GTS(5);
            ALD4R(pa, vo_pa, ds, oM);
            ALD4R(pb, vo_pb, ds, oM);
            ALD1R(st, vo_stat, ds, oM);
            ALD4R(ex, vo_ex, ds, oM);
            SCHEDB();
        }
#pragma unroll
        for (int q = WSPLIT; q < 7; ++q)
#pragma unroll
            for (int a = 0; a < 2; ++a) { ae[a] = mfma32(Rq[q], bw[a][2 * q], ae[a]); ao[a] = mfma32(Rq[q], bw[a][2 * q + 1], ao[a]); }
        u32x2 wop[2]; u32x4 wst[2];
#pragma unroll
        for (int a = 0; a < 2; ++a) {
            float wv[4];
#pragma unroll
            for (int rr = 0; rr < 4; ++rr) { const float x = (lane < 32) ? ae[a][rr] : ao[a][rr]; float u, v; swap32(x, u, v); wv[rr] = u + v; }
            const u32x2 wp = {pk2(wv[0], wv[1]), pk2(wv[2], wv[3])};
            wop[a].x = diag ? wp.x : 0u; wop[a].y = diag ? wp.y : 0u;
            unsigned a0, b0, a1, b1; uswap16(wp.x, a0, b0); uswap16(wp.y, a1, b1); wst[a] = (u32x4){a0, a1, b0, b1};
        }
        asm volatile("" : "+v"(wop[0]), "+v"(wop[1]), "+v"(wst[0]), "+v"(wst[1]));
        SCHEDB();
        if (!LAST) {
            const u32x4 wA = {wop[0].x, wop[0].y, wop[1].x, wop[1].y};
            unsigned long long bad64;
#define BSTAGE() \
            asm volatile( \
                MF "v[216:219], %[sel], %[pa], 0\n\t" \
                MF "v[216:219], %[sel], %[pb], v[216:219]\n\t" \
                "v_max3_u32 v248, %[pax], %[pay], %[paz]\n\tv_max3_u32 v248, v248, %[paw], %[pbx]\n\t" \
                MF "v[220:223], %[uf0], %[ex], 0\n\t" \
                "v_max3_u32 v248, v248, %[pby], %[pbz]\n\tv_max3_u32 v248, v248, %[pbw], %[exx]\n\t" \
                MF "v[224:227], %[uf1], %[ex], 0\n\t" \
                "v_max3_u32 v248, v248, %[exy], %[exz]\n\tv_max3_u32 v248, v248, %[exw], %[st]\n\t" \
                MF "v[228:231], %[ones], %[ex], 0\n\t" \
                "v_max3_f32 v232, v216, v217, v218\n\tv_max3_f32 v232, v232, v219, %[st]\n\t" \
                "v_mov_b32 v233, v232\n\t" \
                "v_cmp_eq_u32_e64 %[bad], -1, v248\n\t" \
                "s_nop 0\n\t" \
                "v_permlane16_swap_b32 v232, v233\n\t" \
                "v_max_f32 v232, v232, v233\n\t" \
                "v_mul_f32 v234, 0xbdb8aa3b, v232\n\t" \
                "v_fmamk_f32 v235, %[st], 0x3db8aa3b, v234\n\t" \
                "v_fmamk_f32 v236, v216, 0x3db8aa3b, v234\n\tv_fmamk_f32 v237, v217, 0x3db8aa3b, v234\n\t" \
                "v_exp_f32 v235, v235\n\t" \
                "v_fmamk_f32 v238, v218, 0x3db8aa3b, v234\n\tv_fmamk_f32 v239, v219, 0x3db8aa3b, v234\n\t" \
                "v_exp_f32 v236, v236\n\tv_exp_f32 v237, v237\n\t" \
                "v_cndmask_b32_e64 v244, v224, v220, %[lo]\n\tv_cndmask_b32_e64 v245, v225, v221, %[lo]\n\t" \
                "v_exp_f32 v238, v238\n\tv_exp_f32 v239, v239\n\t" \
                "v_cndmask_b32_e64 v246, v226, v222, %[lo]\n\tv_cndmask_b32_e64 v247, v227, v223, %[lo]\n\t" \
                "v_cvt_pk_bf16_f32 v249, v236, v237\n\tv_cvt_pk_bf16_f32 v250, v238, v239\n\t" \
                "v_mul_f32 v244, v244, v235\n\tv_mul_f32 v245, v245, v235\n\t" \
                "v_and_b32 v240, v249, %[mlo]\n\tv_and_b32 v241, v250, %[mlo]\n\tv_and_b32 v242, v249, %[mhi]\n\tv_and_b32 v243, v250, %[mhi]\n\t" \
                "v_mul_f32 v246, v246, v235\n\tv_mul_f32 v247, v247, v235\n\t" \
                "v_add_f32 v236, v236, v237\n\tv_add_f32 v238, v238, v239\n\t" \
                MF "v[244:247], %[wA], v[240:243], v[244:247]\n\t" \
                "v_add_f32 v232, v236, v238\n\t" \
                "v_mov_b32 v233, v232\n\t" \
                "s_nop 1\n\t" \
                "v_permlane16_swap_b32 v232, v233\n\t" \
                "v_add_f32 v232, v232, v233\n\t" \
                "v_fmac_f32 v232, v235, v228\n\t" \
                "v_rcp_f32 v232, v232\n\t" \
                "s_nop 0\n\t" \
                "v_mul_f32 v244, v244, v232\n\tv_mul_f32 v245, v245, v232\n\tv_mul_f32 v246, v246, v232\n\tv_mul_f32 v247, v247, v232\n\t" \
                "ds_write2_b64 %[red], v[244:245], v[246:247] offset1:18\n\t" \
                : [bad] "=s"(bad64) \
                : [sel] "v"(sel), [pa] "v"(pa), [pb] "v"(pb), [ex] "v"(ex), [uf0] "v"(uf0), [uf1] "v"(uf1), [ones] "v"(ones), [wA] "v"(wA), [st] "v"(st), \
                  [pax] "v"(pa.x), [pay] "v"(pa.y), [paz] "v"(pa.z), [paw] "v"(pa.w), [pbx] "v"(pb.x), [pby] "v"(pb.y), [pbz] "v"(pb.z), [pbw] "v"(pb.w), \
                  [exx] "v"(ex.x), [exy] "v"(ex.y), [exz] "v"(ex.z), [exw] "v"(ex.w), [lo] "s"(lomask), [mlo] "v"(mlo), [mhi] "v"(mhi), [red] "v"(red2) \
                : "memory", "v216", "v217", "v218", "v219", "v220", "v221", "v222", "v223", "v224", "v225", "v226", "v227", "v228", "v229", "v230", "v231", "v232", "v233", \
                  "v234", "v235", "v236", "v237", "v238", "v239", "v240", "v241", "v242", "v243", "v244", "v245", "v246", "v247", "v248", "v249", "v250")
            if (__builtin_expect(s + 1 < NSEG - 1 && fu_s < 8u, 0)) await_flag(ds, vo_fu + (unsigned)(s + 1) * 64u, 8, dead);
            {
                const unsigned sn = (unsigned)(s + 1);
                ALD4R(un0, vo_u0, ds, sn * (56u * 8u * 1024u));
                ALD4R(un1, vo_u1, ds, sn * (56u * 8u * 1024u));
                ALD4PR(px, vo_x, ds, sn * 28672u);
                ALD1R(fu, vo_fu, ds, (sn + 1u) * 64u);
            }
            PTB(0);
            GTS(6);
            asm volatile("s_waitcnt vmcnt(4)" : "+v"(pa), "+v"(pb), "+v"(st), "+v"(ex) :: "memory");
            GTS(7);
            PTB(1);
            SCHEDB();
            BSTAGE();
            if (__builtin_expect(bad64 != 0ull, 0)) {
              int spins = 0; const int lim = dead ? 1 : SPIN_MAX; bool bad;
              ldist[1] = sv1;
              unsigned oR = oM ^ ALT;
              _Pragma("nounroll") do {
                ALD4R(pa, vo_pa, ds, oR);
                ALD4R(pb, vo_pb, ds, oR);
                ALD1R(st, vo_stat, ds, oR);
                ALD4R(ex, vo_ex, ds, oR);
                oR ^= ALT;
                asm volatile("s_waitcnt vmcnt(0)" : "+v"(pa), "+v"(pb), "+v"(st), "+v"(ex) :: "memory");
                bad = __any(max(max(max4(pa), max4(pb)), max(max4(ex), st)) == POISON);
              } while (bad && ++spins < lim);
              if (bad) dead = true;
              BSTAGE();
            }
            if (w == 7) EV(3);
            PTB(3);
            GTS(8);
        }
        bar();
        const unsigned tb2 = rt32();
        if (!LAST) PTB(4);
        if (LAST || WPOS != 1) { AST4(wst[0], vo_wc, ds, (unsigned)s * (56u * 8u * 256u)); AST4(wst[1], vo_wc + 256u, ds, (unsigned)s * (56u * 8u * 256u)); }
        if (!LAST) {
            const u32x4 t2 = *(const u32x4*)(lds + L_TS);
            e1 = (unsigned)__builtin_amdgcn_readfirstlane(t2.z);
            tm = tmax((unsigned)__builtin_amdgcn_readfirstlane(t2.x), tb2 + MINM + (unsigned)__builtin_amdgcn_readfirstlane(t2.w)); th = (unsigned)__builtin_amdgcn_readfirstlane(t2.y);
        }
    };
    _Pragma("nounroll") for (int s = 0; s < NSEG - 1; ++s) step(s, BoolC<false>{});
    step(NSEG - 1, BoolC<true>{});
    AWAIT(0);
    bar();
}

DEVI void pretouch(const Params& p) {
    const int lane = threadIdx.x & 63;
    const unsigned off = (unsigned)lane * (2u << 20);
    if (off < (unsigned)p.comm_off + (4u << 20)) { const unsigned v = *(const volatile unsigned*)(p.ws + off); asm volatile("" :: "v"(v)); }
}
DEVI void chain_g(const Params& p, int r, char* lds) {
    const int w = __builtin_amdgcn_readfirstlane(threadIdx.x >> 6);
    const u32x4 ds = mkdesc(p.ws, 0x10000000u);
    const unsigned t_entry = rt32();
    if (w == 3) pretouch(p);
    __builtin_amdgcn_s_sleep(GDELAY);
    if (w < 4) chain_gw(p, r, lds, ds, t_entry); else chain_bw(p, r, lds, ds);
}

DEVI void x_finalize(rsrc_t rs, f32x4 sm, f32x4 s0, f32x4 s1, int s, int h, int it, int lane) {
    const int col = lane & 15, q4 = lane >> 4;
    const float NEG = -3.0e38f;
    float v0[4], v1[4], vm[4];
#pragma unroll
    for (int rr = 0; rr < 4; ++rr) {
        const bool causal = (4 * q4 + rr) <= col;
        v0[rr] = (it == 1 || causal) ? s0[rr] : NEG;
        v1[rr] = (it == 1 && causal) ? s1[rr] : NEG;
        vm[rr] = (q4 < 2) ? sm[rr] : NEG;
    }
    float m = NEG;
#pragma unroll
    for (int rr = 0; rr < 4; ++rr) m = fmaxf(m, fmaxf(fmaxf(v0[rr], v1[rr]), vm[rr]));
    { float a, b2; swap16(m, a, b2); m = fmaxf(a, b2); swap32(m, a, b2); m = fmaxf(a, b2); }
    const float nM = -m * SC2;
    float l = 0;
#pragma unroll
    for (int rr = 0; rr < 4; ++rr) {
        v0[rr] = (v0[rr] == NEG) ? 0.f : __builtin_amdgcn_exp2f(__builtin_fmaf(v0[rr], SC2, nM));
        v1[rr] = (v1[rr] == NEG) ? 0.f : __builtin_amdgcn_exp2f(__builtin_fmaf(v1[rr], SC2, nM));
        vm[rr] = (vm[rr] == NEG) ? 0.f : __builtin_amdgcn_exp2f(__builtin_fmaf(vm[rr], SC2, nM));
        l += v0[rr] + v1[rr] + vm[rr];
    }
    { float a, b2; swap16(l, a, b2); l = a + b2; swap32(l, a, b2); l = a + b2; }
    const float inv = __builtin_amdgcn_rcpf(l);
    const u32x4 px = {pk2(v0[0] * inv, v0[1] * inv), pk2(v0[2] * inv, v0[3] * inv), pk2(v1[0] * inv, v1[1] * inv), pk2(v1[2] * inv, v1[3] * inv)};
    bst4(rs, px, (unsigned)(OFF_PX + (((size_t)s * 16 + h) * 2 + it) * FR + lane * 16), 0);
    if (q4 < 2) { const u32x2 pm = {pk2(vm[0] * inv, vm[1] * inv), pk2(vm[2] * inv, vm[3] * inv)};
        bst2(rs, pm, (unsigned)(OFF_PM + (((size_t)s * 8 + (h >> 1)) * 2 + it) * 512 + (col + 16 * (2 * (h & 1) + q4)) * 8), 0); }
}

DEVI void chain_x(const Params& p, int h, char* lds) {
    const int tid = threadIdx.x, lane = tid & 63, w = __builtin_amdgcn_readfirstlane(tid >> 6);
    const int col = lane & 15, q4 = lane >> 4;
    const rsrc_t rs = mkrsrc(p.ws, 0x10000000u);
    const u32x4 ds = mkdesc(p.ws, 0x10000000u);
    if (w == 0 && (h & 6) == 0) probe_cfg(p, rs, (h & 1) + 2 * (h >> 3));
    const unsigned cfg = get_cfg(p, rs, lds);
    WAITVM0();
    bool dead = false;
    if (w < 4) {
        const int nq = (w < 3) ? 2 : 1;
        const unsigned vo_m0 = (unsigned)(2 * w * 1024 + lane * 16), vo_m1 = (nq == 2) ? vo_m0 + 1024u : OOB;
        LAS char* redw = (LAS char*)lds + L_REDX + w * 1024 + lane * 16;
        unsigned mE = (col < 8) ? 0xFFFFFFFFu : 0u;
        asm volatile("" : "+v"(redw), "+v"(mE));
        f32x4 o = {0, 0, 0, 0};
        const unsigned vo_e = (col < 8) ? (unsigned)(49152 + ((col >> 2) & 1) * 4096 + h * 256 + q4 * 64 + (col & 3) * 16)
                            : ((lane == 8 || lane == 12) ? (unsigned)(STATS_OFF + stat_off(h) + ((lane >> 2) & 1) * 64) : OOB);
        unsigned tmx = start_sync(p, ds, 112 + h, lds);
        unsigned thx = tmx + O1T;
        __builtin_amdgcn_s_setprio(3);
        bar();
        for (int s = 0; s < NSEG; ++s) {
            const unsigned oM = (unsigned)__builtin_amdgcn_readfirstlane((int)chunk_off(p, cfg, s, 0));
            if (s < NSEG - 1) {
                u32x4 g[2][2][2];
#pragma unroll
                for (int qi = 0; qi < 2; ++qi)
#pragma unroll
                    for (int e = 0; e < 2; ++e)
#pragma unroll
                        for (int k = 0; k < 2; ++k) g[qi][e][k] = *(const u32x4*)(lds + L_GX + (s & 1) * 28672 + (k * 14 + 2 * ((w < 3) ? 2 * w + qi : 2 * w) + e) * 1024 + lane * 16);
                if (s > 0) {
                    wait_until(thx);
                    unsigned t1 = 0; int spins = 0; const int lim = dead ? 1 : SPIN_MAX; bool bad;
                    const unsigned oT = (unsigned)__builtin_amdgcn_readfirstlane((int)chunk_off(p, cfg, s - 1, 0)) ^ TSALT; const unsigned vts = (unsigned)(TS_OFF + (lane & 1) * 4);
                    _Pragma("nounroll") do {
                        ALD1R(t1, vts, ds, oT);
                        asm volatile("s_waitcnt vmcnt(0)" : "+v"(t1) :: "memory");
                        bad = __any(t1 == POISON);
                        if (bad) __builtin_amdgcn_s_sleep(4);
                    } while (bad && ++spins < lim);
                    if (bad) dead = true;
                    thx = (unsigned)__builtin_amdgcn_readlane(t1, 1);
                    tmx = (unsigned)__builtin_amdgcn_readlane(t1, 0);
                }
                wait_fine(tmx + XOFFT);
                unsigned long long bad64;
#define XSTAGE(OFF) \
                asm volatile( \
                    "buffer_load_dwordx4 v[200:203], %[m0], %[ds], %[oM] offen sc1\n\t" \
                    "buffer_load_dwordx4 v[204:207], %[m1], %[ds], %[oM] offen sc1\n\t" \
                    "s_waitcnt vmcnt(1)\n\t" \
                    "v_and_b32 v212, v200, %[mE]\n\tv_and_b32 v213, v201, %[mE]\n\tv_and_b32 v214, v202, %[mE]\n\tv_and_b32 v215, v203, %[mE]\n\t" \
                    "v_and_b32_dpp v216, v200, %[mE]" DPP8 "v_and_b32_dpp v217, v201, %[mE]" DPP8 \
                    MF "v[236:239], %[g000], v[212:215], 0\n\t" \
                    "v_and_b32_dpp v218, v202, %[mE]" DPP8 "v_and_b32_dpp v219, v203, %[mE]" DPP8 \
                    MF "v[240:243], %[g001], v[212:215], 0\n\t" \
                    "v_max3_u32 v196, v200, v201, v202\n\tv_max_u32 v196, v196, v203\n\t" \
                    MF "v[236:239], %[g010], v[216:219], v[236:239]\n\t" \
                    "s_waitcnt vmcnt(0)\n\t" \
                    "v_and_b32 v220, v204, %[mE]\n\tv_and_b32 v221, v205, %[mE]\n\t" \
                    MF "v[240:243], %[g011], v[216:219], v[240:243]\n\t" \
                    "v_and_b32 v222, v206, %[mE]\n\tv_and_b32 v223, v207, %[mE]\n\t" \
                    "v_and_b32_dpp v224, v204, %[mE]" DPP8 "v_and_b32_dpp v225, v205, %[mE]" DPP8 \
                    MF "v[236:239], %[g100], v[220:223], v[236:239]\n\t" \
                    "v_and_b32_dpp v226, v206, %[mE]" DPP8 "v_and_b32_dpp v227, v207, %[mE]" DPP8 \
                    MF "v[240:243], %[g101], v[220:223], v[240:243]\n\t" \
                    "v_max3_u32 v196, v196, v204, v205\n\tv_max3_u32 v196, v196, v206, v207\n\t" \
                    MF "v[240:243], %[g111], v[224:227], v[240:243]\n\t" \
                    MF "v[236:239], %[g110], v[224:227], v[236:239]\n\t" \
                    "v_cmp_eq_u32_e64 %[bad], -1, v196\n\t" \
                    "s_nop 6\n\t" \
                    "v_add_f32_dpp v192, v240, v236" DPP8 "v_add_f32_dpp v193, v241, v237" DPP8 "v_add_f32_dpp v194, v242, v238" DPP8 "v_add_f32_dpp v195, v243, v239" DPP8 \
                    "ds_write_b128 %[red], v[192:195]\n\t" \
                    : "={v[192:195]}"(o), [bad] "=s"(bad64) \
                    : [m0] "v"(vo_m0), [m1] "v"(vo_m1), [ds] "s"(ds), [oM] "s"(OFF), [mE] "v"(mE), [red] "v"(redw), \
                      [g000] "v"(g[0][0][0]), [g001] "v"(g[0][0][1]), [g010] "v"(g[0][1][0]), [g011] "v"(g[0][1][1]), \
                      [g100] "v"(g[1][0][0]), [g101] "v"(g[1][0][1]), [g110] "v"(g[1][1][0]), [g111] "v"(g[1][1][1]) \
                    : "memory", "v196", "v200", "v201", "v202", "v203", "v204", "v205", "v206", "v207", "v212", "v213", "v214", "v215", "v216", "v217", "v218", "v219", \
                      "v220", "v221", "v222", "v223", "v224", "v225", "v226", "v227", "v236", "v237", "v238", "v239", "v240", "v241", "v242", "v243")
                XSTAGE(oM);
                if (__builtin_expect(bad64 != 0ull, 0)) {
                    int spins = 0; const int lim = dead ? 1 : SPIN_MAX; bool bad;
                    unsigned oR = oM ^ ALT;
                    _Pragma("nounroll") do { XSTAGE(oR); oR ^= ALT; bad = bad64 != 0ull; } while (bad && ++spins < lim);
                    if (bad) dead = true;
                }
            }
            bar();
            if (w == 0 && s < NSEG - 1) {
                asm volatile(
                    "ds_read_b128 v[200:203], %[red] offset:1024\n\t"
                    "ds_read_b128 v[204:207], %[red] offset:2048\n\t"
                    "ds_read_b128 v[208:211], %[red] offset:3072\n\t"
                    "s_mov_b64 vcc, %[hi]\n\t"
                    "s_waitcnt lgkmcnt(2)\n\t"
                    "v_add_f32 v192, v192, v200\n\tv_add_f32 v193, v193, v201\n\tv_add_f32 v194, v194, v202\n\tv_add_f32 v195, v195, v203\n\t"
                    "s_waitcnt lgkmcnt(0)\n\t"
                    "v_add_f32 v204, v204, v208\n\tv_add_f32 v205, v205, v209\n\tv_add_f32 v206, v206, v210\n\tv_add_f32 v207, v207, v211\n\t"
                    "v_add_f32 v192, v192, v204\n\tv_add_f32 v193, v193, v205\n\tv_add_f32 v194, v194, v206\n\tv_add_f32 v195, v195, v207\n\t"
                    "v_max3_f32 v196, v192, v193, v194\n\tv_max_f32 v196, v196, v195\n\t"
                    "s_nop 1\n\t"
                    "v_max_f32_dpp v196, v196, v196" DPP8
                    "v_mov_b32 v197, v196\n\t"
                    "s_nop 1\n\t"
                    "v_permlane16_swap_b32 v196, v197\n\t"
                    "v_max_f32 v196, v196, v197\n\t"
                    "v_mov_b32 v197, v196\n\t"
                    "s_nop 1\n\t"
                    "v_permlane32_swap_b32 v196, v197\n\t"
                    "v_max_f32 v196, v196, v197\n\t"
                    "v_mul_f32 v197, 0xbdb8aa3b, v196\n\t"
                    "v_min_u32 v196, -2, v196\n\t"
                    "v_fmamk_f32 v192, v192, 0x3db8aa3b, v197\n\tv_fmamk_f32 v193, v193, 0x3db8aa3b, v197\n\t"
                    "v_fmamk_f32 v194, v194, 0x3db8aa3b, v197\n\tv_fmamk_f32 v195, v195, 0x3db8aa3b, v197\n\t"
                    "v_exp_f32 v192, v192\n\tv_exp_f32 v193, v193\n\tv_exp_f32 v194, v194\n\tv_exp_f32 v195, v195\n\t"
                    "v_mov_b32_dpp v200, v196 quad_perm:[0,0,0,0] row_mask:0xf bank_mask:0xf bound_ctrl:1\n\t"
                    "v_mov_b32_dpp v201, v196 quad_perm:[1,1,1,1] row_mask:0xf bank_mask:0xf bound_ctrl:1\n\t"
                    "v_cvt_pk_bf16_f32 v192, v192, v193\n\tv_cvt_pk_bf16_f32 v193, v194, v195\n\t"
                    "v_mov_b32_dpp v202, v196 quad_perm:[2,2,2,2] row_mask:0xf bank_mask:0xf bound_ctrl:1\n\t"
                    "v_mov_b32_dpp v203, v196 quad_perm:[3,3,3,3] row_mask:0xf bank_mask:0xf bound_ctrl:1\n\t"
                    "v_min_u32 v192, -2, v192\n\tv_min_u32 v193, -2, v193\n\t"
                    "v_cndmask_b32 v204, v192, v200, vcc\n\tv_cndmask_b32 v205, v193, v201, vcc\n\t"
                    "v_cndmask_b32_dpp v206, v192, v202, vcc" DPP8 "v_cndmask_b32_dpp v207, v193, v203, vcc" DPP8
                    "buffer_store_dwordx4 v[204:207], %[vo], %[ds], %[oM] offen sc1\n\t"
                    ".if %c[alt]\n\tbuffer_store_dwordx4 v[204:207], %[vo], %[ds], %[oB] offen sc1\n\t.endif\n\ts_nop 1"
                    : "+{v[192:195]}"(o)
                    : [red] "v"(redw), [hi] "s"(0xFF00FF00FF00FF00ull), [vo] "v"(vo_e), [ds] "s"(ds), [oM] "s"(oM), [oB] "s"(oM ^ ALT), [alt] "n"(ALT)
                    : "memory", "vcc", "v196", "v197", "v200", "v201", "v202", "v203", "v204", "v205", "v206", "v207", "v208", "v209", "v210", "v211");
            }
        }
        bar();
    } else {
        const int hw = w - 4;
        u32x4 gb[7];
#pragma unroll
        for (int k = 0; k < 7; ++k) gb[k] = (u32x4){0, 0, 0, 0};
        const unsigned vo_fg = (unsigned)(OFF_FLAG + (FLAG_GX + h) * 4);
        const unsigned vo_g = (unsigned)(OFF_GX + ((size_t)h * 28 + hw * 7) * FR + lane * 16);
        auto issue = [&](int sg) {
#pragma unroll
            for (int k = 0; k < 7; ++k) ALD4R(gb[k], vo_g + (unsigned)k * 1024u, ds, (unsigned)sg * (16u * 28u * 1024u));
        };
        auto put = [&](int sg) {
            asm volatile("s_waitcnt vmcnt(0)" : "+v"(gb[0]), "+v"(gb[1]), "+v"(gb[2]), "+v"(gb[3]), "+v"(gb[4]), "+v"(gb[5]), "+v"(gb[6]) :: "memory");
            SCHEDB();
#pragma unroll
            for (int k = 0; k < 7; ++k) *(u32x4*)(lds + L_GX + (sg & 1) * 28672 + (hw * 7 + k) * 1024 + lane * 16) = gb[k];
            SCHEDB();
        };
        await_flag(ds, vo_fg, 2, dead);
        issue(0);
        (void)start_sync(p, ds, 112 + h, lds);
        put(0);
        bar();
        await_flag(ds, vo_fg + 64u, 2, dead);
        issue(1);
        _Pragma("nounroll") for (int s = 0; s < NSEG; ++s) {
            __builtin_amdgcn_s_sleep(XHD);
            if (h == 0 && w == 7 && s >= 3 && lane == 0) AST1(1u, (unsigned)(OFF_FLAG + FLAG_PROG * 4), ds, (unsigned)(s - 3) * 128u);
            put(s + 1);
            bar();
            __builtin_amdgcn_s_sleep(XHD2);
            const int sn = (s + 2 < NSEG) ? s + 2 : NSEG - 1;
            await_flag(ds, vo_fg + (unsigned)sn * 64u, 2, dead);
            issue(__builtin_amdgcn_readfirstlane(sn));
        }
        AWAIT(0);
        bar();
    }
}

DEVI void wait_prog(const Params& p, int s, bool& dead) {
    const int w = __builtin_amdgcn_readfirstlane(threadIdx.x >> 6), lane = threadIdx.x & 63;
    if (w == 0) {
        const unsigned t0 = __builtin_amdgcn_readfirstlane(ldflag(p, FLAG_T0));
        if (t0) {
            const unsigned target = t0 + (unsigned)((s <= NSEG - 4) ? (s + 2) * (O1T + C2H) + 60 : (NSEG - 1) * (O1T + C2H) + 90);
            int g = 0;
            _Pragma("nounroll") while ((int)(rt32() - target) < 0 && ++g < 20000) __builtin_amdgcn_s_sleep(8);
        }
        int k = dead ? SPIN_MAX : 0;
        _Pragma("nounroll") for (; k < SPIN_MAX; ++k) {
            unsigned v;
            if (s <= NSEG - 4) v = ldflag(p, FLAG_PROG + s * 32);
            else { const unsigned a = ldflag(p, FLAG_DONE + lane), b2 = ldflag(p, FLAG_DONE + (lane < 48 ? 64 + lane : 0)); v = a & b2; }
            if (!__any(v == 0u)) break;
            if (s <= NSEG - 5) __builtin_amdgcn_s_sleep(8); else __builtin_amdgcn_s_sleep(2);
        }
        if (k >= SPIN_MAX) dead = true;
    }
    __syncthreads();
}

DEVI void fin_unit(const Params& p, int s, int half, char* lds, bool& dead) {
    const int tid = threadIdx.x, lane = tid & 63, w = __builtin_amdgcn_readfirstlane(tid >> 6);
    const int h = 8 * half + w;
    const rsrc_t rs = mkrsrc(p.ws, 0x10000000u);
    __syncthreads();
    waitflag(p, FLAG_GX + s * 16 + h, 2, dead);
    u32x4 xs[4], g[28];
#pragma unroll
    for (int j = 0; j < 4; ++j) { const int f = (w + 8 * j < 28) ? w + 8 * j : w; xs[j] = *(const u32x4*)(p.ws + OFF_XF + ((size_t)s * 28 + f) * FR + lane * 16); }
#pragma unroll
    for (int f = 0; f < 28; ++f) g[f] = bld4(rs, (unsigned)(OFF_GX + (((size_t)s * 16 + h) * 28 + f) * FR + lane * 16), 0);
#pragma unroll
    for (int j = 0; j < 4; ++j) { const int f = w + 8 * j; if (f < 28) *(u32x4*)(lds + f * 1024 + lane * 16) = xs[j]; }
    __syncthreads();
    f32x4 xx[3] = {{0,0,0,0},{0,0,0,0},{0,0,0,0}};
#pragma unroll
    for (int t = 0; t < 14; ++t) {
        const u32x4 x0 = *(const u32x4*)(lds + t * 1024 + lane * 16), x1 = *(const u32x4*)(lds + (14 + t) * 1024 + lane * 16);
        xx[0] = mfma32(g[t], x0, xx[0]);
        xx[1] = mfma32(g[t], x1, xx[1]);
        xx[2] = mfma32(g[14 + t], x1, xx[2]);
    }
    wait_prog(p, s, dead);
    f32x4 sm = {0, 0, 0, 0};
    {
        u32x4 xm[7];
#pragma unroll
        for (int c = 0; c < 7; ++c) xm[c] = bld4(rs, (unsigned)(OFF_XM + (((size_t)s * 16 + h) * 7 + c) * 1024 + lane * 16), 0);
#pragma unroll
        for (int c = 0; c < 7; ++c) sm += __builtin_bit_cast(f32x4, xm[c]);
    }
    f32x4 sm1;
#pragma unroll
    for (int rr = 0; rr < 4; ++rr) { float lo, hi; swap32(sm[rr], lo, hi); sm[rr] = lo; sm1[rr] = hi; }
    x_finalize(rs, sm, xx[0], xx[0], s, h, 0, lane);
    x_finalize(rs, sm1, xx[1], xx[2], s, h, 1, lane);
    WAITVM0();
    __syncthreads();
    if (tid == 0) bst1(rs, 1u, (unsigned)(OFF_FLAG + (FLAG_P + s * 32 + half) * 4), 0);
}

DEVI void out_unit(const Params& p, int s, int bg, char* lds, bool& dead) {
    const int tid = threadIdx.x, lane = tid & 63, w = __builtin_amdgcn_readfirstlane(tid >> 6);
    const int col = lane & 15, q4 = lane >> 4;
    const rsrc_t rs = mkrsrc(p.ws, 0x10000000u);
    const int beta = 8 * bg + w;
    constexpr int O_PX = 0, O_PM = 32768, O_W = 40960;
    waitflag(p, FLAG_U + s * 16 + beta / 14, 8, dead);
    u32x4 uf[8];
#pragma unroll
    for (int hp = 0; hp < 8; ++hp) uf[hp] = bld4(rs, (unsigned)(OFF_UF + (((size_t)s * 56 + beta) * 8 + hp) * FR + lane * 16), 0);
    wait_prog(p, s, dead);
    u32x4 wl[2];
#pragma unroll
    for (int j = 0; j < 2; ++j) wl[j] = bld4(rs, (unsigned)(OFF_WC + ((size_t)s * 56 + 8 * bg) * 2048 + (2 * w + j) * 1024 + lane * 16), 0);
    waitall(p, FLAG_P + s * 32, 2, dead);
    u32x4 pl[4], pml;
#pragma unroll
    for (int j = 0; j < 4; ++j) pl[j] = bld4(rs, (unsigned)(OFF_PX + ((size_t)s * 32 + 4 * w + j) * FR + lane * 16), 0);
    pml = bld4(rs, (unsigned)(OFF_PM + (size_t)s * 8192 + w * 1024 + lane * 16), 0);
#pragma unroll
    for (int j = 0; j < 2; ++j) *(u32x4*)(lds + O_W + (2 * w + j) * 1024 + lane * 16) = wl[j];
#pragma unroll
    for (int j = 0; j < 4; ++j) *(u32x4*)(lds + O_PX + (4 * w + j) * 1024 + lane * 16) = pl[j];
    *(u32x4*)(lds + O_PM + w * 1024 + lane * 16) = pml;
    __syncthreads();
    const bool diag = (col >> 3) == (q4 >> 1);
    u32x2 wv[8];
#pragma unroll
    for (int hp = 0; hp < 8; ++hp) { const u32x2 t = *(const u32x2*)(lds + O_W + (w * 8 + hp) * 256 + col * 16 + (q4 & 1) * 8); wv[hp] = diag ? t : (u32x2){0u, 0u}; }
#pragma unroll
    for (int it = 0; it < 2; ++it) {
        f32x4 aa = {0,0,0,0}, ab = {0,0,0,0}, am = {0,0,0,0};
#pragma unroll
        for (int hp = 0; hp < 8; ++hp) {
            const u32x4 pa = *(const u32x4*)(lds + O_PX + ((2 * hp) * 2 + it) * 1024 + lane * 16), pb = *(const u32x4*)(lds + O_PX + ((2 * hp + 1) * 2 + it) * 1024 + lane * 16);
            const u32x2 pm = *(const u32x2*)(lds + O_PM + (hp * 2 + it) * 512 + lane * 8);
            aa = mfma32(uf[hp], pa, aa);
            ab = mfma32(uf[hp], pb, ab);
            am = mfma16(wv[hp], pm, am);
        }
        float v[4];
#pragma unroll
        for (int rr = 0; rr < 4; ++rr) { const float x = ((q4 >> 1) == 0 ? aa[rr] : ab[rr]) + am[rr]; float a, b2; swap32(x, a, b2); v[rr] = a + b2; }
        if (lane < 32) { const u32x2 o = {pk2(v[0], v[1]), pk2(v[2], v[3])};
            *(u32x2*)(p.out + (size_t)(32 * s + 16 * it + col) * OUTW + 8 * beta + 4 * q4) = o; }
    }
}

DEVI float phi(float t) { return (t > 0.f ? t + 1.f : __builtin_amdgcn_exp2f(t * 1.4426950408889634f)) + 1e-6f; }
DEVI float rbf(float x) { return bf_lo(pk2(x, 0.f)); }

DEVI void b1_unit(const Params& p, int rb, int hp, char* lds, bool& dead) {
    waitflag(p, FLAG_BT, 128, dead);
    const int tid = threadIdx.x, lane = tid & 63, w = __builtin_amdgcn_readfirstlane(tid >> 6);
    const int col = lane & 15, q4 = lane >> 4;
    const rsrc_t rs = mkrsrc(p.ws, 0x10000000u);
    const int h = 2 * hp + (w & 1), rt0 = rb * 8 + (w >> 1) * 2;
    u32x4 xr[2][14], stg[11];
    auto wload = [&](int c) {
#pragma unroll
        for (int j = 0; j < 11; ++j) {
            const int i = (w + 8 * j < 84) ? w + 8 * j : w, tt = i % 7, g = i / 7, tile = g & 1, mat = (g >> 1) % 3, hh = g / 6;
            stg[j] = bld4(rs, (unsigned)(OFF_BW + ((size_t)((mat * 16 + 2 * hp + hh) * 2 + tile) * 14 + 7 * c + tt) * FR + lane * 16), 0);
        }
    };
    auto wput = [&]() {
#pragma unroll
        for (int j = 0; j < 11; ++j) if (w + 8 * j < 84) *(u32x4*)(lds + (w + 8 * j) * 1024 + lane * 16) = stg[j];
    };
    wload(0);
#pragma unroll
    for (int a = 0; a < 2; ++a)
#pragma unroll
        for (int t = 0; t < 14; ++t) xr[a][t] = bld4(rs, (unsigned)(OFF_BX + ((size_t)(rt0 + a) * 14 + t) * FR + lane * 16), 0);
    f32x4 aq[2][2], ak[2][2], av[2][2];
#pragma unroll
    for (int a = 0; a < 2; ++a)
#pragma unroll
        for (int c = 0; c < 2; ++c) { aq[a][c] = (f32x4){0,0,0,0}; ak[a][c] = (f32x4){0,0,0,0}; av[a][c] = (f32x4){0,0,0,0}; }
    __syncthreads();
    wput();
    wload(1);
    __syncthreads();
#pragma unroll
    for (int c2 = 0; c2 < 2; ++c2) {
#pragma unroll
        for (int tt = 0; tt < 7; ++tt) {
            const char* wb = lds + ((w & 1) * 42 + tt) * 1024 + lane * 16;
            u32x4 wq[2], wk[2], wv[2];
#pragma unroll
            for (int c = 0; c < 2; ++c) { wq[c] = *(const u32x4*)(wb + (0 + c) * 7168); wk[c] = *(const u32x4*)(wb + (2 + c) * 7168); wv[c] = *(const u32x4*)(wb + (4 + c) * 7168); }
#pragma unroll
            for (int a = 0; a < 2; ++a)
#pragma unroll
                for (int c = 0; c < 2; ++c) {
                    aq[a][c] = mfma32(wq[c], xr[a][7 * c2 + tt], aq[a][c]);
                    ak[a][c] = mfma32(xr[a][7 * c2 + tt], wk[c], ak[a][c]);
                    av[a][c] = mfma32(xr[a][7 * c2 + tt], wv[c], av[a][c]);
                }
        }
        if (c2 == 0) { __syncthreads(); wput(); __syncthreads(); }
    }
    __syncthreads();
#pragma unroll
    for (int a = 0; a < 2; ++a) {
        f32x4 p0, p1;
#pragma unroll
        for (int rr = 0; rr < 4; ++rr) {
            p0[rr] = phi(rbf(aq[a][0][rr]));
            p1[rr] = (8 * q4 + 4 + rr < 28) ? phi(rbf(aq[a][1][rr])) : 0.f;
        }
        bst4(rs, pack8(p0, p1), (unsigned)(OFF_BQ + ((size_t)(rt0 + a) * 16 + h) * FR + lane * 16), 0);
    }
    u32x4 pk[2], vv[2];
#pragma unroll
    for (int c = 0; c < 2; ++c) {
        f32x4 k0, k1, v0, v1;
        const int d = 16 * c + col;
#pragma unroll
        for (int rr = 0; rr < 4; ++rr) {
            k0[rr] = (d < 28) ? phi(rbf(ak[0][c][rr])) : 0.f; k1[rr] = (d < 28) ? phi(rbf(ak[1][c][rr])) : 0.f;
            v0[rr] = (d == 28) ? 1.f : av[0][c][rr]; v1[rr] = (d == 28) ? 1.f : av[1][c][rr];
        }
        pk[c] = pack8(k0, k1); vv[c] = pack8(v0, v1);
    }
    float* red = (float*)lds;
#pragma unroll
    for (int dc = 0; dc < 2; ++dc)
#pragma unroll
        for (int ec = 0; ec < 2; ++ec) {
            const f32x4 d = mfma32(pk[dc], vv[ec], (f32x4){0,0,0,0});
            *(f32x4*)(red + ((w * 4 + dc * 2 + ec) * 64 + lane) * 4) = d;
        }
    bar();
    {
        const int hh = w & 1, tile = w >> 1;
        f32x4 t4 = {0,0,0,0};
#pragma unroll
        for (int g = 0; g < 4; ++g) t4 += *(const f32x4*)(red + (((g * 2 + hh) * 4 + tile) * 64 + lane) * 4);
        bst4(rs, __builtin_bit_cast(u32x4, t4), (unsigned)(OFF_BKV + (((size_t)rb * 16 + 2 * hp + hh) * 4 + tile) * 1024 + lane * 16), 0);
    }
    WAITVM0();
    bar();
    if (tid == 0) signal(p, FLAG_B1);
}

DEVI void b2_unit(const Params& p, int h, char* lds, bool& dead) {
    const int tid = threadIdx.x, lane = tid & 63, w = __builtin_amdgcn_readfirstlane(tid >> 6);
    const int col = lane & 15, q4 = lane >> 4;
    const rsrc_t rs = mkrsrc(p.ws, 0x10000000u);
    float* kvs = (float*)lds;
    waitflag(p, FLAG_B1, 128, dead);
    if (w < 4) {
        f32x4 t4 = {0,0,0,0};
#pragma unroll
        for (int rb = 0; rb < 16; ++rb) t4 += __builtin_bit_cast(f32x4, bld4(rs, (unsigned)(OFF_BKV + (((size_t)rb * 16 + h) * 4 + w) * 1024 + lane * 16), 0));
        const int dc = w >> 1, ec = w & 1;
#pragma unroll
        for (int rr = 0; rr < 4; ++rr) kvs[(16 * dc + 4 * q4 + rr) * 33 + 16 * ec + col] = t4[rr];
    }
    bar();
    if (tid < 32) bst1(rs, __float_as_uint(kvs[tid * 33 + 28]), (unsigned)(OFF_BZ + (h * 32 + tid) * 4), 0);
    u32x4 ka[2];
#pragma unroll
    for (int bb = 0; bb < 2; ++bb) {
        const int d = 8 * (col >> 2) + 4 * bb + (col & 3);
        float e[8];
#pragma unroll
        for (int j = 0; j < 8; ++j) e[j] = (8 * q4 + j < 28) ? kvs[d * 33 + 8 * q4 + j] : 0.f;
        ka[bb] = (u32x4){pk2(e[0], e[1]), pk2(e[2], e[3]), pk2(e[4], e[5]), pk2(e[6], e[7])};
    }
    for (int ct = w; ct < 28; ct += 8) {
        u16 e[8];
#pragma unroll
        for (int j = 0; j < 8; ++j) e[j] = (8 * q4 + j < 28) ? p.bwo[(size_t)(28 * h + 8 * q4 + j) * DM + 32 * (ct >> 1) + 8 * (col >> 2) + 4 * (ct & 1) + (col & 3)] : (u16)0;
        const u32x4 wf = {e[0] | ((unsigned)e[1] << 16), e[2] | ((unsigned)e[3] << 16), e[4] | ((unsigned)e[5] << 16), e[6] | ((unsigned)e[7] << 16)};
        const f32x4 d0 = mfma32(ka[0], wf, (f32x4){0,0,0,0}), d1 = mfma32(ka[1], wf, (f32x4){0,0,0,0});
        bst4(rs, pack8(d0, d1), (unsigned)(OFF_BM + ((size_t)h * 28 + ct) * FR + lane * 16), 0);
    }
    WAITVM0();
    bar();
    if (tid == 0) signal(p, FLAG_B2);
}

DEVI void b3_unit(const Params& p, int u, char* lds, bool& dead) {
    const int tid = threadIdx.x, lane = tid & 63, w = __builtin_amdgcn_readfirstlane(tid >> 6);
    const int col = lane & 15, q4 = lane >> 4;
    const rsrc_t rs = mkrsrc(p.ws, 0x10000000u);
    waitflag(p, FLAG_B2, 16, dead);
    __syncthreads();
#pragma unroll
    for (int k = 0; k < 4; ++k) {
        const int h = 2 * w + (k >> 1), rt = 2 * u + (k & 1);
        const u32x4 pq = bld4(rs, (unsigned)(OFF_BQ + ((size_t)rt * 16 + h) * FR + lane * 16), 0);
        const f32x4 z0 = __builtin_bit_cast(f32x4, bld4(rs, (unsigned)(OFF_BZ + (h * 32 + 8 * q4) * 4), 0)), z1 = __builtin_bit_cast(f32x4, bld4(rs, (unsigned)(OFF_BZ + (h * 32 + 8 * q4 + 4) * 4), 0));
        float f[8] = {bf_lo(pq.x), bf_hi(pq.x), bf_lo(pq.y), bf_hi(pq.y), bf_lo(pq.z), bf_hi(pq.z), bf_lo(pq.w), bf_hi(pq.w)};
        float den = f[0] * z0.x + f[1] * z0.y + f[2] * z0.z + f[3] * z0.w + f[4] * z1.x + f[5] * z1.y + f[6] * z1.z + f[7] * z1.w;
        { float a, b2; swap16(den, a, b2); den = a + b2; swap32(den, a, b2); den = a + b2; }
        const float inv = 1.0f / (den + 1e-6f);
        const u32x4 pn = {pk2(f[0] * inv, f[1] * inv), pk2(f[2] * inv, f[3] * inv), pk2(f[4] * inv, f[5] * inv), pk2(f[6] * inv, f[7] * inv)};
        *(u32x4*)(lds + ((k & 1) * 16 + h) * 1024 + lane * 16) = pn;
    }
    u32x4 bm[2][16];
    auto bload = [&](int pr) {
#pragma unroll
        for (int a = 0; a < 2; ++a)
#pragma unroll
            for (int h = 0; h < 16; ++h) bm[a][h] = bld4(rs, (unsigned)(OFF_BM + ((size_t)h * 28 + 2 * pr + a) * FR + lane * 16), 0);
    };
    if (w < 7) bload(2 * w);
    __syncthreads();
    if (w < 7) {
#pragma unroll
        for (int jp = 0; jp < 2; ++jp) {
            const int pr = 2 * w + jp;
            f32x4 acc[2][2] = {{{0,0,0,0},{0,0,0,0}},{{0,0,0,0},{0,0,0,0}}};
#pragma unroll
            for (int h = 0; h < 16; ++h) {
                const u32x4 p0 = *(const u32x4*)(lds + h * 1024 + lane * 16), p1 = *(const u32x4*)(lds + (16 + h) * 1024 + lane * 16);
#pragma unroll
                for (int a = 0; a < 2; ++a) { acc[a][0] = mfma32(bm[a][h], p0, acc[a][0]); acc[a][1] = mfma32(bm[a][h], p1, acc[a][1]); }
            }
            if (jp == 0) bload(2 * w + 1);
#pragma unroll
            for (int rt = 0; rt < 2; ++rt)
                *(u32x4*)(p.out + (size_t)(32 * u + 16 * rt + col) * OUTW + 448 + 32 * pr + 8 * q4) = pack8(acc[0][rt], acc[1][rt]);
        }
    }
}

DEVI void worker(const Params& p, int u, char* lds) {
    bool dead = false;
    if (p.mode != 2) gemm_worker(p, u, lds);
    if (p.mode == 1) return;
    if (p.mode == 2 && (p.mode2 & 1)) return;
    b1_unit(p, u >> 3, u & 7, lds, dead);
    if (u < 16) b2_unit(p, u, lds, dead);
    if (u >= 64) b3_unit(p, u - 64, lds, dead);
    _Pragma("nounroll") for (int j = u; j < 9 * NSEG; j += 128) {
        const int s = j / 9, k = j % 9;
        if (PACEA > 0) {
            const unsigned t0 = __builtin_amdgcn_readfirstlane(ldflag(p, FLAG_T0));
            if (t0) {
                const unsigned target = t0 + (unsigned)(((PACE0 * 1024 + j * PACEA) >> 10) * (O1T + C2H)) + (unsigned)((((j * PACEA) & 1023) * (O1T + C2H)) >> 10);
                int g = 0;
                _Pragma("nounroll") while ((int)(rt32() - target) < 0 && ++g < 20000) __builtin_amdgcn_s_sleep(16);
            }
        }
        if (k < 2) fin_unit(p, s, k, lds, dead); else out_unit(p, s, k - 2, lds, dead);
    }
}

__global__ void __launch_bounds__(512, 2) k_main(Params p) {
    extern __shared__ __attribute__((aligned(16))) char lds[];
    const int b = blockIdx.x;
    const int idx = (b >> 3) * 4 + (b & 3);
    if ((b & 7) < 4) {
        if (p.mode == 1) return;
        const int r = (b >> 4) * 8 + (b & 3) * 2 + ((b >> 3) & 1);
        if (r < 112) chain_g(p, r, lds); else chain_x(p, r - 112, lds);
    } else {
        worker(p, idx, lds);
    }
}

extern "C" void kernel_launch(void* const* d_in, const int* in_sizes, int n_in, void* d_out, int out_size, void* d_ws, size_t ws_size, hipStream_t stream) {
    Params p;
    p.x = (const u16*)d_in[0]; p.mem0 = (const u16*)d_in[1]; p.Wq = (const u16*)d_in[2]; p.Wk = (const u16*)d_in[3];
    p.Wv = (const u16*)d_in[4]; p.Wo = (const u16*)d_in[5]; p.bx = (const u16*)d_in[7]; p.wqkv = (const u16*)d_in[8]; p.bwo = (const u16*)d_in[9];
    p.out = (u16*)d_out; p.ws = (char*)d_ws;
    const uintptr_t base = (uintptr_t)d_ws;
    const uintptr_t al = (base + OFF_COMM_RAW + ((2u << 20) - 1)) & ~(uintptr_t)((2u << 20) - 1);
    p.comm_off = (unsigned long long)(al - base);
    (void)hipFuncSetAttribute((const void*)k0_prep, hipFuncAttributeMaxDynamicSharedMemorySize, 136000);
    (void)hipFuncSetAttribute((const void*)k_main, hipFuncAttributeMaxDynamicSharedMemorySize, 160000);
    k0_prep<<<256, 512, 136000, stream>>>(p);
    p.mode = 0; p.mode2 = 0;
    k_main<<<256, 512, 160000, stream>>>(p);
}
```

```cpp
#include <hip/hip_runtime.h>
#include <hip/hip_bf16.h>
#include <stdint.h>

typedef __attribute__((ext_vector_type(8))) __bf16 bf16x8;
typedef __attribute__((ext_vector_type(4))) short s16x4;
typedef __attribute__((ext_vector_type(2))) __bf16 bf16x2;
typedef __attribute__((ext_vector_type(2))) float f32x2;
typedef __attribute__((ext_vector_type(4))) float f32x4;
typedef __attribute__((ext_vector_type(4))) unsigned u32x4;
typedef __attribute__((ext_vector_type(2))) unsigned u32x2;
typedef unsigned short u16;
#define LAS __attribute__((address_space(3)))
#define DEVI __device__ __forceinline__

constexpr int DM = 448, NH = 16, NSEG = 64, SEGL = 32, OUTW = 896;
constexpr size_t FR = 1024;
constexpr size_t OFF_FLAG = 0;
constexpr size_t SZ_FLAG = 69632;
constexpr size_t OFF_AF = OFF_FLAG + SZ_FLAG;
constexpr size_t SZ_AF = (size_t)16 * 28 * 14 * FR;
constexpr size_t OFF_BF = OFF_AF + SZ_AF;
constexpr size_t OFF_XF = OFF_BF + SZ_AF;
constexpr size_t SZ_XF = (size_t)64 * 28 * FR;
constexpr size_t OFF_GX = OFF_XF + SZ_XF;
constexpr size_t SZ_GX = (size_t)64 * 16 * 28 * FR;
constexpr size_t OFF_UF = OFF_GX + SZ_GX;
constexpr size_t SZ_UF = (size_t)64 * 56 * 8 * FR;
constexpr size_t OFF_XM = OFF_UF + SZ_UF;
constexpr size_t SZ_XM = (size_t)64 * 16 * 7 * 1024;
constexpr size_t OFF_WC = OFF_XM + SZ_XM;
constexpr size_t SZ_WC = (size_t)64 * 56 * 8 * 256;
constexpr size_t OFF_PX = OFF_WC + SZ_WC;
constexpr size_t SZ_PX = (size_t)64 * 16 * 2 * FR;
constexpr size_t OFF_PM = OFF_PX + SZ_PX;
constexpr size_t SZ_PM = (size_t)64 * 8 * 2 * 512;
constexpr size_t OFF_BQ = OFF_PM + SZ_PM;
constexpr size_t SZ_BQ = (size_t)128 * 16 * FR;
constexpr size_t OFF_BW = OFF_BQ + SZ_BQ;
constexpr size_t SZ_BW = (size_t)3 * 16 * 2 * 14 * FR;
constexpr size_t OFF_BX = OFF_BW + SZ_BW;
constexpr size_t SZ_BX = (size_t)128 * 14 * FR;
constexpr size_t OFF_BKV = OFF_BX + SZ_BX;
constexpr size_t SZ_BKV = (size_t)16 * 16 * 4 * 1024;
constexpr size_t OFF_BM = OFF_BKV + SZ_BKV;
constexpr size_t SZ_BM = (size_t)16 * 28 * FR;
constexpr size_t OFF_BZ = OFF_BM + SZ_BM;
constexpr size_t SZ_BZ = 16 * 32 * 4;
constexpr size_t OFF_COMM_RAW = OFF_BZ + SZ_BZ;
constexpr size_t COMM_BYTES = (size_t)8 << 20;
constexpr unsigned POISON = 0xFFFFFFFFu;
constexpr int FLAG_CFG = 0;
constexpr int FLAG_B1 = 32;
constexpr int FLAG_B2 = 64;
constexpr int FLAG_BT = 128;
constexpr int FLAG_PROG = 8192;
constexpr int NPZ = 4;
constexpr int FLAG_T0 = 384;
constexpr int FLAG_GX = 2048;
constexpr int FLAG_U = 3072;
constexpr int FLAG_P = 4096;
constexpr unsigned STEP_BYTES = 131072u, CLS = 16384u, PA_OFF = 32768u, EX_OFF = 65536u, PB_OFF = 98304u;
constexpr unsigned STATS_OFF = 7168u;
constexpr unsigned CLK_OFF = 7680u;
constexpr unsigned PROBE_OFF = PA_OFF + 7168u;
constexpr unsigned LAT_OFF = 8u << 20;
constexpr unsigned GEN_OFF = LAT_OFF + 1024u;
constexpr unsigned PX_OFF = LAT_OFF + 8192u;
constexpr unsigned P0_OFF = LAT_OFF + 32768u;
#define P0S 128
struct Params {
    const u16 *x, *mem0, *Wq, *Wk, *Wv, *Wo, *bx, *wqkv, *bwo;
    u16* out;
    char* ws;
    unsigned long long comm_off;
};

DEVI int xcd_of(int r) { return (r >> 1) & 3; }
DEVI int part_slot(int r) { return xcd_of(r) * 28 + (r >> 3) * 2 + (r & 1); }
DEVI int g_head(int r) { return 4 * ((part_slot(r) % 28) / 7) + xcd_of(r); }
DEVI int g_chunk(int r) { return (part_slot(r) % 28) % 7; }
DEVI int slot_of(int h, int c7) { return (h & 3) * 28 + (h >> 2) * 7 + c7; }
DEVI int prep_blk(int mat, int h, int th, int cg) { return mat == 0 ? (h & 3) + 8 * ((h >> 2) + 4 * (th + 2 * cg)) : 4 + ((h >> 1) & 3) + 8 * ((h & 1) + 2 * (h >> 3) + 4 * (th + 2 * cg)); }
DEVI int gemm_v(int u) {
    const int x = u & 3, k = (u & 63) >> 3, par = (u >> 2) & 1;
    return (u >> 6) ? ((x + 4 * (k & 1)) << 3) | ((k >> 1) << 1) | par : ((x | (k << 2)) << 1) | par;
}
DEVI int stat_off(int h) { return (h & 3) * 128 + (h >> 2) * 32; }
DEVI int m_lane_off(int lane) { const int col = lane & 15; return (2 * (lane >> 4) + (col >> 3)) * 128 + (col & 7) * 16; }
DEVI unsigned pk2(float a, float b) { f32x2 v = {a, b}; bf16x2 r = __builtin_convertvector(v, bf16x2); return __builtin_bit_cast(unsigned, r); }
DEVI f32x4 mfma32(u32x4 a, u32x4 b, f32x4 c) { return __builtin_amdgcn_mfma_f32_16x16x32_bf16(__builtin_bit_cast(bf16x8, a), __builtin_bit_cast(bf16x8, b), c, 0, 0, 0); }
DEVI f32x4 mfma16(u32x2 a, u32x2 b, f32x4 c) { return __builtin_amdgcn_mfma_f32_16x16x16bf16_1k(__builtin_bit_cast(s16x4, a), __builtin_bit_cast(s16x4, b), c, 0, 0, 0); }
DEVI u32x4 pack8(f32x4 a, f32x4 b) { u32x4 r; r.x = pk2(a.x, a.y); r.y = pk2(a.z, a.w); r.z = pk2(b.x, b.y); r.w = pk2(b.z, b.w); return r; }
DEVI float fmx(float a, float b) { return __builtin_amdgcn_fmed3f(a, b, 3.4028235e38f); }
template <int CTRL> DEVI float dppf(float x) { return __uint_as_float((unsigned)__builtin_amdgcn_update_dpp(0, (int)__float_as_uint(x), CTRL, 0xf, 0xf, true)); }
constexpr int DPP_ROR8 = 0x128, DPP_ROR4 = 0x124, DPP_QP_1032 = 0xB1, DPP_QP_2301 = 0x4E;
DEVI void swap16(float x, float& a, float& b) { auto s = __builtin_amdgcn_permlane16_swap(__float_as_uint(x), __float_as_uint(x), false, false); a = __uint_as_float(s[0]); b = __uint_as_float(s[1]); }
DEVI void swap32(float x, float& a, float& b) { auto s = __builtin_amdgcn_permlane32_swap(__float_as_uint(x), __float_as_uint(x), false, false); a = __uint_as_float(s[0]); b = __uint_as_float(s[1]); }
DEVI void uswap16(unsigned x, unsigned& a, unsigned& b) { auto s = __builtin_amdgcn_permlane16_swap(x, x, false, false); a = s[0]; b = s[1]; }
DEVI void uswap32(unsigned x, unsigned& a, unsigned& b) { auto s = __builtin_amdgcn_permlane32_swap(x, x, false, false); a = s[0]; b = s[1]; }
template <int CTRL> DEVI unsigned dppu(unsigned x) { return (unsigned)__builtin_amdgcn_update_dpp(0, (int)x, CTRL, 0xf, 0xf, true); }
constexpr int DPP_SHL4 = 0x104, DPP_SHL8 = 0x108;
DEVI float bf_lo(unsigned v) { return __uint_as_float(v << 16); }
DEVI float bf_hi(unsigned v) { return __uint_as_float(v & 0xFFFF0000u); }
DEVI int kcol(int t, int q4) { return 64 * (t >> 1) + 16 * q4 + 8 * (t & 1); }
DEVI unsigned nopoison(unsigned v) { return v < 0xFFFFFFFEu ? v : 0xFFFFFFFEu; }
DEVI unsigned max4(u32x4 v) { unsigned a = v.x > v.y ? v.x : v.y, b = v.z > v.w ? v.z : v.w; return a > b ? a : b; }

typedef __amdgpu_buffer_rsrc_t rsrc_t;
DEVI rsrc_t mkrsrc(const void* p, unsigned bytes) { return __builtin_amdgcn_make_buffer_rsrc((void*)p, 0, bytes, 0x00020000); }
DEVI u32x4 bld4(rsrc_t r, unsigned v, unsigned s) { return __builtin_amdgcn_raw_buffer_load_b128(r, v, s, 16); }
DEVI u32x2 bld2(rsrc_t r, unsigned v, unsigned s) { return __builtin_amdgcn_raw_buffer_load_b64(r, v, s, 16); }
DEVI unsigned bld1(rsrc_t r, unsigned v, unsigned s) { return __builtin_amdgcn_raw_buffer_load_b32(r, v, s, 16); }
DEVI u32x4 bld4p(rsrc_t r, unsigned v, unsigned s) { return __builtin_amdgcn_raw_buffer_load_b128(r, v, s, 0); }
DEVI void bst4(rsrc_t r, u32x4 d, unsigned v, unsigned s) { __builtin_amdgcn_raw_buffer_store_b128(d, r, v, s, 16); }
DEVI void bst2(rsrc_t r, u32x2 d, unsigned v, unsigned s) { __builtin_amdgcn_raw_buffer_store_b64(d, r, v, s, 16); }
DEVI void bst1(rsrc_t r, unsigned d, unsigned v, unsigned s) { __builtin_amdgcn_raw_buffer_store_b32(d, r, v, s, 16); }
#define MEMFENCE() asm volatile("" ::: "memory")
#define WAITVM0() do { asm volatile("s_waitcnt vmcnt(0)" ::: "memory"); __builtin_amdgcn_s_waitcnt(0x0F70); } while (0)
DEVI void bar() { asm volatile("s_waitcnt lgkmcnt(0)\n\ts_barrier" ::: "memory"); }
constexpr unsigned OOB = 0xF0000000u;
DEVI u32x4 mkdesc(const void* p, unsigned bytes) {
    const unsigned long long a = (unsigned long long)p;
    u32x4 d; d.x = __builtin_amdgcn_readfirstlane((unsigned)a); d.y = __builtin_amdgcn_readfirstlane((unsigned)(a >> 32)) & 0xFFFFu; d.z = bytes; d.w = 0x00020000u; return d;
}
#define ALD4(dst, vo, ds, so) asm volatile("buffer_load_dwordx4 %0, %1, %2, %3 offen sc1" : "=v"(dst) : "v"(vo), "s"(ds), "s"(so) : "memory")
#define ALD4R(dst, vo, ds, so) asm volatile("buffer_load_dwordx4 %0, %1, %2, %3 offen sc1" : "+v"(dst) : "v"(vo), "s"(ds), "s"(so) : "memory")
#define ALD4PR(dst, vo, ds, so) asm volatile("buffer_load_dwordx4 %0, %1, %2, %3 offen" : "+v"(dst) : "v"(vo), "s"(ds), "s"(so) : "memory")
#define ALDU16R(dst, vo, ds, so) asm volatile("buffer_load_ushort %0, %1, %2, %3 offen" : "+v"(dst) : "v"(vo), "s"(ds), "s"(so) : "memory")
#define ALD1(dst, vo, ds, so) asm volatile("buffer_load_dword %0, %1, %2, %3 offen sc1" : "=v"(dst) : "v"(vo), "s"(ds), "s"(so) : "memory")
#define ALD1R(dst, vo, ds, so) asm volatile("buffer_load_dword %0, %1, %2, %3 offen sc1" : "+v"(dst) : "v"(vo), "s"(ds), "s"(so) : "memory")
#define AST4(dat, vo, ds, so) asm volatile("buffer_store_dwordx4 %0, %1, %2, %3 offen sc1\n\ts_nop 1" :: "v"(dat), "v"(vo), "s"(ds), "s"(so) : "memory")
#define AST2(dat, vo, ds, so) asm volatile("buffer_store_dwordx2 %0, %1, %2, %3 offen sc1\n\ts_nop 1" :: "v"(dat), "v"(vo), "s"(ds), "s"(so) : "memory")
#define AST1(dat, vo, ds, so) asm volatile("buffer_store_dword %0, %1, %2, %3 offen sc1" :: "v"(dat), "v"(vo), "s"(ds), "s"(so) : "memory")
#define AADD1(vo, ds) asm volatile("buffer_atomic_add %0, %1, %2, 0 offen" :: "v"(1u), "v"(vo), "s"(ds) : "memory")
#define SCHEDB() __builtin_amdgcn_sched_barrier(0)
#define PIN(x) asm volatile("" : "+v"(x))
#define AWAIT(n) asm volatile("s_waitcnt vmcnt(" #n ")" ::: "memory")

#define K0AUX 16
#define CODEPF 64
#define PBSL 2
#define UPOLL 1
#define XASG 2
#define CPOLL 0
#define K0ST(v, off) __builtin_amdgcn_raw_buffer_store_b128(v, rs, off, 0, K0AUX)
template <bool V> struct BoolC { static constexpr bool value = V; };
DEVI void gemm_offsets(int u, int w, int lane, unsigned& base, unsigned& vo0, unsigned& vo1) {
    const int isU = u >> 6, par = (u >> 2) & 1, v = gemm_v(u);
    if (!isU) {
        const int h = v >> 2, tp = 7 * ((v >> 1) & 1) + w; base = (unsigned)(OFF_AF + (size_t)(h * 28 + 2 * tp) * 14 * FR);
        vo0 = (unsigned)(OFF_GX + ((size_t)(h * 2 + 0) * 14 + tp) * FR + lane * 16);
        vo1 = (unsigned)(OFF_GX + ((size_t)(h * 2 + 1) * 14 + tp) * FR + lane * 16);
    } else {
        const int hp = v >> 3, b0 = 14 * ((v >> 1) & 3) + 2 * w; base = (unsigned)(OFF_BF + (size_t)(hp * 56 + b0) * 14 * FR);
        vo0 = (unsigned)(OFF_UF + ((size_t)(b0 + 0) * 8 + hp) * FR + lane * 16);
        vo1 = (unsigned)(OFF_UF + ((size_t)(b0 + 1) * 8 + hp) * FR + lane * 16);
    }
}
struct Tag { unsigned a, b; };
DEVI Tag make_tag(unsigned gen) {
    Tag tg;
    tg.a = (unsigned)__builtin_amdgcn_readfirstlane(gen) * 0x9E3779B1u + 0x7F4A7C15u;
    tg.b = (tg.a ^ 0x5BD1E995u) * 0x85EBCA6Bu;
    return tg;
}
DEVI void prep(const Params& p, char* lds, unsigned gen) {
    const rsrc_t rs = mkrsrc(p.ws, 0x10000000u);
    const int b = blockIdx.x, tid = threadIdx.x, lane = tid & 63, w = __builtin_amdgcn_readfirstlane(tid >> 6);
    const int l15 = lane & 15, q4 = lane >> 4;
    const int mat = (b >> 2) & 1, sub = b >> 5, th = sub & 1, cg = sub >> 1;
    const int h = mat == 0 ? 4 * ((b >> 3) & 3) + (b & 3) : 2 * (4 * ((b >> 4) & 1) + (b & 3)) + ((b >> 3) & 1);
    unsigned cpf;
    if (CODEPF > 0 && w >= 6) {
        const unsigned long long pc = __builtin_amdgcn_s_getpc() & ~127ull;
        const unsigned line = (unsigned)(((b >> 3) * 2 + (w - 6)) * 64 + lane);
        if (line < (unsigned)CODEPF * 8u) asm volatile("global_load_dword %0, %1, off" : "=v"(cpf) : "v"(pc + (unsigned long long)line * 128ull) : "memory");
    }
#define NPROBE 6
#define SWZ1 0
#define RWORD 1
#define FROLL 0
#define PBAR 1
#define NPX 8
#define HPERM 1
    const int hW = HPERM ? 4 * (h & 3) + (h >> 2) : h;
    constexpr int K_SCR = SWZ1 ? 65536 : 61440, K_XT = K_SCR + 7 * 8448, K_CNT = K_XT + 16 * 912;
    const int lr = lane >> 2, lp = lane & 3;
    u32x4 stg[7];
    u32x4 rw[2][8];
    const int t = 7 * th + (w < 7 ? w : 0);
    if (w < 7 || RWORD == 0) {
        const u16* Wr = mat ? p.Wv : p.Wk;
#pragma unroll
        for (int bb = 0; bb < 2; ++bb) {
            const int d = 64 * (t >> 1) + 16 * (lr >> 2) + 8 * (t & 1) + 4 * bb + (lr & 3);
#pragma unroll
            for (int k = 0; k < 8; ++k) rw[bb][k] = *(const u32x4*)(Wr + ((size_t)d * 16 + hW) * 256 + 32 * k + 8 * lp);
        }
    }
    if (mat == 0) {
#pragma unroll
        for (int i = 0; i < 7; ++i) {
            const int ct = 7 * cg + i;
            const int c = 64 * (ct >> 2) + 16 * (lr >> 2) + 4 * (ct & 3) + (lr & 3);
            stg[i] = *(const u32x4*)(p.Wq + ((size_t)c * 16 + hW) * 256 + 32 * w + 8 * lp);
        }
    } else {
#pragma unroll
        for (int i = 0; i < 7; ++i) {
            const int n = tid + 512 * i, e = n / 14, ch = n % 14;
            stg[i] = *(const u32x4*)(p.Wo + ((size_t)hW * 256 + e) * 448 + 112 * cg + 8 * ch);
        }
    }
    const int rt = (XASG == 1 || XASG == 3) ? (b >> 3) * 4 + (b & 3) : (XASG == 0 ? b >> 1 : (b >> 4) * 8 + ((b & 7) ^ 4));
    const bool xon = XASG == 3 ? mat == 1 : XASG == 1 ? mat == 0 : (XASG == 0 ? !(b & 1) : !((b >> 3) & 1));
    const char* xsrc = (const char*)p.x + (size_t)rt * 14336;
    u32x4 xt0, xt1;
    if (xon) { xt0 = *(const u32x4*)(xsrc + tid * 16); xt1 = *(const u32x4*)(xsrc + 8192 + (tid < 384 ? tid : 0) * 16); }
    {
        const u32x4 pz = {POISON, POISON, POISON, POISON};
        if ((b & 7) < 4 && (b >> 3) >= 28 && (b >> 3) < 30) {
            const int j = ((b >> 3) - 28) * 4 + (b & 3);
            const unsigned cb = (unsigned)p.comm_off + (unsigned)(j >> 2) * STEP_BYTES + (unsigned)(j & 3) * 32768u;
            u32x4 m0 = pz;
            if (j == 0 && tid < 448) m0 = *(const u32x4*)(p.mem0 + (tid & 7) * DM + 8 * (tid >> 3));
#pragma unroll
            for (int k = 0; k < 4; ++k) K0ST(m0, cb + (unsigned)k * 8192u + tid * 16);
        }
        if ((b & 7) >= 4 && tid < 56) {
            const int bp = b - 4, side = bp & 1, pair = ((bp >> 1) & 1) + 2 * (bp >> 3), blk = pair >> 2, cand = pair & 3;
            K0ST(pz, (unsigned)p.comm_off + (unsigned)(4 * blk + 1 + 2 * side) * STEP_BYTES + (unsigned)cand * 8192u + PROBE_OFF + (unsigned)tid * 16u);
        }
    }
    if ((b & 7) >= 4 && w < 7) {
        const int u = (b >> 3) * 4 + (b & 3);
        unsigned base, v0, v1;
        gemm_offsets(u, w, lane, base, v0, v1);
        const u32x4 pz = {POISON, POISON, POISON, POISON};
        const unsigned so = (unsigned)((u >> 2) & 1) * (16u * 28u * 1024u);
        K0ST(pz, v0 + so);
        K0ST(pz, v1 + so);
    }
    if (b < 34 && tid < 128) K0ST(((u32x4){0, 0, 0, 0}), (unsigned)(OFF_FLAG + (size_t)b * 2048 + tid * 16));
    if (w < 7) {
        char* scr = lds + K_SCR + w * 8448;
#pragma unroll
        for (int bb = 0; bb < 2; ++bb) {
#pragma unroll
            for (int k = 0; k < 8; ++k) *(u32x4*)(scr + lr * 528 + 64 * k + 16 * lp) = rw[bb][k];
            asm volatile("s_waitcnt lgkmcnt(0)" ::: "memory");
#pragma unroll
            for (int k = 0; k < 8; ++k) rw[bb][k] = *(const u32x4*)(scr + l15 * 528 + 64 * k + 16 * q4);
            asm volatile("s_waitcnt lgkmcnt(0)" ::: "memory");
        }
    }
    if (mat == 0) {
#pragma unroll
        for (int i = 0; i < 7; ++i) *(u32x4*)(lds + (i * 8 + w) * 1024 + (lp * 16 + lr) * 16) = stg[i];
    } else {
#pragma unroll
        for (int i = 0; i < 7; ++i) { const int n = tid + 512 * i, e = n / 14, ch = n % 14;
            *(u32x4*)(lds + (SWZ1 ? e * 256 + (((ch >> 1) + (e & 3) + ((e >> 1) & 4)) & 7) * 32 + (ch & 1) * 16 : e * 240 + ch * 16)) = stg[i]; }
    }
    if (xon) { const int o0 = tid * 16, o1 = 8192 + tid * 16;
      *(u32x4*)(lds + K_XT + (o0 / 896) * 912 + (o0 % 896)) = xt0;
      if (tid < 384) *(u32x4*)(lds + K_XT + (o1 / 896) * 912 + (o1 % 896)) = xt1; }
    if (tid == 0) *(unsigned*)(lds + K_CNT) = 0u;
    __builtin_amdgcn_s_waitcnt(0x0F70);
    asm volatile("" :: "v"(cpf));
    __syncthreads();
    if (xon && rt < NPX) {
        if (w == 7) {
#pragma unroll
            for (int tt = 0; tt < 14; ++tt) {
                const u32x4 v = *(const u32x4*)(lds + K_XT + l15 * 912 + kcol(tt, q4) * 2);
                K0ST(v, (unsigned)(OFF_XF + ((size_t)rt * 14 + tt) * FR + lane * 16));
            }
            const Tag tg = make_tag(gen);
            WAITVM0();
            if (lane == 0) __builtin_amdgcn_raw_buffer_store_b64((u32x2){tg.a, tg.b + 256u + (unsigned)rt}, rs, (unsigned)p.comm_off + PX_OFF + (unsigned)rt * 128u, 0, 16);
        }
    } else {
#pragma unroll
        for (int j = 0; j < 2; ++j) {
            const int tt = w + 7 * j;
            if (w < 7 && xon) {
                const u32x4 v = *(const u32x4*)(lds + K_XT + l15 * 912 + kcol(tt, q4) * 2);
                K0ST(v, (unsigned)(OFF_XF + ((size_t)rt * 14 + tt) * FR + lane * 16));
            }
        }
    }
    if (w == 7 && (b & 7) < 4) {
        const int side = b & 1, pair = ((b >> 1) & 1) + 2 * (b >> 3), blk = pair >> 2, cand = pair & 3;
        unsigned best = 1u << 20;
        for (int k = 0; k < NPROBE; ++k) {
            const unsigned off = (unsigned)p.comm_off + (unsigned)(4 * blk + 1 + 2 * side) * STEP_BYTES + (unsigned)cand * 8192u + PROBE_OFF + 128u * k;
            const unsigned long long t0 = __builtin_amdgcn_s_memtime();
            unsigned vv = __builtin_amdgcn_raw_buffer_load_b32(rs, off, 0, 16);
            asm volatile("s_waitcnt vmcnt(0)" : "+v"(vv) :: "memory");
            const unsigned long long t1 = __builtin_amdgcn_s_memtime();
            if (k > 0) best = min(best, (unsigned)(t1 - t0));
        }
        if (lane == 0) __builtin_amdgcn_raw_buffer_store_b64((u32x2){make_tag(gen).b + 512u + (unsigned)(64 * side + pair), (make_tag(gen).a & 0xFFF00000u) | min(best, 0xFFFFFu)}, rs, (unsigned)p.comm_off + LAT_OFF + (unsigned)(64 * side + pair) * 8u, 0, 16);
    }
    if (w == 7) {
        const unsigned off = (unsigned)lane * (2u << 20);
        if (off < (unsigned)p.comm_off + (10u << 20)) { const unsigned v = *(const volatile unsigned*)(p.ws + off); asm volatile("" :: "v"(v)); }
    }
    if (w < 7) {
      auto fold = [&](auto MATC) {
        constexpr bool MAT1 = decltype(MATC)::value;
        auto ldcf = [&](int i, u32x4 (&cf)[8]) {
#pragma unroll
            for (int k = 0; k < 8; ++k) {
                if (!MAT1) cf[k] = *(const u32x4*)(lds + (i * 8 + k) * 1024 + lane * 16);
                else {
                    const int qq = l15 >> 2, pp = l15 & 3;
                    const int row0 = 32 * k + 8 * q4 + qq;
                    typedef __attribute__((ext_vector_type(4))) short v4s;
                    const int o0 = SWZ1 ? row0 * 256 + ((i + qq + 4 * (q4 & 1)) & 7) * 32 + pp * 8 : row0 * 240 + (16 * i + 4 * pp) * 2;
                    v4s lo = __builtin_amdgcn_ds_read_tr16_b64_v4i16((LAS v4s*)(lds + o0));
                    v4s hi = __builtin_amdgcn_ds_read_tr16_b64_v4i16((LAS v4s*)(lds + o0 + (SWZ1 ? 1024 : 960)));
                    u32x2 l2 = __builtin_bit_cast(u32x2, lo), h2 = __builtin_bit_cast(u32x2, hi);
                    cf[k] = (u32x4){l2.x, l2.y, h2.x, h2.y};
                }
            }
        };
        u32x4 cfa[8], cfb[8];
        const unsigned so = MAT1 ? (unsigned)(OFF_BF + ((size_t)((h >> 1) * 56 + 14 * cg + (l15 >> 3)) * 14 + t) * FR + (16 * q4 + 8 * (h & 1) + (l15 & 7)) * 16)
                                 : (unsigned)(OFF_AF + ((size_t)(h * 28 + 7 * cg) * 14 + t) * FR + lane * 16);
        auto tile = [&](int i, const u32x4 (&cf)[8]) {
            f32x4 a0 = {0, 0, 0, 0}, a1 = {0, 0, 0, 0};
#pragma unroll
            for (int k = 0; k < 8; ++k) { a0 = mfma32(rw[0][k], cf[k], a0);
                a1 = mfma32(rw[1][k], cf[k], a1); }
            const u32x4 o = pack8(a0, a1);
            K0ST(o, so + (unsigned)(i * (MAT1 ? 28 : 14) * FR));
        };
        ldcf(0, cfa);
        if (FROLL) {
            _Pragma("nounroll") for (int i = 0; i < 6; i += 2) {
                ldcf(i + 1, cfb);
                SCHEDB();
                tile(i, cfa);
                SCHEDB();
                ldcf(i + 2, cfa);
                SCHEDB();
                tile(i + 1, cfb);
                SCHEDB();
            }
            tile(6, cfa);
        } else {
#pragma unroll
        for (int i = 0; i < 7; i += 2) {
            if (i + 1 < 7) ldcf(i + 1, cfb);
            tile(i, cfa);
            if (i + 2 < 7) ldcf(i + 2, cfa);
            if (i + 1 < 7) tile(i + 1, cfb);
        }
        }
      };
      if (mat == 0) fold(BoolC<false>{}); else fold(BoolC<true>{});
      WAITVM0();
      if (lane == 0 && __hip_atomic_fetch_add((unsigned*)(lds + K_CNT), 1u, __ATOMIC_RELAXED, __HIP_MEMORY_SCOPE_WORKGROUP) == 6u) {
          const Tag tg = make_tag(gen);
          __builtin_amdgcn_raw_buffer_store_b64((u32x2){tg.a, tg.b + (unsigned)b}, rs, (unsigned)p.comm_off + P0_OFF + (unsigned)b * (unsigned)P0S, 0, 16);
      }
    }
}


#define STAG 0
#define GPACE 0
#define GP0 1
constexpr int SPIN_MAX = 200000;
DEVI unsigned* flagp(const Params& p, int idx) { return (unsigned*)(p.ws + OFF_FLAG) + idx; }
DEVI void signal(const Params& p, int idx) { __hip_atomic_fetch_add(flagp(p, idx), 1u, __ATOMIC_RELAXED, __HIP_MEMORY_SCOPE_AGENT); }
DEVI unsigned ldflag(const Params& p, int idx) { return __hip_atomic_load(flagp(p, idx), __ATOMIC_RELAXED, __HIP_MEMORY_SCOPE_AGENT); }
DEVI void waitflag(const Params& p, int idx, unsigned target, bool& dead, int sleep = 4) {
    int k = dead ? SPIN_MAX : 0;
    _Pragma("nounroll") for (; k < SPIN_MAX; ++k) {
        const unsigned v = __builtin_amdgcn_readfirstlane(ldflag(p, idx));
        if (v >= target) break;
        __builtin_amdgcn_s_sleep(4);
    }
    if (k >= SPIN_MAX) dead = true;
    MEMFENCE();
}

DEVI void waitall(const Params& p, int idx, int n, bool& dead) {
    int k = dead ? SPIN_MAX : 0;
    const int _l = (threadIdx.x & 63) & (n - 1);
    _Pragma("nounroll") for (; k < SPIN_MAX; ++k) {
        const unsigned v = ldflag(p, idx + _l);
        if (!__any(v == 0u)) break;
        __builtin_amdgcn_s_sleep(3);
    }
    if (k >= SPIN_MAX) dead = true;
    MEMFENCE();
}

DEVI void await_prep(const Params& p, u32x4 ds, Tag tg, int blk) {
    unsigned o = blk >= 0 ? (unsigned)p.comm_off + (blk < 256 ? P0_OFF + (unsigned)blk * (unsigned)P0S : PX_OFF + (unsigned)(blk - 256) * 128u) : OOB;
    _Pragma("nounroll") for (int it = 0; it < SPIN_MAX; ++it) {
        u32x2 v;
        asm volatile("buffer_load_dwordx2 %0, %1, %2, 0 offen sc1\n\ts_waitcnt vmcnt(0)" : "=&v"(v) : "v"(o), "s"(ds) : "memory");
        if (v.x == tg.a && v.y == tg.b + (unsigned)blk) o = OOB;
        if (!__any(o != OOB)) break;
        __builtin_amdgcn_s_sleep(PBSL);
    }
}
template <bool LATS_> DEVI void await_prep_all(const Params& p, u32x4 ds, Tag tg, unsigned& l0, unsigned& l1) {
    const int lane = threadIdx.x & 63;
    const unsigned vo = (unsigned)p.comm_off + P0_OFF + (unsigned)lane * (unsigned)P0S, t2 = tg.b + (unsigned)lane;
    unsigned o0 = vo, o1 = vo + 64u * P0S, o2 = vo + 128u * P0S, o3 = vo + 192u * P0S;
    unsigned o4 = LATS_ ? (unsigned)p.comm_off + LAT_OFF + (unsigned)lane * 8u : OOB, o5 = LATS_ ? o4 + 512u : OOB;
    unsigned o6 = (LATS_ && lane < NPX) ? (unsigned)p.comm_off + PX_OFF + (unsigned)lane * 128u : OOB;
    l0 = 0; l1 = 0;
    _Pragma("nounroll") for (int it = 0; it < SPIN_MAX; ++it) {
        u32x2 v0, v1, v2, v3, v4, v5, v6;
        if (LATS_) asm volatile("buffer_load_dwordx2 %0, %3, %6, 0 offen sc1\n\tbuffer_load_dwordx2 %1, %4, %6, 0 offen sc1\n\tbuffer_load_dwordx2 %2, %5, %6, 0 offen sc1"
                                : "=&v"(v4), "=&v"(v5), "=&v"(v6) : "v"(o4), "v"(o5), "v"(o6), "s"(ds) : "memory");
        asm volatile("buffer_load_dwordx2 %0, %4, %8, 0 offen sc1\n\tbuffer_load_dwordx2 %1, %5, %8, 0 offen sc1\n\tbuffer_load_dwordx2 %2, %6, %8, 0 offen sc1\n\tbuffer_load_dwordx2 %3, %7, %8, 0 offen sc1\n\ts_waitcnt vmcnt(0)"
                     : "=&v"(v0), "=&v"(v1), "=&v"(v2), "=&v"(v3) : "v"(o0), "v"(o1), "v"(o2), "v"(o3), "s"(ds) : "memory");
        if (LATS_) {
            asm volatile("" : "+v"(v4), "+v"(v5), "+v"(v6));
            if (v6.x == tg.a && v6.y == t2 + 256u) o6 = OOB;
            if (o4 != OOB && v4.x == t2 + 512u && (v4.y ^ tg.a) <= 0xFFFFFu) { l0 = v4.y & 0xFFFFFu; o4 = OOB; }
            if (o5 != OOB && v5.x == t2 + 576u && (v5.y ^ tg.a) <= 0xFFFFFu) { l1 = v5.y & 0xFFFFFu; o5 = OOB; }
        }
        if (v0.x == tg.a && v0.y == t2) o0 = OOB;
        if (v1.x == tg.a && v1.y == t2 + 64u) o1 = OOB;
        if (v2.x == tg.a && v2.y == t2 + 128u) o2 = OOB;
        if (v3.x == tg.a && v3.y == t2 + 192u) o3 = OOB;
        if (!__any((o0 & o1 & o2 & o3 & o4 & o5 & o6) != OOB)) break;
        __builtin_amdgcn_s_sleep(PBSL);
    }
    if (blockIdx.x == 0 && lane == 0) AST1(tg.a, (unsigned)p.comm_off + GEN_OFF, ds, 0u);
}
DEVI unsigned nopoison4(u32x4& v) { v.x = nopoison(v.x); v.y = nopoison(v.y); v.z = nopoison(v.z); v.w = nopoison(v.w); return 0; }

DEVI void gemm_worker(const Params& p, int u, char* lds, Tag tg) {
    const int tid = threadIdx.x, lane = tid & 63, w = __builtin_amdgcn_readfirstlane(tid >> 6);
    const u32x4 ds = mkdesc(p.ws, 0x10000000u);
    const rsrc_t rs = mkrsrc(p.ws, 0x10000000u);
    const int isU = u >> 6, par = (u >> 2) & 1, v = gemm_v(u);
    constexpr unsigned SEG_STRIDE = 16 * 28 * 1024;
    const int flag_idx = isU ? FLAG_U + ((v >> 1) & 3) : FLAG_GX + (v >> 2);
    if (w == 0 || w == 7) {
        int blk = -1;
        if (w == 7) { if (lane < NPX / 2) blk = 256 + 2 * par + (lane & 1) + 4 * (lane >> 1); }
        else if (lane < 4) blk = isU ? prep_blk(1, 2 * (v >> 3) + (lane >> 1), lane & 1, (v >> 1) & 3) : prep_blk(0, v >> 2, lane & 1, 2 * ((v >> 1) & 1) + (lane >> 1));
        await_prep(p, ds, tg, blk);
    }
    if (w < 7) {
      auto compute = [&](auto ISUC) {
        constexpr bool ISU = decltype(ISUC)::value;
        u32x4 wt[2][14];
        unsigned vo_out[2];
        unsigned base;
        gemm_offsets(u, w, lane, base, vo_out[0], vo_out[1]);
        if (UPOLL) bar();
#pragma unroll
        for (int t = 0; t < 14; ++t)
#pragma unroll
            for (int a = 0; a < 2; ++a) ALD4(wt[a][t], base + (unsigned)(lane * 16), ds, (unsigned)((a * 14 + t) * FR));
        bar();
        int buf = 0;
        auto iter = [&](const int s, auto FIRSTC) {
            constexpr bool FIRST = decltype(FIRSTC)::value;
            f32x4 acc[2][2];
#pragma unroll
            for (int a = 0; a < 2; ++a)
#pragma unroll
                for (int kt = 0; kt < 2; ++kt) acc[a][kt] = (f32x4){0, 0, 0, 0};
            const char* xb = lds + buf * 28672 + lane * 16;
            u32x4 xf[28];
#pragma unroll
            for (int k = 0; k < 14; ++k) xf[k] = *(const u32x4*)(xb + ((k & 1) * 14 + (k >> 1)) * 1024);
            SCHEDB();
#define WWAIT(t, n) if (FIRST && k == 2 * (t)) asm volatile("s_waitcnt vmcnt(" #n ")" : "+v"(wt[0][t]), "+v"(wt[1][t]) :: "memory")
#define WWAITS() WWAIT(0, 26); WWAIT(1, 24); WWAIT(2, 22); WWAIT(3, 20); WWAIT(4, 18); WWAIT(5, 16); WWAIT(6, 14); WWAIT(7, 12); WWAIT(8, 10); WWAIT(9, 8); WWAIT(10, 6); WWAIT(11, 4); WWAIT(12, 2); WWAIT(13, 0)
#pragma unroll
            for (int k = 0; k < 28; ++k) {
                if (k + 14 < 28) xf[k + 14] = *(const u32x4*)(xb + (((k + 14) & 1) * 14 + ((k + 14) >> 1)) * 1024);
                WWAITS();
                if (!ISU) { acc[0][k & 1] = mfma32(wt[0][k >> 1], xf[k], acc[0][k & 1]); acc[1][k & 1] = mfma32(wt[1][k >> 1], xf[k], acc[1][k & 1]); }
                else { acc[0][k & 1] = mfma32(xf[k], wt[0][k >> 1], acc[0][k & 1]); acc[1][k & 1] = mfma32(xf[k], wt[1][k >> 1], acc[1][k & 1]); }
                SCHEDB();
            }
            u32x4 o0, o1;
            if (!ISU) { o0 = pack8(acc[0][0], acc[1][0]); o1 = pack8(acc[0][1], acc[1][1]); }
            else { o0 = pack8(acc[0][0], acc[0][1]); o1 = pack8(acc[1][0], acc[1][1]); }
            if (FIRST) { nopoison4(o0); nopoison4(o1); }
            const unsigned so = (unsigned)s * SEG_STRIDE;
            AST4(o0, vo_out[0], ds, so);
            AST4(o1, vo_out[1], ds, so);
            if (__builtin_expect((s >> 1) == 26 || (s >> 1) == 27, 0)) {
                const int pv = ((s >> 1) - 26) * 896 + u * 7 + w;
                const unsigned psg = (unsigned)(NSEG - NPZ + pv / 264), po = (unsigned)(pv % 264);
                const unsigned poff = po < 112 ? (unsigned)OFF_XM + psg * 114688u + po * 1024u : (po < 224 ? (unsigned)OFF_WC + psg * 114688u + (po - 112) * 1024u
                                    : (po < 256 ? (unsigned)OFF_PX + psg * 32768u + (po - 224) * 1024u : (unsigned)OFF_PM + psg * 8192u + (po - 256) * 1024u));
                const u32x4 pz = {POISON, POISON, POISON, POISON};
                AST4(pz, pv < NPZ * 264 ? poff + (unsigned)lane * 16u : OOB, ds, 0u);
                AWAIT(3);
            } else if (s >> 1 == 1) AWAIT(0); else AWAIT(2);
            bar();
            buf ^= 1;
        };
        iter(par, BoolC<true>{});
        _Pragma("nounroll") for (int s = par + 2; s < NSEG; s += 2) iter(s, BoolC<false>{});
      };
        if (!isU) compute(BoolC<false>{}); else compute(BoolC<true>{});
        AWAIT(0);
        bar();
    } else {
        u32x4 nx[28];
#pragma unroll
        for (int f = 0; f < 28; ++f) nx[f] = (u32x4){0, 0, 0, 0};
        const unsigned vo_x = (unsigned)(OFF_XF + lane * 16);
        auto issue = [&](int sg) {
#pragma unroll
            for (int f = 0; f < 28; ++f) ALD4R(nx[f], vo_x + (unsigned)f * 1024u, ds, (unsigned)sg * 28672u);
        };
        issue(par);
        if (UPOLL) bar();
        AWAIT(0);
#pragma unroll
        for (int f = 0; f < 28; ++f) asm volatile("" : "+v"(nx[f]));
        SCHEDB();
#pragma unroll
        for (int f = 0; f < 28; ++f) *(u32x4*)(lds + f * 1024 + lane * 16) = nx[f];
        SCHEDB();
        if (NPX == 8) issue(par + 2);
        bar();
        { unsigned l0, l1; await_prep_all<false>(p, ds, tg, l0, l1); }
        if (NPX != 8) issue(par + 2);
        int buf = 0;
        const u32x4 dbx = mkdesc(p.bx, 2048u * 448u * 2u), dwq = mkdesc(p.wqkv, 448u * 1344u * 2u);
        const int l15 = lane & 15, q4 = lane >> 4;
        u32x4 tq = {0, 0, 0, 0};
        unsigned tw[8] = {0, 0, 0, 0, 0, 0, 0, 0};
        unsigned tp0 = 0;
        _Pragma("nounroll") for (int s = par; s < NSEG; s += 2) {
            const int it = s >> 1;
            AWAIT(0);
#pragma unroll
            for (int f = 0; f < 28; ++f) asm volatile("" : "+v"(nx[f]));
            asm volatile("" : "+v"(tq), "+v"(tw[0]), "+v"(tw[1]), "+v"(tw[2]), "+v"(tw[3]), "+v"(tw[4]), "+v"(tw[5]), "+v"(tw[6]), "+v"(tw[7]));
            SCHEDB();
#pragma unroll
            for (int f = 0; f < 28; ++f) *(u32x4*)(lds + (buf ^ 1) * 28672 + f * 1024 + lane * 16) = nx[f];
            SCHEDB();
            {
                const unsigned so1 = (it >= 1 && it <= 14) ? (unsigned)(OFF_BX + (size_t)((u * 14 + it - 1) * 1024)) + lane * 16 : OOB;
                const unsigned so2 = (it >= 15 && it <= 25 && u + 128 * (it - 15) < 1344) ? (unsigned)(OFF_BW + (size_t)((u + 128 * (it - 15)) * 1024)) + lane * 16 : OOB;
                const u32x4 o = {tw[0] | (tw[1] << 16), tw[2] | (tw[3] << 16), tw[4] | (tw[5] << 16), tw[6] | (tw[7] << 16)};
                AST4(tq, so1, ds, 0u);
                AST4(o, so2, ds, 0u);
            }
            const int sn = (s + 4 < NSEG) ? s + 4 : s;
            issue(__builtin_amdgcn_readfirstlane(sn));
            {
                const unsigned vo1 = (it < 14) ? (unsigned)(((16 * u + l15) * 448 + kcol(it, q4)) * 2) : OOB;
                const int f = u + 128 * (it - 14), tt = f % 14, tile = (f / 14) & 1, hh = (f / 28) & 15, t3 = f / 448;
                const int col = (t3 == 0) ? (8 * (l15 >> 2) + 4 * tile + (l15 & 3)) : (16 * tile + l15);
                const unsigned vo2 = (it >= 14 && it < 25 && f < 1344 && col < 28) ? (unsigned)((kcol(tt, q4) * 1344 + t3 * 448 + hh * 28 + col) * 2) : OOB;
                ALD4PR(tq, vo1, dbx, 0u);
#pragma unroll
                for (int j = 0; j < 8; ++j) ALDU16R(tw[j], vo2, dwq, (unsigned)j * 2688u);
            }
            if (GPACE > 0) {
                if (it == GP0) tp0 = (unsigned)__builtin_amdgcn_s_memrealtime();
                if (it > GP0) {
                    const unsigned target = tp0 + (unsigned)(it - GP0) * GPACE;
                    int g = 0;
                    _Pragma("nounroll") while ((int)((unsigned)__builtin_amdgcn_s_memrealtime() - target) < 0 && ++g < 4000) __builtin_amdgcn_s_sleep(2);
                }
            }
            bar();
            if (it == 26 && lane == 0) signal(p, FLAG_BT);
            if (lane == 0) {
                if (it == 1) { signal(p, flag_idx + (s - 2) * 16); signal(p, flag_idx + s * 16); }
                else if (it >= 3) signal(p, flag_idx + (s - 2) * 16);
            }
            if (STAG > 0 && s < 2) {
                const int n = ((u * 13) & 31) * STAG;
                _Pragma("nounroll") for (int k = 0; k < n; ++k) __builtin_amdgcn_s_sleep(1);
            }
            buf ^= 1;
        }
        AWAIT(0);
        bar();
        if (lane == 0) signal(p, flag_idx + (NSEG - 2 + par) * 16);
    }
}

constexpr float SC2 = 0.0625f * 1.4426950408889634f;
constexpr int L_R = 0;
constexpr int L_RED1 = 7168;
constexpr int L_RED2 = L_RED1 + 4352;
constexpr int L_REDX = L_RED2 + 2432;
constexpr int L_XX = L_REDX + 8192;
constexpr int L_CFG = L_XX + 73728;

DEVI unsigned step_base(const Params& p, unsigned cfg, int s) { return (unsigned)p.comm_off + (unsigned)s * STEP_BYTES + ((cfg >> (2 * (s >> 2))) & 1u) * 8192u; }
DEVI unsigned step_cls(unsigned cfg, int s) { return ((cfg >> (2 * (s >> 2) + 1)) & 1u) * CLS; }

DEVI unsigned rt32() { return (unsigned)__builtin_amdgcn_s_memrealtime(); }
DEVI void wait_until(unsigned target) {
    const unsigned now = rt32();
    const int d = (int)(target - now);
    if (d <= 0) return;
    if (d > 2000) target = now + 2000u;
    _Pragma("nounroll") while ((int)(rt32() - target) < 0) { }
}
#define NOP4 "s_nop 0\n\ts_nop 0\n\ts_nop 0\n\ts_nop 0\n\t"
#define FINEK 44
DEVI void wait_fine(unsigned target) {
    unsigned now = rt32();
    int d = (int)(target - now);
    if (d > 2000) { target = now + 2000u; d = 2000; }
    _Pragma("nounroll") while (d > 5) d = (int)(target - rt32());
    if (d >= 2) {
        const unsigned off = 12u + 80u - ((unsigned)(d * 23 - FINEK) & 0x7Cu);
        asm volatile("s_getpc_b64 vcc\n\ts_add_u32 vcc_lo, vcc_lo, %0\n\ts_addc_u32 vcc_hi, vcc_hi, 0\n\ts_setpc_b64 vcc\n\t" NOP4 NOP4 NOP4 NOP4 NOP4 :: "s"(off) : "vcc", "scc", "memory");
    }
}
#define O1T 78
#define C2H 80
#define GDELAY 0
#define WPOS 0
#define XHD2 4
#define XHD 0
#define XOFFT 0
#define PACE0 36
#define PACEA 40
#define BDLY 2
#define LATR 60
#define UEARLY 1
#define DZ1 20
#define DZ2 12
#define XB 52
#define HOP1R 50
#define W0OFF 6
#define STARTD 14
#define XNOW 1
#define UDEF 1
DEVI unsigned get_cfg(const Params& p, rsrc_t rs, char* lds, u32x4 ds, Tag tg) {
    const int tid = threadIdx.x, lane = tid & 63, w = __builtin_amdgcn_readfirstlane(tid >> 6);
    if (w == 7) {
        unsigned v, u;
        await_prep_all<true>(p, ds, tg, v, u);
        unsigned l0 = dppu<0x00>(v), l1 = dppu<0x55>(v), l2 = dppu<0xAA>(v), l3 = dppu<0xFF>(v);
        unsigned u0 = dppu<0x00>(u), u1 = dppu<0x55>(u), u2 = dppu<0xAA>(u), u3 = dppu<0xFF>(u);
        asm volatile("" : "+v"(l0), "+v"(l1), "+v"(l2), "+v"(l3), "+v"(u0), "+v"(u1), "+v"(u2), "+v"(u3));
        const int dp = (int)min(min(l0, l2), min(u0, u2)) - (int)min(min(l1, l3), min(u1, u3));
        const int e0 = max(-100, min(100, (int)l0 - (int)l2)) - max(-100, min(100, (int)u0 - (int)u2)), e1 = max(-100, min(100, (int)l1 - (int)l3)) - max(-100, min(100, (int)u1 - (int)u3));
        int dpar[16], dcls[16];
#pragma unroll
        for (int b = 0; b < 16; ++b) dpar[b] = __builtin_amdgcn_readlane(dp, 4 * b);
        unsigned cfg = 0;
#pragma unroll
        for (int pg = 0; pg < 4; ++pg) {
            int sgn = 0;
#pragma unroll
            for (int j = 0; j < 4; ++j) sgn += dpar[4 * pg + j] > 0 ? 1 : -1;
            const bool par = sgn > 0 || (sgn == 0 && dpar[4 * pg] + dpar[4 * pg + 1] + dpar[4 * pg + 2] + dpar[4 * pg + 3] > 0);
#pragma unroll
            for (int j = 0; j < 4; ++j) {
                const int b = 4 * pg + j;
                const int d0 = __builtin_amdgcn_readlane(e0, 4 * b), d1 = __builtin_amdgcn_readlane(e1, 4 * b);
                dcls[b] = par ? d1 : d0;
                cfg |= (par ? 1u : 0u) << (2 * b);
            }
        }
        int bestm = 0, bests = -1;
#pragma unroll
        for (int m = 0; m < 4; ++m) {
            int sc = 0;
#pragma unroll
            for (int pg = 0; pg < 4; ++pg) {
                int t = 0;
#pragma unroll
                for (int j = 0; j < 4; ++j) t += (__builtin_popcount(j & m) & 1) ? -dcls[4 * pg + j] : dcls[4 * pg + j];
                sc += t < 0 ? -t : t;
            }
            if (sc > bests) { bests = sc; bestm = m; }
        }
#pragma unroll
        for (int pg = 0; pg < 4; ++pg) {
            int t = 0;
#pragma unroll
            for (int j = 0; j < 4; ++j) t += (__builtin_popcount(j & bestm) & 1) ? -dcls[4 * pg + j] : dcls[4 * pg + j];
#pragma unroll
            for (int j = 0; j < 4; ++j) cfg |= (unsigned)(((t > 0) ? 1 : 0) ^ (__builtin_popcount(j & bestm) & 1)) << (2 * (4 * pg + j) + 1);
        }
        if (lane == 0) *(unsigned*)(lds + L_CFG) = cfg;
    }
    __syncthreads();
    return __builtin_amdgcn_readfirstlane(*(unsigned*)(lds + L_CFG));
}
DEVI unsigned start_sync(const Params& p, u32x4 ds, int r, char* lds, Tag tg) {
    const int tid = threadIdx.x, lane = tid & 63, w = __builtin_amdgcn_readfirstlane(tid >> 6);
    bar();
    if (w == 3) {
        unsigned t0;
        {
            const unsigned long long t = __builtin_amdgcn_s_memrealtime() + STARTD;
            t0 = (unsigned)t - (unsigned)(t % LATR) + LATR;
        }
        if (r == 0 && lane == 0) AST1(t0 | 1u, (unsigned)(OFF_FLAG + FLAG_T0 * 4), ds, 0u);
        if (lane == 0) *(unsigned*)(lds + L_CFG + 4) = t0;
    }
    bar();
    return __builtin_amdgcn_readfirstlane(*(unsigned*)(lds + L_CFG + 4));
}

template <int DELAY> DEVI void pause() { if (DELAY > 0) __builtin_amdgcn_s_sleep(DELAY); }


constexpr int L_TS = L_CFG + 64;
constexpr int L_RF = L_CFG + 128;
constexpr int L_PU = L_CFG + 256;
constexpr int L_PX = L_PU + 8192;
constexpr int L_GX = L_CFG + 256;
#define WSPLIT 4
#define MINH (O1T - 56)
#define MINM (C2H - 62)
DEVI unsigned tmax(unsigned a, unsigned b) { return ((int)(a - b) > 0) ? a : b; }
DEVI unsigned lat_shift(unsigned base, int late, int dz) { return __builtin_expect(late > dz, 0) ? base + (unsigned)((late - dz + LATR - 1) / LATR) * LATR : base; }
DEVI unsigned lat_next(unsigned base) { const int d = (int)(rt32() + 3u - base); return base + (unsigned)((d < 0 ? 0 : d) / LATR + 1) * LATR; }

DEVI void await_flag(u32x4 ds, unsigned vo, unsigned target, bool& dead) {
    int spins = 0; const int lim = dead ? 1 : SPIN_MAX; bool bad;
    _Pragma("nounroll") do {
        unsigned v; ALD1(v, vo, ds, 0u);
        asm volatile("s_waitcnt vmcnt(0)" : "+v"(v) :: "memory");
        bad = (unsigned)__builtin_amdgcn_readfirstlane(v) < target;
        if (bad) __builtin_amdgcn_s_sleep(2);
    } while (bad && ++spins < lim);
    if (bad) dead = true;
}
DEVI void await_all(u32x4 ds, unsigned vo, bool& dead) {
    int spins = 0; const int lim = dead ? 1 : SPIN_MAX; bool bad;
    _Pragma("nounroll") do {
        unsigned v; ALD1(v, vo, ds, 0u);
        asm volatile("s_waitcnt vmcnt(0)" : "+v"(v) :: "memory");
        bad = __any(v == 0u);
        if (bad) __builtin_amdgcn_s_sleep(2);
    } while (bad && ++spins < lim);
    if (bad) dead = true;
}


#define CLK_F0 2260
#define CLK_K 35
#define CLK_MAX 6
#define CK0 2
#define CK1 9
#define LV0 1
DEVI void poison_rest(const Params& p, int c, rsrc_t rs, unsigned cfg);
DEVI void clk_edge(unsigned& c, unsigned& t) {
    const unsigned t0 = rt32();
    int g = 0;
    _Pragma("nounroll") do { t = rt32(); } while (t == t0 && ++g < 1000);
    c = (unsigned)__builtin_amdgcn_s_memtime();
}
DEVI unsigned clk_margin(u32x4 ds, unsigned off, bool& dead) {
    const int lane = threadIdx.x & 63;
    unsigned v, vo = (unsigned)(lane & 3) * 4u;
    int spins = 0; const int lim = dead ? 1 : SPIN_MAX; bool bad;
    _Pragma("nounroll") do {
        ALD1(v, vo, ds, off);
        asm volatile("s_waitcnt vmcnt(0)" : "+v"(v) :: "memory");
        bad = __any(v == POISON);
    } while (bad && ++spins < lim);
    if (bad) dead = true;
    const int fm = min(min(__builtin_amdgcn_readlane(v, 0), __builtin_amdgcn_readlane(v, 1)), min(__builtin_amdgcn_readlane(v, 2), __builtin_amdgcn_readlane(v, 3)));
    return (unsigned)max(0, min(CLK_MAX, ((CLK_F0 - fm) * CLK_K + 500) / 1000));
}
DEVI void chain_gw(const Params& p, int r, char* lds, u32x4 ds, unsigned t_entry, Tag tg) {
    const rsrc_t rs = mkrsrc(p.ws, 0x10000000u);
    const int tid = threadIdx.x, lane = tid & 63, w = __builtin_amdgcn_readfirstlane(tid >> 6);
    const int col = lane & 15, q4 = lane >> 4;
    const int h = g_head(r), c7 = g_chunk(r), beta = r >> 1, rh = r & 1;
    u32x4 aw[2][2][4];
    const int nq = (w < 3) ? 2 : 1;
    const int qf0 = (c7 + 2 * w) % 7, qf1 = (c7 + 2 * w + nq - 1) % 7;
    if (!CPOLL) await_prep(p, ds, tg, lane < 4 ? prep_blk(0, h, lane & 1, (4 * c7 + 3 * (lane >> 1)) / 7) : -1);
#pragma unroll
    for (int qi = 0; qi < 2; ++qi)
#pragma unroll
        for (int e = 0; e < 2; ++e)
#pragma unroll
            for (int tau = 0; tau < 4; ++tau) {
                const int q = qi ? qf1 : qf0;
                aw[qi][e][tau] = bld4(rs, (unsigned)(OFF_AF + ((size_t)(h * 28 + 4 * c7 + tau) * 14 + 2 * q + e) * FR + lane * 16), 0);
                if (qi == 1 && nq == 1) aw[qi][e][tau] = (u32x4){0, 0, 0, 0};
            }
    unsigned mE = (col < 8) ? 0xFFFFFFFFu : 0u;
    asm volatile("" : "+v"(mE));
    const unsigned cfg = get_cfg(p, rs, lds, ds, tg);
    poison_rest(p, r, rs, cfg);
    WAITVM0();
    bool dead = false;
    const unsigned vo_m0 = (unsigned)(qf0 * 1024 + m_lane_off(lane)), vo_m1 = (nq == 2) ? (unsigned)(qf1 * 1024 + m_lane_off(lane)) : OOB, vo_mc = (unsigned)(c7 * 1024 + m_lane_off(lane));
    const unsigned vo_pp = (unsigned)((((lane >> 4) & 1) ? PB_OFF : PA_OFF) + (lane >> 5) * CLS + part_slot(r) * 64 + (lane & 15) * 4);
    const unsigned vo_mp = (lane >= 32) ? OOB : (unsigned)((lane >> 4) * CLS + beta * 128 + rh * 64 + (lane & 15) * 4);
    const unsigned vo_xm = (unsigned)(OFF_XM + ((size_t)h * 7 + c7) * 1024 + lane * 16);
    unsigned tm = 0, lv = LV0;
    unsigned ck_c = 0, ck_t = 0;
    LAS char* lbase = (LAS char*)lds;
    LAS char* red1 = lbase + L_RED1 + ((col < 8 && q4 < 2) ? w * 256 + (q4 * 8 + col) * 16 : 1024 + w * 768 + (q4 < 2 ? q4 * 8 + (col & 7) : 16 + (q4 - 2) * 16 + col) * 16);
    LAS char* redr = lbase + L_RED1 + ((((lane & 3) >> 1) * 8 + ((lane >> 2) & 7)) * 16 + (lane & 1) * 8);
    LAS char* red2r = lbase + L_RED2 + (lane & 15) * 144 + (lane >> 4) * 32;
    asm volatile("" : "+v"(red1), "+v"(redr), "+v"(red2r));
    tm = start_sync(p, ds, r, lds, tg);
    __builtin_amdgcn_s_setprio(2);
    auto step = [&](const int s, auto LASTC) {
        constexpr bool LAST = decltype(LASTC)::value;
        const unsigned oB = (unsigned)__builtin_amdgcn_readfirstlane((int)step_base(p, cfg, s)), kc = (unsigned)__builtin_amdgcn_readfirstlane((int)step_cls(cfg, s));
        const unsigned oM = oB + (kc ^ ((unsigned)(beta & 1) * CLS)), oMn = (unsigned)__builtin_amdgcn_readfirstlane((int)step_base(p, cfg, s + 1));
        unsigned vo_pps = vo_pp ^ kc; PIN(vo_pps);
        wait_fine(tm);
        u32x4 gE, gO;
        unsigned long long bad64;
#define GDLY "s_sleep 1\n\t"
#define MF "v_mfma_f32_16x16x32_bf16 "
#define DPP8 " row_ror:8 row_mask:0xf bank_mask:0xf bound_ctrl:1\n\t"
#define GSTAGE(OFF) \
            asm volatile( \
                "buffer_load_dwordx4 v[200:203], %[m0], %[ds], %[oM] offen sc1\n\t" \
                GDLY \
                "buffer_load_dwordx4 v[204:207], %[m1], %[ds], %[oM] offen sc1\n\t" \
                "buffer_load_dwordx4 v[208:211], %[mc], %[ds], %[oM] offen sc1\n\t" \
                "s_waitcnt vmcnt(2)\n\t" \
                "v_and_b32 v212, v200, %[mE]\n\tv_and_b32 v213, v201, %[mE]\n\tv_and_b32 v214, v202, %[mE]\n\tv_and_b32 v215, v203, %[mE]\n\t" \
                "s_nop 1\n\t" \
                MF "v[236:239], %[a000], v[212:215], 0\n\t" \
                "v_and_b32_dpp v216, v200, %[mE]" DPP8 "v_and_b32_dpp v217, v201, %[mE]" DPP8 \
                MF "v[240:243], %[a001], v[212:215], 0\n\t" \
                "v_and_b32_dpp v218, v202, %[mE]" DPP8 "v_and_b32_dpp v219, v203, %[mE]" DPP8 \
                MF "v[244:247], %[a002], v[212:215], 0\n\t" \
                "v_max3_u32 v196, v200, v201, v202\n\t" \
                MF "v[248:251], %[a003], v[212:215], 0\n\t" \
                "v_max_u32 v196, v196, v203\n\t" \
                MF "v[236:239], %[a010], v[216:219], v[236:239]\n\t" \
                MF "v[240:243], %[a011], v[216:219], v[240:243]\n\t" \
                "s_waitcnt vmcnt(1)\n\t" \
                "v_and_b32 v220, v204, %[mE]\n\tv_and_b32 v221, v205, %[mE]\n\t" \
                MF "v[244:247], %[a012], v[216:219], v[244:247]\n\t" \
                "v_and_b32 v222, v206, %[mE]\n\tv_and_b32 v223, v207, %[mE]\n\t" \
                MF "v[248:251], %[a013], v[216:219], v[248:251]\n\t" \
                "v_and_b32_dpp v224, v204, %[mE]" DPP8 "v_and_b32_dpp v225, v205, %[mE]" DPP8 \
                MF "v[236:239], %[a100], v[220:223], v[236:239]\n\t" \
                "v_and_b32_dpp v226, v206, %[mE]" DPP8 "v_and_b32_dpp v227, v207, %[mE]" DPP8 \
                MF "v[240:243], %[a101], v[220:223], v[240:243]\n\t" \
                "v_max3_u32 v196, v196, v204, v205\n\tv_max3_u32 v196, v196, v206, v207\n\t" \
                MF "v[236:239], %[a110], v[224:227], v[236:239]\n\t" \
                "s_waitcnt vmcnt(0)\n\t" \
                "v_and_b32 v228, v208, %[mE]\n\tv_and_b32 v229, v209, %[mE]\n\t" \
                MF "v[240:243], %[a111], v[224:227], v[240:243]\n\t" \
                "v_and_b32 v230, v210, %[mE]\n\tv_and_b32 v231, v211, %[mE]\n\t" \
                MF "v[244:247], %[a102], v[220:223], v[244:247]\n\t" \
                "v_and_b32_dpp v232, v208, %[mE]" DPP8 "v_and_b32_dpp v233, v209, %[mE]" DPP8 \
                MF "v[248:251], %[a103], v[220:223], v[248:251]\n\t" \
                "v_and_b32_dpp v234, v210, %[mE]" DPP8 "v_and_b32_dpp v235, v211, %[mE]" DPP8 \
                MF "v[244:247], %[a112], v[224:227], v[244:247]\n\t" \
                "v_cvt_pk_bf16_f32 v184, v236, v237\n\tv_cvt_pk_bf16_f32 v185, v238, v239\n\t" \
                MF "v[248:251], %[a113], v[224:227], v[248:251]\n\t" \
                "v_cvt_pk_bf16_f32 v186, v240, v241\n\tv_cvt_pk_bf16_f32 v187, v242, v243\n\t" \
                "v_max3_u32 v196, v196, v208, v209\n\tv_max3_u32 v196, v196, v210, v211\n\t" \
                MF "v[192:195], v[184:187], v[228:231], 0\n\t" \
                "v_cvt_pk_bf16_f32 v188, v244, v245\n\tv_cvt_pk_bf16_f32 v189, v246, v247\n\t" \
                "s_nop 0\n\t" \
                "v_cvt_pk_bf16_f32 v190, v248, v249\n\tv_cvt_pk_bf16_f32 v191, v250, v251\n\t" \
                "v_cmp_eq_u32_e64 %[bad], -1, v196\n\t" \
                "s_nop 0\n\t" \
                MF "v[192:195], v[188:191], v[232:235], v[192:195]\n\t" \
                "s_nop 7\n\t" \
                "ds_write_b128 %[red], v[192:195]\n\t" \
                : "={v[184:187]}"(gE), "={v[188:191]}"(gO), [bad] "=s"(bad64) \
                : [m0] "v"(vo_m0), [m1] "v"(vo_m1), [mc] "v"(vo_mc), [ds] "s"(ds), [oM] "s"(OFF), [mE] "v"(mE), [red] "v"(red1), \
                  [a000] "v"(aw[0][0][0]), [a001] "v"(aw[0][0][1]), [a002] "v"(aw[0][0][2]), [a003] "v"(aw[0][0][3]), \
                  [a010] "v"(aw[0][1][0]), [a011] "v"(aw[0][1][1]), [a012] "v"(aw[0][1][2]), [a013] "v"(aw[0][1][3]), \
                  [a100] "v"(aw[1][0][0]), [a101] "v"(aw[1][0][1]), [a102] "v"(aw[1][0][2]), [a103] "v"(aw[1][0][3]), \
                  [a110] "v"(aw[1][1][0]), [a111] "v"(aw[1][1][1]), [a112] "v"(aw[1][1][2]), [a113] "v"(aw[1][1][3]) \
                : "memory", "v192", "v193", "v194", "v195", "v196", "v200", "v201", "v202", "v203", "v204", "v205", "v206", "v207", "v208", "v209", "v210", "v211", \
                  "v212", "v213", "v214", "v215", "v216", "v217", "v218", "v219", "v220", "v221", "v222", "v223", "v224", "v225", "v226", "v227", "v228", "v229", "v230", "v231", \
                  "v232", "v233", "v234", "v235", "v236", "v237", "v238", "v239", "v240", "v241", "v242", "v243", "v244", "v245", "v246", "v247", "v248", "v249", "v250", "v251")
        GSTAGE(oM);
        if (__builtin_expect(bad64 != 0ull, 0)) {
            int spins = 0; const int lim = dead ? 1 : SPIN_MAX; bool bad;
            _Pragma("nounroll") do { if (!dead) wait_fine(lat_next(tm)); GSTAGE(oM); bad = bad64 != 0ull; } while (bad && ++spins < lim);
            if (bad) dead = true;
        }
        bar();
        if (!LAST && w == 0) {
            const f32x2 a = *(const LAS f32x2*)(redr), b2 = *(const LAS f32x2*)(redr + 256), c = *(const LAS f32x2*)(redr + 512), d = *(const LAS f32x2*)(redr + 768);
            const f32x2 t2 = (a + b2) + (c + d);
            const unsigned o = nopoison(pk2(t2.x, t2.y));
            AST1(o, vo_pps, ds, oB);
            SCHEDB();
        }
        const unsigned tb1 = rt32() - (w == 0 ? (unsigned)W0OFF : 0u);
        const unsigned th = lat_shift(tm + O1T + lv, (int)(tb1 + MINH - (tm + O1T + lv)), DZ1);
        if (__builtin_expect(!LAST && w == 1 && (s == CK0 || s == CK1 || s == CK1 + 1), 0)) {
            if (s == CK1 + 1) {
                const unsigned x = clk_margin(ds, oM + CLK_OFF, dead);
                if (lane == 0) *(volatile LAS unsigned*)((LAS char*)lds + L_TS) = x;
            } else if (r < 8 && !(r & 1)) {
                unsigned c, t;
                clk_edge(c, t);
                if (s == CK0) { ck_c = c; ck_t = t; }
                else {
                    unsigned fq = nopoison((unsigned)(((unsigned long long)(c - ck_c) * 100ull) / (unsigned long long)max(t - ck_t, 1u))); PIN(fq);
                    AST1(fq, lane < 2 ? (unsigned)(lane * CLS + CLK_OFF + (r >> 1) * 4) : OOB, ds, oMn);
                }
            }
        }
#pragma unroll
        for (int it = 0; it < 2; ++it) {
            const char* pxb = lds + L_PX + lane * 16;
            const u32x4 x0 = *(const u32x4*)(pxb + (it * 2 + 0) * 1024), x1 = *(const u32x4*)(pxb + (it * 2 + 1) * 1024);
            f32x4 dx = mfma32(gE, x0, (f32x4){0, 0, 0, 0});
            dx = mfma32(gO, x1, dx);
            *(f32x4*)(lds + L_REDX + (2 * w + (q4 >> 1)) * 1024 + ((it * 2 + (q4 & 1)) * 16 + col) * 16) = dx;
        }
        bar();
        if (!LAST && w == 3) {
            asm volatile(
                "ds_read_b128 v[200:203], %[red]\n\t"
                "ds_read_b128 v[204:207], %[red] offset:16\n\t"
                "s_waitcnt lgkmcnt(1)\n\t"
                "v_add_f32 v200, v200, v202\n\tv_add_f32 v201, v201, v203\n\t"
                "s_waitcnt lgkmcnt(0)\n\t"
                "v_add_f32 v204, v204, v206\n\tv_add_f32 v205, v205, v207\n\t"
                "v_add_f32 v200, v200, v204\n\tv_add_f32 v201, v201, v205\n\t"
                "v_mov_b32 v202, v200\n\tv_mov_b32 v203, v201\n\t"
                "s_nop 0\n\t"
                "v_permlane16_swap_b32 v200, v202\n\tv_permlane16_swap_b32 v201, v203\n\t"
                "v_add_f32 v200, v200, v202\n\tv_add_f32 v201, v201, v203\n\t"
                "v_mov_b32 v202, v200\n\tv_mov_b32 v203, v201\n\t"
                "s_nop 0\n\t"
                "v_permlane32_swap_b32 v200, v202\n\tv_permlane32_swap_b32 v201, v203\n\t"
                "v_add_f32 v200, v200, v202\n\tv_add_f32 v201, v201, v203\n\t"
                "v_cvt_pk_bf16_f32 v200, v200, v201\n\t"
                "v_min_u32 v200, -2, v200\n\t"
                "buffer_store_dword v200, %[vo], %[ds], %[oM] offen sc1\n\t"
                :: [red] "v"(red2r), [vo] "v"(vo_mp), [ds] "s"(ds), [oM] "s"(oMn)
                : "memory", "v200", "v201", "v202", "v203", "v204", "v205", "v206", "v207");
            SCHEDB();
        }
        const unsigned tb2 = rt32() - (w == 3 ? 9u : 0u);
        if (w == 2) {
            f32x4 t4 = *(const f32x4*)(lds + L_REDX + lane * 16);
#pragma unroll
            for (int pp = 1; pp < 4; ++pp) t4 += *(const f32x4*)(lds + L_REDX + 2 * pp * 1024 + lane * 16);
            u32x4 xm = __builtin_bit_cast(u32x4, t4);
            nopoison4(xm);
            AST4(xm, vo_xm, ds, (unsigned)s * (16u * 7u * 1024u));
        }
        if (__builtin_expect(!LAST && s == CK1 + 1, 0)) lv = (unsigned)__builtin_amdgcn_readfirstlane(*(volatile LAS unsigned*)((LAS char*)lds + L_TS));
        tm = lat_shift(th + C2H + lv, (int)(tb2 + MINM - (th + C2H + lv)), DZ2);
    };
    _Pragma("nounroll") for (int s = 0; s < NSEG - 1; ++s) step(s, BoolC<false>{});
    step(NSEG - 1, BoolC<true>{});
    AWAIT(0);
    bar();
}

DEVI void chain_bw(const Params& p, int r, char* lds, u32x4 ds, Tag tg) {
    const rsrc_t rs = mkrsrc(p.ws, 0x10000000u);
    const int tid = threadIdx.x, lane = tid & 63, w = __builtin_amdgcn_readfirstlane(tid >> 6), hw = w & 3;
    const int col = lane & 15, q4 = lane >> 4;
    const int c7 = g_chunk(r), beta = r >> 1, rh = r & 1;
    u32x4 bw[2][14];
    if (!CPOLL) await_prep(p, ds, tg, lane < 8 ? prep_blk(1, 4 * hw + (lane >> 1), lane & 1, beta / 14) : -1);
#pragma unroll
    for (int a = 0; a < 2; ++a)
#pragma unroll
        for (int t = 0; t < 14; ++t) bw[a][t] = bld4(rs, (unsigned)(OFF_BF + ((size_t)((2 * hw + a) * 56 + beta) * 14 + t) * FR + lane * 16), 0);
    u32x4 sel;
    { const int j = col & 7; const unsigned one = (j & 1) ? 0x3F800000u : 0x00003F80u; sel.x = (j >> 1) == 0 ? one : 0; sel.y = (j >> 1) == 1 ? one : 0; sel.z = (j >> 1) == 2 ? one : 0; sel.w = (j >> 1) == 3 ? one : 0; }
    const unsigned cfg = get_cfg(p, rs, lds, ds, tg);
    poison_rest(p, r, rs, cfg);
    WAITVM0();
    bool dead = false;
    const int hh = col >> 2, i2 = col & 3, hB = 4 * hw + hh;
    const unsigned vo_pa = (unsigned)((rh ? PB_OFF : PA_OFF) + slot_of(hB, q4) * 64 + i2 * 16);
    const unsigned vo_pb = (q4 == 3) ? OOB : (unsigned)((rh ? PB_OFF : PA_OFF) + slot_of(hB, 4 + q4) * 64 + i2 * 16);
    const unsigned vo_stat = (unsigned)(STATS_OFF + stat_off(hB) + rh * 16 + i2 * 4);
    const unsigned vo_ex = (unsigned)(EX_OFF + hB * 512 + rh * 256 + q4 * 64 + i2 * 16);
    const unsigned vo_u0 = (unsigned)(OFF_UF + ((size_t)beta * 8 + 2 * hw) * FR + lane * 16), vo_u1 = vo_u0 + 1024;
    const unsigned vo_x = (unsigned)(OFF_XF + (size_t)((hw >> 1) * 14 + 2 * c7 + (hw & 1)) * FR + lane * 16);
    const unsigned vo_fu = (unsigned)(OFF_FLAG + (FLAG_U + beta / 14) * 4);
    const unsigned vo_wc = (rh == 0 && lane < 16) ? (unsigned)(OFF_WC + ((size_t)beta * 8 + 2 * hw) * 256 + col * 16) : OOB;
    const bool diag = (col >> 3) == (q4 >> 1);
    const bool outv = (hh & 1) == (q4 >> 1);
    const unsigned long long lomask = 0x00FF00FF00FF00FFull;
    unsigned mlo = (hh < 2) ? 0xFFFFFFFFu : 0u, mhi = ~mlo;
    u32x4 ones = {0x3F803F80u, 0x3F803F80u, 0x3F803F80u, 0x3F803F80u};
    LAS char* red2 = (LAS char*)lds + (outv ? L_RED2 + ((i2 * 4 + 2 * (q4 & 1)) * 36 + hB * 2) * 4 : L_XX + 2048 + hw * 1024 + lane * 8);
    asm volatile("" : "+v"(mlo), "+v"(mhi), "+v"(ones), "+v"(red2));
    u32x4 un0 = {0,0,0,0}, un1 = {0,0,0,0}, px = {0,0,0,0};
    unsigned fu = 0;
    u32x4 pa = {0,0,0,0}, pb = {0,0,0,0}, ex = {0,0,0,0}; unsigned st = 0;
    auto u_first = [&](unsigned so) {
        int spins = 0; const int lim = dead ? 1 : SPIN_MAX; bool bad;
        _Pragma("nounroll") do {
            ALD4R(un0, vo_u0, ds, so); ALD4R(un1, vo_u1, ds, so);
            asm volatile("s_waitcnt vmcnt(0)" : "+v"(un0), "+v"(un1) :: "memory");
            bad = __any(max(max4(un0), max4(un1)) == POISON);
            if (bad) __builtin_amdgcn_s_sleep(4);
        } while (bad && ++spins < lim);
        if (bad) dead = true;
    };
    if (!UDEF) u_first(0u);
    ALD4R(px, vo_x, ds, 0u);
    fu = 8u;
    const unsigned vo_s0 = (unsigned)(2 * hw * 1024 + m_lane_off(lane)), vo_s1 = (hw < 3) ? vo_s0 + 1024u : OOB;
    LAS char* ls0 = (LAS char*)lds + L_R + 2 * hw * 1024 + lane * 16; LAS char* ls1 = (LAS char*)lds + ((hw < 3) ? (unsigned)(L_R + (2 * hw + 1) * 1024 + lane * 16) : (unsigned)(L_XX + lane * 16));
    asm volatile("" : "+v"(ls0), "+v"(ls1));
    unsigned tm = start_sync(p, ds, r, lds, tg), lv = LV0;
    auto step = [&](const int s, auto LASTC) {
        constexpr bool LAST = decltype(LASTC)::value;
        const unsigned oM = (unsigned)__builtin_amdgcn_readfirstlane((int)(step_base(p, cfg, s) + (step_cls(cfg, s) ^ ((unsigned)(beta & 1) * CLS))));
        wait_fine(tm);
        __builtin_amdgcn_s_sleep(BDLY);
        u32x4 S0, S1;
#define SLOAD() asm volatile("buffer_load_dwordx4 v[200:203], %[v0], %[ds], %[oM] offen sc1\n\tbuffer_load_dwordx4 v[204:207], %[v1], %[ds], %[oM] offen sc1" \
            : "={v[200:203]}"(S0), "={v[204:207]}"(S1) : [v0] "v"(vo_s0), [v1] "v"(vo_s1), [ds] "s"(ds), [oM] "s"(oM) : "memory")
#define SPUT(N0, N1) asm volatile( \
            "s_waitcnt vmcnt(" #N0 ")\n\t" \
            "ds_write_b128 %[l0], v[200:203]\n\t" \
            "v_max3_u32 v208, v200, v201, v202\n\tv_max_u32 v208, v208, v203\n\t" \
            "s_waitcnt vmcnt(" #N1 ")\n\t" \
            "ds_write_b128 %[l1], v[204:207]\n\t" \
            "v_max3_u32 v208, v208, v204, v205\n\tv_max3_u32 v208, v208, v206, v207\n\t" \
            "v_cmp_eq_u32_e64 %[bad], -1, v208\n\t" \
            : [bad] "=s"(sbad), "+{v[200:203]}"(S0), "+{v[204:207]}"(S1) : [l0] "v"(ls0), [l1] "v"(ls1) : "memory", "v208")
        unsigned long long sbad;
        if (UDEF && __builtin_expect(s == 0, 0)) { ALD4R(un0, vo_u0, ds, 0u); ALD4R(un1, vo_u1, ds, 0u); }
        SLOAD();
        asm volatile("s_waitcnt vmcnt(2)" : "+v"(un0), "+v"(un1), "+v"(px), "+v"(fu) :: "memory");
        SCHEDB();
        if (!UDEF && __builtin_expect(s == 1, 0)) { if (__any(max(max4(un0), max4(un1)) == POISON)) { asm volatile("s_waitcnt vmcnt(0)" : "+{v[200:203]}"(S0), "+{v[204:207]}"(S1) :: "memory"); u_first(56u * 8u * 1024u); } }
        u32x4 uf0 = un0, uf1 = un1;
        asm volatile("" : "+v"(uf0), "+v"(uf1));
        const unsigned fu_s = (unsigned)__builtin_amdgcn_readfirstlane(fu);
        *(u32x4*)(lds + L_PX + hw * 1024 + lane * 16) = px;
        SCHEDB();
        if (UEARLY > 0 && !LAST) {
            if (__builtin_expect(s + 1 < NSEG - 1 && fu_s < 8u, 0)) await_flag(ds, vo_fu + (unsigned)(s + 1) * 64u, 8, dead);
            const unsigned sn = (unsigned)(s + 1);
            ALD4R(un0, vo_u0, ds, sn * (56u * 8u * 1024u));
            if (UEARLY != 3) ALD4R(un1, vo_u1, ds, sn * (56u * 8u * 1024u));
            if (UEARLY == 2) { ALD4R(px, vo_x, ds, sn * 28672u); ALD1R(fu, vo_fu, ds, (sn + 1u) * 64u); }
            SCHEDB();
            if (UEARLY == 2) SPUT(5, 4); else if (UEARLY == 1) SPUT(3, 2); else SPUT(2, 1);
        } else SPUT(1, 0);
        if (__builtin_expect(sbad != 0ull, 0)) {
            int spins = 0; const int lim = dead ? 1 : SPIN_MAX; bool bad;
            _Pragma("nounroll") do {
                if (!dead) { wait_fine(lat_next(tm)); __builtin_amdgcn_s_sleep(BDLY); }
                SLOAD();
                SPUT(1, 0);
                bad = sbad != 0ull;
            } while (bad && ++spins < lim);
            if (bad) dead = true;
        }
        bar();
        if (LAST) bar();
        const unsigned tb1 = rt32();
        SCHEDB();
        u32x4 Rq[7];
#pragma unroll
        for (int q = 0; q < 7; ++q) Rq[q] = *(const u32x4*)(lds + L_R + q * 1024 + lane * 16);
        SCHEDB();
        f32x4 ae[2] = {{0,0,0,0},{0,0,0,0}}, ao[2] = {{0,0,0,0},{0,0,0,0}};
        const unsigned th = lat_shift(tm + O1T + lv, (int)(tb1 + MINH - (tm + O1T + lv)), DZ1);
#pragma unroll
        for (int q = 0; q < WSPLIT; ++q)
#pragma unroll
            for (int a = 0; a < 2; ++a) { ae[a] = mfma32(Rq[q], bw[a][2 * q], ae[a]); ao[a] = mfma32(Rq[q], bw[a][2 * q + 1], ao[a]); }
        SCHEDB();
        if (!LAST) {
            wait_fine(th);
            ALD4R(pa, vo_pa, ds, oM);
            ALD4R(pb, vo_pb, ds, oM);
            ALD1R(st, vo_stat, ds, oM);
            ALD4R(ex, vo_ex, ds, oM);
            SCHEDB();
        }
#pragma unroll
        for (int q = WSPLIT; q < 7; ++q)
#pragma unroll
            for (int a = 0; a < 2; ++a) { ae[a] = mfma32(Rq[q], bw[a][2 * q], ae[a]); ao[a] = mfma32(Rq[q], bw[a][2 * q + 1], ao[a]); }
        u32x2 wop[2]; u32x4 wst[2];
#pragma unroll
        for (int a = 0; a < 2; ++a) {
            float wv[4];
#pragma unroll
            for (int rr = 0; rr < 4; ++rr) { const float x = (lane < 32) ? ae[a][rr] : ao[a][rr]; float u, v; swap32(x, u, v); wv[rr] = u + v; }
            const u32x2 wp = {pk2(wv[0], wv[1]), pk2(wv[2], wv[3])};
            wop[a].x = diag ? wp.x : 0u; wop[a].y = diag ? wp.y : 0u;
            unsigned a0, b0, a1, b1; uswap16(wp.x, a0, b0); uswap16(wp.y, a1, b1); wst[a] = (u32x4){a0, a1, b0, b1};
            nopoison4(wst[a]);
        }
        asm volatile("" : "+v"(wop[0]), "+v"(wop[1]), "+v"(wst[0]), "+v"(wst[1]));
        SCHEDB();
        if (!LAST) {
            const u32x4 wA = {wop[0].x, wop[0].y, wop[1].x, wop[1].y};
            unsigned long long bad64;
#define BSTAGE() \
            asm volatile( \
                MF "v[216:219], %[sel], %[pa], 0\n\t" \
                MF "v[216:219], %[sel], %[pb], v[216:219]\n\t" \
                "v_max3_u32 v248, %[pax], %[pay], %[paz]\n\tv_max3_u32 v248, v248, %[paw], %[pbx]\n\t" \
                MF "v[220:223], %[uf0], %[ex], 0\n\t" \
                "v_max3_u32 v248, v248, %[pby], %[pbz]\n\tv_max3_u32 v248, v248, %[pbw], %[exx]\n\t" \
                MF "v[224:227], %[uf1], %[ex], 0\n\t" \
                "v_max3_u32 v248, v248, %[exy], %[exz]\n\tv_max3_u32 v248, v248, %[exw], %[st]\n\t" \
                MF "v[228:231], %[ones], %[ex], 0\n\t" \
                "v_max3_f32 v232, v216, v217, v218\n\tv_max3_f32 v232, v232, v219, %[st]\n\t" \
                "v_mov_b32 v233, v232\n\t" \
                "v_cmp_eq_u32_e32 vcc, -1, v248\n\t" \
                "s_cbranch_vccnz .LBX%=\n\t" \
                "v_permlane16_swap_b32 v232, v233\n\t" \
                "v_max_f32 v232, v232, v233\n\t" \
                "v_mul_f32 v234, 0xbdb8aa3b, v232\n\t" \
                "v_fmamk_f32 v235, %[st], 0x3db8aa3b, v234\n\t" \
                "v_fmamk_f32 v236, v216, 0x3db8aa3b, v234\n\tv_fmamk_f32 v237, v217, 0x3db8aa3b, v234\n\t" \
                "v_exp_f32 v235, v235\n\t" \
                "v_fmamk_f32 v238, v218, 0x3db8aa3b, v234\n\tv_fmamk_f32 v239, v219, 0x3db8aa3b, v234\n\t" \
                "v_exp_f32 v236, v236\n\tv_exp_f32 v237, v237\n\t" \
                "v_cndmask_b32_e64 v244, v224, v220, %[lo]\n\tv_cndmask_b32_e64 v245, v225, v221, %[lo]\n\t" \
                "v_exp_f32 v238, v238\n\tv_exp_f32 v239, v239\n\t" \
                "v_cndmask_b32_e64 v246, v226, v222, %[lo]\n\tv_cndmask_b32_e64 v247, v227, v223, %[lo]\n\t" \
                "v_cvt_pk_bf16_f32 v249, v236, v237\n\tv_cvt_pk_bf16_f32 v250, v238, v239\n\t" \
                "v_mul_f32 v244, v244, v235\n\tv_mul_f32 v245, v245, v235\n\t" \
                "v_and_b32 v240, v249, %[mlo]\n\tv_and_b32 v241, v250, %[mlo]\n\tv_and_b32 v242, v249, %[mhi]\n\tv_and_b32 v243, v250, %[mhi]\n\t" \
                "v_mul_f32 v246, v246, v235\n\tv_mul_f32 v247, v247, v235\n\t" \
                "v_add_f32 v236, v236, v237\n\tv_add_f32 v238, v238, v239\n\t" \
                MF "v[244:247], %[wA], v[240:243], v[244:247]\n\t" \
                "v_add_f32 v232, v236, v238\n\t" \
                "v_mov_b32 v233, v232\n\t" \
                "s_nop 1\n\t" \
                "v_permlane16_swap_b32 v232, v233\n\t" \
                "v_add_f32 v232, v232, v233\n\t" \
                "v_fmac_f32 v232, v235, v228\n\t" \
                "v_rcp_f32 v232, v232\n\t" \
                "s_nop 0\n\t" \
                "v_mul_f32 v244, v244, v232\n\tv_mul_f32 v245, v245, v232\n\tv_mul_f32 v246, v246, v232\n\tv_mul_f32 v247, v247, v232\n\t" \
                "ds_write2_b64 %[red], v[244:245], v[246:247] offset1:18\n" \
                ".LBX%=:\n\ts_mov_b64 %[bad], vcc\n\t" \
                : [bad] "=s"(bad64) \
                : [sel] "v"(sel), [pa] "v"(pa), [pb] "v"(pb), [ex] "v"(ex), [uf0] "v"(uf0), [uf1] "v"(uf1), [ones] "v"(ones), [wA] "v"(wA), [st] "v"(st), \
                  [pax] "v"(pa.x), [pay] "v"(pa.y), [paz] "v"(pa.z), [paw] "v"(pa.w), [pbx] "v"(pb.x), [pby] "v"(pb.y), [pbz] "v"(pb.z), [pbw] "v"(pb.w), \
                  [exx] "v"(ex.x), [exy] "v"(ex.y), [exz] "v"(ex.z), [exw] "v"(ex.w), [lo] "s"(lomask), [mlo] "v"(mlo), [mhi] "v"(mhi), [red] "v"(red2) \
                : "memory", "vcc", "v216", "v217", "v218", "v219", "v220", "v221", "v222", "v223", "v224", "v225", "v226", "v227", "v228", "v229", "v230", "v231", "v232", "v233", \
                  "v234", "v235", "v236", "v237", "v238", "v239", "v240", "v241", "v242", "v243", "v244", "v245", "v246", "v247", "v248", "v249", "v250")
            if (UDEF && __builtin_expect(s <= 1, 0)) {
                int spins = 0; const int lim = dead ? 1 : SPIN_MAX;
                bool bad = __any(max(max4(uf0), max4(uf1)) == POISON);
                _Pragma("nounroll") while (bad && spins++ < lim) {
                    ALD4R(uf0, vo_u0, ds, (unsigned)s * (56u * 8u * 1024u)); ALD4R(uf1, vo_u1, ds, (unsigned)s * (56u * 8u * 1024u));
                    asm volatile("s_waitcnt vmcnt(0)" : "+v"(uf0), "+v"(uf1) :: "memory");
                    bad = __any(max(max4(uf0), max4(uf1)) == POISON);
                }
                if (bad) dead = true;
            }
            if (UEARLY == 0 && __builtin_expect(s + 1 < NSEG - 1 && fu_s < 8u, 0)) await_flag(ds, vo_fu + (unsigned)(s + 1) * 64u, 8, dead);
            {
                const unsigned sn = (unsigned)(s + 1);
                const unsigned sfu = sn;
                const unsigned sfx = sn;
                if (UEARLY < 1) ALD4R(un0, vo_u0, ds, sfu * (56u * 8u * 1024u));
                if (UEARLY < 1 || UEARLY == 3) ALD4R(un1, vo_u1, ds, sfu * (56u * 8u * 1024u));
                if (UEARLY != 2) { ALD4R(px, vo_x, ds, sfx * 28672u); ALD1R(fu, vo_fu, ds, (sn + 1u) * 64u); }
            }
            if (UEARLY == 0) asm volatile("s_waitcnt vmcnt(4)" : "+v"(pa), "+v"(pb), "+v"(st), "+v"(ex) :: "memory");
            else if (UEARLY == 1) asm volatile("s_waitcnt vmcnt(2)" : "+v"(pa), "+v"(pb), "+v"(st), "+v"(ex) :: "memory");
            else if (UEARLY == 3) asm volatile("s_waitcnt vmcnt(3)" : "+v"(pa), "+v"(pb), "+v"(st), "+v"(ex) :: "memory");
            else asm volatile("s_waitcnt vmcnt(0)" : "+v"(pa), "+v"(pb), "+v"(st), "+v"(ex) :: "memory");
            SCHEDB();
            BSTAGE();
            if (__builtin_expect(bad64 != 0ull, 0)) {
              int spins = 0; const int lim = dead ? 1 : SPIN_MAX; bool bad;
              _Pragma("nounroll") do {
                ALD4R(pa, vo_pa, ds, oM);
                ALD4R(pb, vo_pb, ds, oM);
                ALD1R(st, vo_stat, ds, oM);
                ALD4R(ex, vo_ex, ds, oM);
                asm volatile("s_waitcnt vmcnt(0)" : "+v"(pa), "+v"(pb), "+v"(st), "+v"(ex) :: "memory");
                bad = __any(max(max(max4(pa), max4(pb)), max(max4(ex), st)) == POISON);
              } while (bad && ++spins < lim);
              if (bad) dead = true;
              BSTAGE();
            }
        }
        if (!LAST) bar();
        const unsigned tb2 = rt32();
        if (LAST || WPOS != 1) { AST4(wst[0], vo_wc, ds, (unsigned)s * (56u * 8u * 256u)); AST4(wst[1], vo_wc + 256u, ds, (unsigned)s * (56u * 8u * 256u)); }
        if (__builtin_expect(!LAST && s == CK1 + 1, 0)) lv = (unsigned)__builtin_amdgcn_readfirstlane(*(volatile LAS unsigned*)((LAS char*)lds + L_TS));
        tm = lat_shift(th + C2H + lv, (int)(tb2 + MINM - (th + C2H + lv)), DZ2);
    };
    _Pragma("nounroll") for (int s = 0; s < NSEG - 1; ++s) step(s, BoolC<false>{});
    step(NSEG - 1, BoolC<true>{});
    AWAIT(0);
    bar();
}

DEVI void poison_rest(const Params& p, int c, rsrc_t rs, unsigned cfg) {
    const int lane = threadIdx.x & 63, w = __builtin_amdgcn_readfirstlane(threadIdx.x >> 6);
    const u32x4 pz = {POISON, POISON, POISON, POISON};
#pragma unroll
    for (int j = 0; j < 4; ++j) {
        const int u = c * 8 + w + 1024 * j, st = 2 + (u >> 6), ch = (u >> 3) & 7;
        if (u < 62 * 64) __builtin_amdgcn_raw_buffer_store_b128(pz, rs, step_base(p, cfg, st) + (unsigned)(ch >> 1) * 32768u + (unsigned)(ch & 1) * CLS + (unsigned)(u & 7) * 1024u + (unsigned)lane * 16u, 0, 16);
    }
}
DEVI void chain_g(const Params& p, int r, char* lds, Tag tg) {
    const int w = __builtin_amdgcn_readfirstlane(threadIdx.x >> 6);
    const u32x4 ds = mkdesc(p.ws, 0x10000000u);
    const unsigned t_entry = rt32();
    __builtin_amdgcn_s_sleep(GDELAY);
    if (CPOLL) {
        const int lane = threadIdx.x & 63;
        if (CPOLL == 1) {
            if (w == 7) await_prep(p, ds, tg, lane < 32 ? prep_blk(1, lane >> 1, lane & 1, (r >> 1) / 14) : (lane < 36 ? prep_blk(0, g_head(r), lane & 1, (4 * g_chunk(r) + 3 * ((lane >> 1) & 1)) / 7) : -1));
            bar();
        } else {
            if (w == 7) await_prep(p, ds, tg, lane < 32 ? prep_blk(1, lane >> 1, lane & 1, (r >> 1) / 14) : -1);
            if (w == 0) await_prep(p, ds, tg, lane < 4 ? prep_blk(0, g_head(r), lane & 1, (4 * g_chunk(r) + 3 * ((lane >> 1) & 1)) / 7) : -1);
            volatile LAS unsigned* fl = (volatile LAS unsigned*)((LAS char*)lds + L_CFG + 32);
            if (lane == 0 && (w == 0 || w == 7)) fl[w == 7] = tg.a;
            int k = 0;
            _Pragma("nounroll") while ((unsigned)__builtin_amdgcn_readfirstlane(fl[w >= 4]) != tg.a && ++k < SPIN_MAX) __builtin_amdgcn_s_sleep(1);
        }
    }
    if (w < 4) chain_gw(p, r, lds, ds, t_entry, tg); else chain_bw(p, r, lds, ds, tg);
}

struct XSoft { float e0[4], e1[4], mx, lx; };
DEVI XSoft xs_pre(f32x4 s0, f32x4 s1, int it, int lane) {
    const int col = lane & 15, q4 = lane >> 4;
    const float NEG = -3.0e38f;
    XSoft r;
    float m = NEG;
#pragma unroll
    for (int rr = 0; rr < 4; ++rr) {
        const bool causal = (4 * q4 + rr) <= col;
        r.e0[rr] = (it == 1 || causal) ? s0[rr] : NEG;
        r.e1[rr] = (it == 1 && causal) ? s1[rr] : NEG;
        m = fmaxf(m, fmaxf(r.e0[rr], r.e1[rr]));
    }
    { float a, b2; swap16(m, a, b2); m = fmaxf(a, b2); swap32(m, a, b2); m = fmaxf(a, b2); }
    const float nM = -m * SC2;
    float l = 0;
#pragma unroll
    for (int rr = 0; rr < 4; ++rr) {
        r.e0[rr] = (r.e0[rr] == NEG) ? 0.f : __builtin_amdgcn_exp2f(__builtin_fmaf(r.e0[rr], SC2, nM));
        r.e1[rr] = (r.e1[rr] == NEG) ? 0.f : __builtin_amdgcn_exp2f(__builtin_fmaf(r.e1[rr], SC2, nM));
        l += r.e0[rr] + r.e1[rr];
    }
    { float a, b2; swap16(l, a, b2); l = a + b2; swap32(l, a, b2); l = a + b2; }
    r.mx = m; r.lx = l;
    return r;
}
DEVI void xs_post(rsrc_t rs, const XSoft& x, f32x4 sm, int s, int h, int it, int lane) {
    const int col = lane & 15, q4 = lane >> 4;
    float m = fmaxf(fmaxf(sm[0], sm[1]), fmaxf(sm[2], sm[3]));
    { float a, b2; swap16(m, a, b2); m = fmaxf(a, b2); }
    m = fmaxf(m, x.mx);
    const float nM = -m * SC2, al = __builtin_amdgcn_exp2f(__builtin_fmaf(x.mx, SC2, nM));
    float vm[4], l = 0;
#pragma unroll
    for (int rr = 0; rr < 4; ++rr) { vm[rr] = __builtin_amdgcn_exp2f(__builtin_fmaf(sm[rr], SC2, nM)); l += vm[rr]; }
    { float a, b2; swap16(l, a, b2); l = a + b2; }
    const float inv = __builtin_amdgcn_rcpf(__builtin_fmaf(x.lx, al, l)), ix = inv * al;
    u32x4 px = {pk2(x.e0[0] * ix, x.e0[1] * ix), pk2(x.e0[2] * ix, x.e0[3] * ix), pk2(x.e1[0] * ix, x.e1[1] * ix), pk2(x.e1[2] * ix, x.e1[3] * ix)};
    nopoison4(px);
    bst4(rs, px, (unsigned)(OFF_PX + (((size_t)s * 16 + h) * 2 + it) * FR + lane * 16), 0);
    if (q4 < 2) { const u32x2 pm = {nopoison(pk2(vm[0] * inv, vm[1] * inv)), nopoison(pk2(vm[2] * inv, vm[3] * inv))};
        bst2(rs, pm, (unsigned)(OFF_PM + (((size_t)s * 8 + (h >> 1)) * 2 + it) * 512 + (col + 16 * (2 * (h & 1) + q4)) * 8), 0); }
}

DEVI void chain_x(const Params& p, int h, char* lds, Tag tg) {
    const int tid = threadIdx.x, lane = tid & 63, w = __builtin_amdgcn_readfirstlane(tid >> 6);
    const int col = lane & 15, q4 = lane >> 4;
    const rsrc_t rs = mkrsrc(p.ws, 0x10000000u);
    const u32x4 ds = mkdesc(p.ws, 0x10000000u);
    const unsigned cfg = get_cfg(p, rs, lds, ds, tg);
    poison_rest(p, 112 + h, rs, cfg);
    WAITVM0();
    bool dead = false;
    if (w < 4) {
        const int nq = (w < 3) ? 2 : 1;
        const unsigned vo_m0 = (unsigned)(2 * w * 1024 + m_lane_off(lane)), vo_m1 = (nq == 2) ? vo_m0 + 1024u : OOB;
        LAS char* redw = (LAS char*)lds + L_REDX + w * 1024 + lane * 16;
        unsigned mE = (col < 8) ? 0xFFFFFFFFu : 0u;
        asm volatile("" : "+v"(redw), "+v"(mE));
        f32x4 o = {0, 0, 0, 0};
        const unsigned vo_e = (unsigned)(EX_OFF + (col >> 3) * CLS + h * 512 + ((col >> 2) & 1) * 256 + q4 * 64 + (col & 3) * 16);
        const unsigned vo_es2 = (q4 == 0) ? (unsigned)(STATS_OFF + (col >> 3) * CLS + stat_off(h) + (col & 7) * 4) : OOB;
        const unsigned side = (unsigned)(h & 1) * CLS;
        volatile LAS unsigned* lts = (volatile LAS unsigned*)((LAS char*)lds + L_TS);
        unsigned lv = LV0;
        if (tid == 0) { lts[0] = POISON; lts[2] = POISON; lts[4] = LV0; lts[5] = LV0; }
        unsigned tmx = start_sync(p, ds, 112 + h, lds, tg);
        if (tid == 0) lts[1] = tmx;
        const unsigned vo_v = (unsigned)(lane * 16), vo_vx = (unsigned)(EX_OFF + (lane >> 4) * 512 + (lane & 15) * 16), vo_vs = (unsigned)(STATS_OFF + lane * 8);
        __builtin_amdgcn_s_setprio(3);
        for (int s = 0; s < NSEG; ++s) {
            const unsigned oB = (unsigned)__builtin_amdgcn_readfirstlane((int)step_base(p, cfg, s)), kc = (unsigned)__builtin_amdgcn_readfirstlane((int)step_cls(cfg, s));
            const unsigned oM = oB + (kc ^ side);
            const unsigned vo_es = vo_e;
            if (s < NSEG - 1) {
                u32x4 g[2][2][2];
#pragma unroll
                for (int qi = 0; qi < 2; ++qi)
#pragma unroll
                    for (int e = 0; e < 2; ++e)
#pragma unroll
                        for (int k = 0; k < 2; ++k) g[qi][e][k] = *(const u32x4*)(lds + L_GX + (s & 1) * 28672 + (k * 14 + 2 * ((w < 3) ? 2 * w + qi : 2 * w) + e) * 1024 + lane * 16);
                if (s > 0 && w != 1) {
                    int k = dead ? SPIN_MAX : 0;
                    _Pragma("nounroll") for (; k < SPIN_MAX; ++k) { if ((unsigned)__builtin_amdgcn_readfirstlane(lts[(s & 1) * 2]) == (unsigned)s) break; __builtin_amdgcn_s_sleep(1); }
                    if (k >= SPIN_MAX) dead = true;
                    tmx = (unsigned)__builtin_amdgcn_readfirstlane(lts[(s & 1) * 2 + 1]);
                }
                if (!XNOW || s > 0) wait_fine(tmx + XOFFT);
                unsigned long long bad64;
#define XSTAGE(OFF) \
                asm volatile( \
                    "buffer_load_dwordx4 v[200:203], %[m0], %[ds], %[oM] offen sc1\n\t" \
                    "buffer_load_dwordx4 v[204:207], %[m1], %[ds], %[oM] offen sc1\n\t" \
                    "s_waitcnt vmcnt(1)\n\t" \
                    "v_and_b32 v212, v200, %[mE]\n\tv_and_b32 v213, v201, %[mE]\n\tv_and_b32 v214, v202, %[mE]\n\tv_and_b32 v215, v203, %[mE]\n\t" \
                    "v_and_b32_dpp v216, v200, %[mE]" DPP8 "v_and_b32_dpp v217, v201, %[mE]" DPP8 \
                    MF "v[236:239], %[g000], v[212:215], 0\n\t" \
                    "v_and_b32_dpp v218, v202, %[mE]" DPP8 "v_and_b32_dpp v219, v203, %[mE]" DPP8 \
                    MF "v[240:243], %[g001], v[212:215], 0\n\t" \
                    "v_max3_u32 v196, v200, v201, v202\n\tv_max_u32 v196, v196, v203\n\t" \
                    MF "v[236:239], %[g010], v[216:219], v[236:239]\n\t" \
                    "s_waitcnt vmcnt(0)\n\t" \
                    "v_and_b32 v220, v204, %[mE]\n\tv_and_b32 v221, v205, %[mE]\n\t" \
                    MF "v[240:243], %[g011], v[216:219], v[240:243]\n\t" \
                    "v_and_b32 v222, v206, %[mE]\n\tv_and_b32 v223, v207, %[mE]\n\t" \
                    "v_and_b32_dpp v224, v204, %[mE]" DPP8 "v_and_b32_dpp v225, v205, %[mE]" DPP8 \
                    MF "v[236:239], %[g100], v[220:223], v[236:239]\n\t" \
                    "v_and_b32_dpp v226, v206, %[mE]" DPP8 "v_and_b32_dpp v227, v207, %[mE]" DPP8 \
                    MF "v[240:243], %[g101], v[220:223], v[240:243]\n\t" \
                    "v_max3_u32 v196, v196, v204, v205\n\tv_max3_u32 v196, v196, v206, v207\n\t" \
                    MF "v[240:243], %[g111], v[224:227], v[240:243]\n\t" \
                    MF "v[236:239], %[g110], v[224:227], v[236:239]\n\t" \
                    "v_cmp_eq_u32_e64 %[bad], -1, v196\n\t" \
                    "s_nop 6\n\t" \
                    "v_add_f32_dpp v192, v240, v236" DPP8 "v_add_f32_dpp v193, v241, v237" DPP8 "v_add_f32_dpp v194, v242, v238" DPP8 "v_add_f32_dpp v195, v243, v239" DPP8 \
                    "ds_write_b128 %[red], v[192:195]\n\t" \
                    : "={v[192:195]}"(o), [bad] "=s"(bad64) \
                    : [m0] "v"(vo_m0), [m1] "v"(vo_m1), [ds] "s"(ds), [oM] "s"(OFF), [mE] "v"(mE), [red] "v"(redw), \
                      [g000] "v"(g[0][0][0]), [g001] "v"(g[0][0][1]), [g010] "v"(g[0][1][0]), [g011] "v"(g[0][1][1]), \
                      [g100] "v"(g[1][0][0]), [g101] "v"(g[1][0][1]), [g110] "v"(g[1][1][0]), [g111] "v"(g[1][1][1]) \
                    : "memory", "v196", "v200", "v201", "v202", "v203", "v204", "v205", "v206", "v207", "v212", "v213", "v214", "v215", "v216", "v217", "v218", "v219", \
                      "v220", "v221", "v222", "v223", "v224", "v225", "v226", "v227", "v236", "v237", "v238", "v239", "v240", "v241", "v242", "v243")
                XSTAGE(oM);
                if (__builtin_expect(bad64 != 0ull, 0)) {
                    int spins = 0; const int lim = dead ? 1 : SPIN_MAX; bool bad;
                    _Pragma("nounroll") do { if (!dead) wait_fine(lat_next(tmx) + XOFFT); XSTAGE(oM); bad = bad64 != 0ull; } while (bad && ++spins < lim);
                    if (bad) dead = true;
                }
            }
            bar();
            if (w == 0 && s < NSEG - 1) {
                asm volatile(
                    "ds_read_b128 v[200:203], %[red] offset:1024\n\t"
                    "ds_read_b128 v[204:207], %[red] offset:2048\n\t"
                    "ds_read_b128 v[208:211], %[red] offset:3072\n\t"
                    "s_mov_b64 vcc, %[hi]\n\t"
                    "s_waitcnt lgkmcnt(2)\n\t"
                    "v_add_f32 v192, v192, v200\n\tv_add_f32 v193, v193, v201\n\tv_add_f32 v194, v194, v202\n\tv_add_f32 v195, v195, v203\n\t"
                    "s_waitcnt lgkmcnt(0)\n\t"
                    "v_add_f32 v204, v204, v208\n\tv_add_f32 v205, v205, v209\n\tv_add_f32 v206, v206, v210\n\tv_add_f32 v207, v207, v211\n\t"
                    "v_add_f32 v192, v192, v204\n\tv_add_f32 v193, v193, v205\n\tv_add_f32 v194, v194, v206\n\tv_add_f32 v195, v195, v207\n\t"
                    "v_max3_f32 v196, v192, v193, v194\n\tv_max_f32 v196, v196, v195\n\t"
                    "s_nop 1\n\t"
                    "v_max_f32_dpp v196, v196, v196" DPP8
                    "v_mov_b32 v197, v196\n\t"
                    "s_nop 1\n\t"
                    "v_permlane16_swap_b32 v196, v197\n\t"
                    "v_max_f32 v196, v196, v197\n\t"
                    "v_mov_b32 v197, v196\n\t"
                    "s_nop 1\n\t"
                    "v_permlane32_swap_b32 v196, v197\n\t"
                    "v_max_f32 v196, v196, v197\n\t"
                    "v_mul_f32 v197, 0xbdb8aa3b, v196\n\t"
                    "v_min_u32 v196, -2, v196\n\t"
                    "v_fmamk_f32 v192, v192, 0x3db8aa3b, v197\n\tv_fmamk_f32 v193, v193, 0x3db8aa3b, v197\n\t"
                    "buffer_store_dword v196, %[vs], %[ds], %[oM] offen sc1\n\t"
                    "v_fmamk_f32 v194, v194, 0x3db8aa3b, v197\n\tv_fmamk_f32 v195, v195, 0x3db8aa3b, v197\n\t"
                    "v_exp_f32 v192, v192\n\tv_exp_f32 v193, v193\n\tv_exp_f32 v194, v194\n\tv_exp_f32 v195, v195\n\t"
                    "s_nop 0\n\t"
                    "v_cvt_pk_bf16_f32 v192, v192, v193\n\tv_cvt_pk_bf16_f32 v193, v194, v195\n\t"
                    "v_min_u32 v192, -2, v192\n\tv_min_u32 v193, -2, v193\n\t"
                    "s_nop 1\n\t"
                    "v_cndmask_b32_dpp v206, v192, v192, vcc" DPP8 "v_cndmask_b32_dpp v207, v193, v193, vcc" DPP8
                    "s_not_b64 vcc, vcc\n\t"
                    "v_cndmask_b32_dpp v204, v192, v192, vcc" DPP8 "v_cndmask_b32_dpp v205, v193, v193, vcc" DPP8
                    "buffer_store_dwordx4 v[204:207], %[vo], %[ds], %[oM] offen sc1\n\ts_nop 1"
                    : "+{v[192:195]}"(o)
                    : [red] "v"(redw), [hi] "s"(0xFF00FF00FF00FF00ull), [vo] "v"(vo_es), [vs] "v"(vo_es2), [ds] "s"(ds), [oM] "s"(oB)
                    : "memory", "vcc", "v196", "v197", "v200", "v201", "v202", "v203", "v204", "v205", "v206", "v207", "v208", "v209", "v210", "v211");
            }
            if (w == 1 && s < NSEG - 2) {
                const unsigned thx = lat_shift(tmx + O1T + lv, (int)(rt32() - tmx) - XB - (int)lv, DZ1);
                u32x4 va[11]; u32x2 vs2; unsigned vs;
                unsigned ti = thx;
                wait_fine(thx);
                int spins = 0; const int lim = dead ? 1 : SPIN_MAX; bool bad;
                _Pragma("nounroll") do {
#pragma unroll
                    for (int k = 0; k < 7; ++k) ALD4(va[k], vo_v + (unsigned)(PA_OFF + k * 1024), ds, oM);
#pragma unroll
                    for (int k = 0; k < 4; ++k) ALD4(va[7 + k], vo_vx + (unsigned)(k * 2048), ds, oM);
                    asm volatile("buffer_load_dwordx2 %0, %1, %2, %3 offen sc1" : "=v"(vs2) : "v"(vo_vs), "s"(ds), "s"(oM) : "memory");
                    asm volatile("s_waitcnt vmcnt(0)" : "+v"(va[0]), "+v"(va[1]), "+v"(va[2]), "+v"(va[3]), "+v"(va[4]), "+v"(va[5]), "+v"(va[6]), "+v"(va[7]), "+v"(va[8]), "+v"(va[9]), "+v"(va[10]), "+v"(vs2) :: "memory");
                    vs = max(vs2.x, vs2.y);
#pragma unroll
                    for (int k = 0; k < 11; ++k) vs = max(vs, max4(va[k]));
                    bad = __any(vs == POISON);
                    if (bad) ti = rt32();
                } while (bad && ++spins < lim);
                if (bad) dead = true;
                if (__builtin_expect(s == CK1 + 1, 0)) lv = clk_margin(ds, oM + CLK_OFF, dead);
                tmx = lat_shift(thx + C2H + lv, (int)(ti + (HOP1R + MINM) - (thx + C2H + lv)), DZ2);
                if (lane == 0) { lts[4 + ((s + 1) & 1)] = lv; lts[((s + 1) & 1) * 2 + 1] = tmx; lts[((s + 1) & 1) * 2] = (unsigned)(s + 1); }
            }
        }
        bar();
    } else {
        const int hw = w - 4;
        u32x4 gb[7];
#pragma unroll
        for (int k = 0; k < 7; ++k) gb[k] = (u32x4){0, 0, 0, 0};
        const unsigned vo_fg = (unsigned)(OFF_FLAG + (FLAG_GX + h) * 4);
        const unsigned vo_g = (unsigned)(OFF_GX + ((size_t)h * 28 + hw * 7) * FR + lane * 16);
        auto issue = [&](int sg) {
#pragma unroll
            for (int k = 0; k < 7; ++k) ALD4R(gb[k], vo_g + (unsigned)k * 1024u, ds, (unsigned)sg * (16u * 28u * 1024u));
        };
        auto put = [&](int sg) {
            asm volatile("s_waitcnt vmcnt(0)" : "+v"(gb[0]), "+v"(gb[1]), "+v"(gb[2]), "+v"(gb[3]), "+v"(gb[4]), "+v"(gb[5]), "+v"(gb[6]) :: "memory");
            SCHEDB();
#pragma unroll
            for (int k = 0; k < 7; ++k) *(u32x4*)(lds + L_GX + (sg & 1) * 28672 + (hw * 7 + k) * 1024 + lane * 16) = gb[k];
            SCHEDB();
        };
        {
            u32x4 gc[7];
            int spins = 0; bool bad;
            _Pragma("nounroll") do {
#pragma unroll
                for (int k = 0; k < 7; ++k) ALD4(gc[k], vo_g + (unsigned)k * 1024u, ds, 0u);
                issue(1);
                asm volatile("s_waitcnt vmcnt(0)" : "+v"(gc[0]), "+v"(gc[1]), "+v"(gc[2]), "+v"(gc[3]), "+v"(gc[4]), "+v"(gc[5]), "+v"(gc[6]),
                             "+v"(gb[0]), "+v"(gb[1]), "+v"(gb[2]), "+v"(gb[3]), "+v"(gb[4]), "+v"(gb[5]), "+v"(gb[6]) :: "memory");
                unsigned mx = 0;
#pragma unroll
                for (int k = 0; k < 7; ++k) mx = max(mx, max(max4(gc[k]), max4(gb[k])));
                bad = __any(mx == POISON);
                if (bad) __builtin_amdgcn_s_sleep(4);
            } while (bad && ++spins < SPIN_MAX);
            if (bad) dead = true;
#pragma unroll
            for (int k = 0; k < 7; ++k) *(u32x4*)(lds + L_GX + (hw * 7 + k) * 1024 + lane * 16) = gc[k];
            put(1);
        }
        (void)start_sync(p, ds, 112 + h, lds, tg);
        _Pragma("nounroll") for (int s = 0; s < NSEG; ++s) {
            __builtin_amdgcn_s_sleep(XHD);
            if (h == 0 && w == 7 && s >= 3 && lane == 0) AST1(1u, (unsigned)(OFF_FLAG + FLAG_PROG * 4), ds, (unsigned)(s - 3) * 128u);
            if (s > 0) put(s + 1);
            bar();
            {
                const unsigned tq = (unsigned)__builtin_amdgcn_readfirstlane(((volatile LAS unsigned*)((LAS char*)lds + L_TS))[(s & 1) * 2 + 1]);
                const unsigned lq = (unsigned)__builtin_amdgcn_readfirstlane(((volatile LAS unsigned*)((LAS char*)lds + L_TS))[4 + (s & 1)]) & 15u;
                wait_until(lat_shift(tq + O1T + lq, (int)(rt32() - tq) - XB - (int)lq, DZ1) + XHD2);
            }
            const int sn = (s + 2 < NSEG) ? s + 2 : NSEG - 1;
            await_flag(ds, vo_fg + (unsigned)sn * 64u, 2, dead);
            issue(__builtin_amdgcn_readfirstlane(sn));
        }
        AWAIT(0);
        bar();
    }
}

DEVI void wait_prog(const Params& p, int s, bool& dead) {
    const int w = __builtin_amdgcn_readfirstlane(threadIdx.x >> 6);
    if (s >= NSEG - NPZ) s -= 3;
    if (w == 0) {
        const unsigned t0 = __builtin_amdgcn_readfirstlane(ldflag(p, FLAG_T0));
        if (t0) {
            const unsigned target = t0 + (unsigned)(min(s + 2, NSEG - 1) * (O1T + C2H) + 60);
            int g = 0;
            _Pragma("nounroll") while ((int)(rt32() - target) < 0 && ++g < 20000) __builtin_amdgcn_s_sleep(8);
        }
        int k = dead ? SPIN_MAX : 0;
        _Pragma("nounroll") for (; k < SPIN_MAX; ++k) {
            if (__builtin_amdgcn_readfirstlane(ldflag(p, FLAG_PROG + s * 32)) != 0u) break;
            if (s <= NSEG - 6) __builtin_amdgcn_s_sleep(8); else __builtin_amdgcn_s_sleep(2);
        }
        if (k >= SPIN_MAX) dead = true;
    }
    __syncthreads();
}

#define FAHD 6
#define PSL 10
#define LASTX 2
DEVI void fin_unit(const Params& p, int s, int h0, int nh, char* lds, bool& dead) {
    const int tid = threadIdx.x, lane = tid & 63, w = __builtin_amdgcn_readfirstlane(tid >> 6);
    const bool act = w < nh;
    const int h = h0 + (act ? w : 0), half = h0 >> 3;
    const rsrc_t rs = mkrsrc(p.ws, 0x10000000u);
    __syncthreads();
    waitflag(p, FLAG_GX + s * 16 + h, 2, dead);
    u32x4 xs[4], g[28];
#pragma unroll
    for (int j = 0; j < 4; ++j) { const int f = (w + 8 * j < 28) ? w + 8 * j : w; xs[j] = bld4(rs, (unsigned)(OFF_XF + ((size_t)s * 28 + f) * FR + lane * 16), 0); }
#pragma unroll
    for (int f = 0; f < 28; ++f) g[f] = bld4(rs, act ? (unsigned)(OFF_GX + (((size_t)s * 16 + h) * 28 + f) * FR + lane * 16) : OOB, 0);
#pragma unroll
    for (int j = 0; j < 4; ++j) { const int f = w + 8 * j; if (f < 28) *(u32x4*)(lds + f * 1024 + lane * 16) = xs[j]; }
    __syncthreads();
    f32x4 xx[3] = {{0,0,0,0},{0,0,0,0},{0,0,0,0}};
    if (act) {
#pragma unroll
        for (int t = 0; t < 14; ++t) {
            const u32x4 x0 = *(const u32x4*)(lds + t * 1024 + lane * 16), x1 = *(const u32x4*)(lds + (14 + t) * 1024 + lane * 16);
            xx[0] = mfma32(g[t], x0, xx[0]);
            xx[1] = mfma32(g[t], x1, xx[1]);
            xx[2] = mfma32(g[14 + t], x1, xx[2]);
        }
    }
    const XSoft xs0 = xs_pre(xx[0], xx[0], 0, lane), xs1 = xs_pre(xx[1], xx[2], 1, lane);
    wait_prog(p, s, dead);
    if (!act) return;
    f32x4 sm;
    u32x4 fa0, fa1, fa2, fa3, fa4, fa5, fa6, fb0, fb1, fb2, fb3, fb4, fb5, fb6;
    {
        const unsigned vo = (unsigned)(OFF_XM + (((size_t)s * 16 + h) * 7) * 1024 + lane * 16);
        const bool pz = s >= NSEG - NPZ;
        if (pz && s != NSEG - 1) {
            int spins = dead ? SPIN_MAX : 0;
            _Pragma("nounroll") for (; spins < SPIN_MAX; ++spins) {
                const unsigned v = bld1(rs, lane < 7 ? vo - (unsigned)lane * 16u + (unsigned)lane * 1024u + 1020u : OOB, 0);
                if (!__any(v == POISON)) break;
                MEMFENCE();
            }
        }
        const u32x4 ds = mkdesc(p.ws, 0x10000000u);
        unsigned lim = (unsigned)__builtin_amdgcn_readfirstlane(!pz ? 0 : (dead ? 1 : SPIN_MAX));
#define XISSUE(B) "buffer_load_dwordx4 v[" #B "+0:" #B "+3], %[vx], %[ds], 0 offen sc1\n\tbuffer_load_dwordx4 v[" #B "+4:" #B "+7], %[vx], %[ds], 0 offen offset:1024 sc1\n\t" \
            "buffer_load_dwordx4 v[" #B "+8:" #B "+11], %[vx], %[ds], 0 offen offset:2048 sc1\n\tbuffer_load_dwordx4 v[" #B "+12:" #B "+15], %[vx], %[ds], 0 offen offset:3072 sc1\n\t" \
            "buffer_load_dwordx4 v[" #B "+16:" #B "+19], %[vy], %[ds], 0 offen sc1\n\tbuffer_load_dwordx4 v[" #B "+20:" #B "+23], %[vy], %[ds], 0 offen offset:1024 sc1\n\t" \
            "buffer_load_dwordx4 v[" #B "+24:" #B "+27], %[vy], %[ds], 0 offen offset:2048 sc1\n\t"
#define XM3(D, B, A) "v_max3_u32 " #D ", v[" #B "+" #A "], v[" #B "+" #A "+1], v[" #B "+" #A "+2]\n\t"
#define XM3A(D, B, A) "v_max3_u32 " #D ", " #D ", v[" #B "+" #A "], v[" #B "+" #A "+1]\n\t"
#define XCHECK(B) XM3(v240, B, 0) XM3(v241, B, 3) XM3(v242, B, 6) XM3(v243, B, 9) XM3A(v240, B, 12) XM3A(v241, B, 14) XM3A(v242, B, 16) XM3A(v243, B, 18) \
            XM3A(v240, B, 20) XM3A(v241, B, 22) XM3A(v242, B, 24) XM3A(v243, B, 26) "v_max3_u32 v240, v240, v241, v242\n\tv_max_u32 v240, v240, v243\n\tv_cmp_eq_u32 vcc, -1, v240\n\t"
#define XADD(D, B, A, C) "v_pk_add_f32 " D ", v[" #B "+" #A ":" #B "+" #A "+1], v[" #B "+" #C ":" #B "+" #C "+1]\n\t"
#define XSUM(B) XADD("v[240:241]", B, 0, 4) XADD("v[242:243]", B, 2, 6) XADD("v[244:245]", B, 8, 12) XADD("v[246:247]", B, 10, 14) XADD("v[248:249]", B, 16, 20) XADD("v[250:251]", B, 18, 22) \
            "v_pk_add_f32 v[240:241], v[240:241], v[244:245]\n\tv_pk_add_f32 v[242:243], v[242:243], v[246:247]\n\t" \
            "v_pk_add_f32 v[248:249], v[248:249], v[" #B "+24:" #B "+25]\n\tv_pk_add_f32 v[250:251], v[250:251], v[" #B "+26:" #B "+27]\n\t" \
            "v_pk_add_f32 %[o0], v[240:241], v[248:249]\n\tv_pk_add_f32 %[o1], v[242:243], v[250:251]\n\t"
        f32x2 o0, o1;
        unsigned long long bad64;
        WAITVM0();
        asm volatile(
            XISSUE(184)
            "s_mov_b64 %[bad], 0\n\t"
            "s_cmp_eq_u32 %[lim], 0\n\t"
            "s_cbranch_scc1 .LXZ0%=\n\t"
            "s_sleep %[sl]\n"
            ".LXL%=:\n\t"
            XISSUE(212)
            "s_waitcnt vmcnt(7)\n\t"
            XCHECK(184)
            "s_cbranch_vccz .LXA%=\n\t"
            "s_sub_u32 %[lim], %[lim], 1\n\t"
            "s_cbranch_scc1 .LXX%=\n\t"
            "s_cmp_eq_u32 %[lim], 0\n\t"
            "s_cbranch_scc1 .LXX%=\n\t"
            XISSUE(184)
            "s_waitcnt vmcnt(7)\n\t"
            XCHECK(212)
            "s_cbranch_vccnz .LXL%=\n\t"
            XSUM(212)
            "s_branch .LXE%=\n"
            ".LXX%=:\n\t"
            "s_mov_b64 %[bad], -1\n\t"
            "s_branch .LXA%=\n"
            ".LXZ0%=:\n\t"
            "s_waitcnt vmcnt(0)\n"
            ".LXA%=:\n\t"
            XSUM(184)
            ".LXE%=:\n\t"
            "s_nop 1"
            : [o0] "=&v"(o0), [o1] "=&v"(o1), [bad] "=&s"(bad64), [lim] "+s"(lim),
              "=&{v[184:187]}"(fa0), "=&{v[188:191]}"(fa1), "=&{v[192:195]}"(fa2), "=&{v[196:199]}"(fa3), "=&{v[200:203]}"(fa4), "=&{v[204:207]}"(fa5), "=&{v[208:211]}"(fa6),
              "=&{v[212:215]}"(fb0), "=&{v[216:219]}"(fb1), "=&{v[220:223]}"(fb2), "=&{v[224:227]}"(fb3), "=&{v[228:231]}"(fb4), "=&{v[232:235]}"(fb5), "=&{v[236:239]}"(fb6)
            : [vx] "v"(vo), [vy] "v"(vo + 4096u), [ds] "s"(ds), [sl] "n"(PSL)
            : "memory", "vcc", "scc", "v240", "v241", "v242", "v243", "v244", "v245", "v246", "v247", "v248", "v249", "v250", "v251");
        if (bad64 != 0ull) dead = true;
        sm = (f32x4){o0.x, o0.y, o1.x, o1.y};
    }
    f32x4 sm1;
#pragma unroll
    for (int rr = 0; rr < 4; ++rr) { float lo, hi; swap32(sm[rr], lo, hi); sm[rr] = lo; sm1[rr] = hi; }
    xs_post(rs, xs0, sm, s, h, 0, lane);
    xs_post(rs, xs1, sm1, s, h, 1, lane);
    asm volatile("s_waitcnt vmcnt(0)" : "+{v[184:187]}"(fa0), "+{v[188:191]}"(fa1), "+{v[192:195]}"(fa2), "+{v[196:199]}"(fa3), "+{v[200:203]}"(fa4), "+{v[204:207]}"(fa5), "+{v[208:211]}"(fa6),
                 "+{v[212:215]}"(fb0), "+{v[216:219]}"(fb1), "+{v[220:223]}"(fb2), "+{v[224:227]}"(fb3), "+{v[228:231]}"(fb4), "+{v[232:235]}"(fb5), "+{v[236:239]}"(fb6) :: "memory");
    if (s < NSEG - NPZ) {
        WAITVM0();
        __syncthreads();
        if (tid == 0) bst1(rs, 1u, (unsigned)(OFF_FLAG + (FLAG_P + s * 32 + half) * 4), 0);
    }
}

DEVI bool out_unit(const Params& p, int s, int bg, int itsel, char* lds, bool& dead) {
    const int tid = threadIdx.x, lane = tid & 63, w = __builtin_amdgcn_readfirstlane(tid >> 6);
    const int col = lane & 15, q4 = lane >> 4;
    const rsrc_t rs = mkrsrc(p.ws, 0x10000000u);
    const bool act = itsel < 0 || w < 4;
    const int it = itsel < 0 ? (w & 1) : itsel, hq = itsel < 0 ? (w >> 1) : (w & 3);
    constexpr int O_RED = 0, O_W = 40960;
    waitflag(p, FLAG_U + s * 16 + (8 * bg) / 14, 8, dead);
    waitflag(p, FLAG_U + s * 16 + (8 * bg + 7) / 14, 8, dead);
    u32x4 ua[4][2][2];
#pragma unroll
    for (int jp = 0; jp < 4; ++jp)
#pragma unroll
        for (int i = 0; i < 2; ++i) {
            const u32x4 u0 = bld4(rs, act ? (unsigned)(OFF_UF + (((size_t)s * 56 + 8 * bg + 2 * jp) * 8 + 2 * hq + i) * FR + lane * 16) : OOB, 0);
            const u32x4 u1 = bld4(rs, act ? (unsigned)(OFF_UF + (((size_t)s * 56 + 8 * bg + 2 * jp + 1) * 8 + 2 * hq + i) * FR + lane * 16) : OOB, 0);
#pragma unroll
            for (int k = 0; k < 4; ++k) {
                const unsigned r0 = dppu<DPP_ROR8>(u0[k]), r1 = dppu<DPP_ROR8>(u1[k]);
                ua[jp][i][0][k] = col < 8 ? u0[k] : r1;
                ua[jp][i][1][k] = col < 8 ? r0 : u1[k];
            }
        }
#pragma unroll
    for (int jp = 0; jp < 4; ++jp) asm volatile("" : "+v"(ua[jp][0][0]), "+v"(ua[jp][0][1]), "+v"(ua[jp][1][0]), "+v"(ua[jp][1][1]));
    const unsigned tw0 = rt32();
    wait_prog(p, s, dead);
    const bool waited = (int)(rt32() - tw0) > 300;
    const bool pz = s >= NSEG - NPZ;
    {
        u32x4 wl[2];
        int spins = dead ? SPIN_MAX : 0; bool bad;
        _Pragma("nounroll") do {
#pragma unroll
            for (int j = 0; j < 2; ++j) wl[j] = bld4(rs, (unsigned)(OFF_WC + ((size_t)s * 56 + 8 * bg) * 2048 + (2 * w + j) * 1024 + lane * 16), 0);
            bad = pz && __any(max(max4(wl[0]), max4(wl[1])) == POISON);
            MEMFENCE();
        } while (bad && ++spins < SPIN_MAX);
        if (bad) dead = true;
#pragma unroll
        for (int j = 0; j < 2; ++j) *(u32x4*)(lds + O_W + (2 * w + j) * 1024 + lane * 16) = wl[j];
    }
    __syncthreads();
    u32x2 t[4][2];
#pragma unroll
    for (int jp = 0; jp < 4; ++jp)
#pragma unroll
        for (int i = 0; i < 2; ++i) t[jp][i] = *(const u32x2*)(lds + O_W + ((2 * jp + (col >> 3)) * 8 + 2 * hq + i) * 256 + ((q4 >> 1) * 8 + (col & 7)) * 16 + (q4 & 1) * 8);
#pragma unroll
    for (int jp = 0; jp < 4; ++jp) asm volatile("" : "+v"(t[jp][0]), "+v"(t[jp][1]));
    if (!pz) waitall(p, FLAG_P + s * 32, 2, dead);
    u32x4 fa0, fa1, fa2, fa3, fa4, fb0, fb1, fb2, fb3, fb4;
    if (act) {
    u32x4 pl[4]; u32x2 pm[2];
    {
        const u32x4 ds = mkdesc(p.ws, 0x10000000u);
        const unsigned vx = (unsigned)(OFF_PX + (((size_t)s * 16 + 4 * hq) * 2 + it) * FR + lane * 16), vm = (unsigned)(OFF_PM + (((size_t)s * 8 + 2 * hq) * 2 + it) * 512 + lane * 8);
        unsigned lim = (unsigned)__builtin_amdgcn_readfirstlane(!pz ? 0 : (dead ? 1 : SPIN_MAX));
#define PISSUE(B) "buffer_load_dwordx4 v[" #B "+0:" #B "+3], %[vx], %[ds], 0 offen sc1\n\tbuffer_load_dwordx4 v[" #B "+4:" #B "+7], %[vx], %[ds], 0 offen offset:2048 sc1\n\t" \
            "buffer_load_dwordx4 v[" #B "+8:" #B "+11], %[vy], %[ds], 0 offen sc1\n\tbuffer_load_dwordx4 v[" #B "+12:" #B "+15], %[vy], %[ds], 0 offen offset:2048 sc1\n\t" \
            "buffer_load_dwordx2 v[" #B "+16:" #B "+17], %[vm], %[ds], 0 offen sc1\n\tbuffer_load_dwordx2 v[" #B "+18:" #B "+19], %[vm], %[ds], 0 offen offset:1024 sc1\n\t"
#define PCHECK(B) "v_max3_u32 v240, v[" #B "+0], v[" #B "+1], v[" #B "+2]\n\tv_max3_u32 v241, v[" #B "+3], v[" #B "+4], v[" #B "+5]\n\tv_max3_u32 v242, v[" #B "+6], v[" #B "+7], v[" #B "+8]\n\t" \
            "v_max3_u32 v240, v240, v[" #B "+9], v[" #B "+10]\n\tv_max3_u32 v241, v241, v[" #B "+11], v[" #B "+12]\n\tv_max3_u32 v242, v242, v[" #B "+13], v[" #B "+14]\n\t" \
            "v_max3_u32 v240, v240, v[" #B "+15], v[" #B "+16]\n\tv_max3_u32 v241, v241, v[" #B "+17], v[" #B "+18]\n\tv_max3_u32 v242, v242, v[" #B "+19], v240\n\t" \
            "v_max_u32 v241, v241, v242\n\tv_cmp_eq_u32 vcc, -1, v241\n\t"
#define PCOPY(B) "v_mov_b64 %[o0], v[" #B "+0:" #B "+1]\n\tv_mov_b64 %[o1], v[" #B "+2:" #B "+3]\n\tv_mov_b64 %[o2], v[" #B "+4:" #B "+5]\n\tv_mov_b64 %[o3], v[" #B "+6:" #B "+7]\n\t" \
            "v_mov_b64 %[o4], v[" #B "+8:" #B "+9]\n\tv_mov_b64 %[o5], v[" #B "+10:" #B "+11]\n\tv_mov_b64 %[o6], v[" #B "+12:" #B "+13]\n\tv_mov_b64 %[o7], v[" #B "+14:" #B "+15]\n\t" \
            "v_mov_b64 %[o8], v[" #B "+16:" #B "+17]\n\tv_mov_b64 %[o9], v[" #B "+18:" #B "+19]\n\t"
        u32x2 o[10];
        unsigned long long bad64;
        WAITVM0();
        asm volatile(
            PISSUE(200)
            "s_mov_b64 %[bad], 0\n\t"
            "s_cmp_eq_u32 %[lim], 0\n\t"
            "s_cbranch_scc1 .LPZ0%=\n\t"
            "s_sleep %[sl]\n"
            ".LPL%=:\n\t"
            PISSUE(220)
            "s_waitcnt vmcnt(6)\n\t"
            PCHECK(200)
            "s_cbranch_vccz .LPA%=\n\t"
            "s_sub_u32 %[lim], %[lim], 1\n\t"
            "s_cbranch_scc1 .LPX%=\n\t"
            "s_cmp_eq_u32 %[lim], 0\n\t"
            "s_cbranch_scc1 .LPX%=\n\t"
            PISSUE(200)
            "s_waitcnt vmcnt(6)\n\t"
            PCHECK(220)
            "s_cbranch_vccnz .LPL%=\n\t"
            PCOPY(220)
            "s_branch .LPE%=\n"
            ".LPX%=:\n\t"
            "s_mov_b64 %[bad], -1\n\t"
            "s_branch .LPA%=\n"
            ".LPZ0%=:\n\t"
            "s_waitcnt vmcnt(0)\n"
            ".LPA%=:\n\t"
            PCOPY(200)
            ".LPE%=:\n\t"
            "s_nop 1"
            : [o0] "=&v"(o[0]), [o1] "=&v"(o[1]), [o2] "=&v"(o[2]), [o3] "=&v"(o[3]), [o4] "=&v"(o[4]), [o5] "=&v"(o[5]), [o6] "=&v"(o[6]), [o7] "=&v"(o[7]), [o8] "=&v"(o[8]), [o9] "=&v"(o[9]),
              [bad] "=&s"(bad64), [lim] "+s"(lim),
              "=&{v[200:203]}"(fa0), "=&{v[204:207]}"(fa1), "=&{v[208:211]}"(fa2), "=&{v[212:215]}"(fa3), "=&{v[216:219]}"(fa4),
              "=&{v[220:223]}"(fb0), "=&{v[224:227]}"(fb1), "=&{v[228:231]}"(fb2), "=&{v[232:235]}"(fb3), "=&{v[236:239]}"(fb4)
            : [vx] "v"(vx), [vy] "v"(vx + 4096u), [vm] "v"(vm), [ds] "s"(ds), [sl] "n"(PSL)
            : "memory", "vcc", "scc", "v240", "v241", "v242");
        if (bad64 != 0ull) dead = true;
#pragma unroll
        for (int j = 0; j < 4; ++j) pl[j] = (u32x4){o[2 * j].x, o[2 * j].y, o[2 * j + 1].x, o[2 * j + 1].y};
        pm[0] = o[8]; pm[1] = o[9];
    }
    f32x4 acc[4];
#pragma unroll
    for (int jp = 0; jp < 4; ++jp) acc[jp] = mfma32(ua[jp][0][0], pl[0], (f32x4){0, 0, 0, 0});
#pragma unroll
    for (int jp = 0; jp < 4; ++jp) acc[jp] = mfma32(ua[jp][0][1], pl[1], acc[jp]);
#pragma unroll
    for (int jp = 0; jp < 4; ++jp) acc[jp] = mfma32(ua[jp][1][0], pl[2], acc[jp]);
#pragma unroll
    for (int jp = 0; jp < 4; ++jp) acc[jp] = mfma32(ua[jp][1][1], pl[3], acc[jp]);
#pragma unroll
    for (int i = 0; i < 2; ++i)
#pragma unroll
        for (int jp = 0; jp < 4; ++jp) acc[jp] = mfma16(t[jp][i], pm[i], acc[jp]);
    LAS char* red = (LAS char*)lds + O_RED + ((2 * hq + it) * 16 + col) * 288 + q4 * 16;
#pragma unroll
    for (int jp = 0; jp < 4; ++jp) *(LAS f32x4*)(red + jp * 64) = acc[jp];
    }
    bar();
    if (act) {
        const int ot = itsel < 0 ? tid >> 8 : itsel, row = (tid >> 4) & 15, c = tid & 15;
        const LAS char* rb = (const LAS char*)lds + O_RED + (ot * 16 + row) * 288 + c * 16;
        const f32x4 a = *(const LAS f32x4*)(rb), b = *(const LAS f32x4*)(rb + 2 * 16 * 288), c2 = *(const LAS f32x4*)(rb + 4 * 16 * 288), d = *(const LAS f32x4*)(rb + 6 * 16 * 288);
        const f32x4 v = (a + b) + (c2 + d);
        const u32x2 o = {pk2(v[0], v[1]), pk2(v[2], v[3])};
        *(u32x2*)(p.out + (size_t)(32 * s + 16 * ot + row) * OUTW + 64 * bg + 4 * c) = o;
    }
    asm volatile("s_waitcnt vmcnt(0)" : "+{v[200:203]}"(fa0), "+{v[204:207]}"(fa1), "+{v[208:211]}"(fa2), "+{v[212:215]}"(fa3), "+{v[216:219]}"(fa4),
                 "+{v[220:223]}"(fb0), "+{v[224:227]}"(fb1), "+{v[228:231]}"(fb2), "+{v[232:235]}"(fb3), "+{v[236:239]}"(fb4) :: "memory");
    return waited && !pz;
}

DEVI float phi(float t) { return (t > 0.f ? t + 1.f : __builtin_amdgcn_exp2f(t * 1.4426950408889634f)) + 1e-6f; }
DEVI float rbf(float x) { return bf_lo(pk2(x, 0.f)); }

DEVI void b1_unit(const Params& p, int rb, int hp, char* lds, bool& dead) {
    waitflag(p, FLAG_BT, 128, dead);
    const int tid = threadIdx.x, lane = tid & 63, w = __builtin_amdgcn_readfirstlane(tid >> 6);
    const int col = lane & 15, q4 = lane >> 4;
    const rsrc_t rs = mkrsrc(p.ws, 0x10000000u);
    const int h = 2 * hp + (w & 1), rt0 = rb * 8 + (w >> 1) * 2;
    u32x4 xr[2][14], stg[11];
    auto wload = [&](int c) {
#pragma unroll
        for (int j = 0; j < 11; ++j) {
            const int i = (w + 8 * j < 84) ? w + 8 * j : w, tt = i % 7, g = i / 7, tile = g & 1, mat = (g >> 1) % 3, hh = g / 6;
            stg[j] = bld4(rs, (unsigned)(OFF_BW + ((size_t)((mat * 16 + 2 * hp + hh) * 2 + tile) * 14 + 7 * c + tt) * FR + lane * 16), 0);
        }
    };
    auto wput = [&]() {
#pragma unroll
        for (int j = 0; j < 11; ++j) if (w + 8 * j < 84) *(u32x4*)(lds + (w + 8 * j) * 1024 + lane * 16) = stg[j];
    };
    wload(0);
#pragma unroll
    for (int a = 0; a < 2; ++a)
#pragma unroll
        for (int t = 0; t < 14; ++t) xr[a][t] = bld4(rs, (unsigned)(OFF_BX + ((size_t)(rt0 + a) * 14 + t) * FR + lane * 16), 0);
    f32x4 aq[2][2], ak[2][2], av[2][2];
#pragma unroll
    for (int a = 0; a < 2; ++a)
#pragma unroll
        for (int c = 0; c < 2; ++c) { aq[a][c] = (f32x4){0,0,0,0}; ak[a][c] = (f32x4){0,0,0,0}; av[a][c] = (f32x4){0,0,0,0}; }
    __syncthreads();
    wput();
    wload(1);
    __syncthreads();
#pragma unroll
    for (int c2 = 0; c2 < 2; ++c2) {
#pragma unroll
        for (int tt = 0; tt < 7; ++tt) {
            const char* wb = lds + ((w & 1) * 42 + tt) * 1024 + lane * 16;
            u32x4 wq[2], wk[2], wv[2];
#pragma unroll
            for (int c = 0; c < 2; ++c) { wq[c] = *(const u32x4*)(wb + (0 + c) * 7168); wk[c] = *(const u32x4*)(wb + (2 + c) * 7168); wv[c] = *(const u32x4*)(wb + (4 + c) * 7168); }
#pragma unroll
            for (int a = 0; a < 2; ++a)
#pragma unroll
                for (int c = 0; c < 2; ++c) {
                    aq[a][c] = mfma32(wq[c], xr[a][7 * c2 + tt], aq[a][c]);
                    ak[a][c] = mfma32(xr[a][7 * c2 + tt], wk[c], ak[a][c]);
                    av[a][c] = mfma32(xr[a][7 * c2 + tt], wv[c], av[a][c]);
                }
        }
        if (c2 == 0) { __syncthreads(); wput(); __syncthreads(); }
    }
    __syncthreads();
#pragma unroll
    for (int a = 0; a < 2; ++a) {
        f32x4 p0, p1;
#pragma unroll
        for (int rr = 0; rr < 4; ++rr) {
            p0[rr] = phi(rbf(aq[a][0][rr]));
            p1[rr] = (8 * q4 + 4 + rr < 28) ? phi(rbf(aq[a][1][rr])) : 0.f;
        }
        bst4(rs, pack8(p0, p1), (unsigned)(OFF_BQ + ((size_t)(rt0 + a) * 16 + h) * FR + lane * 16), 0);
    }
    u32x4 pk[2], vv[2];
#pragma unroll
    for (int c = 0; c < 2; ++c) {
        f32x4 k0, k1, v0, v1;
        const int d = 16 * c + col;
#pragma unroll
        for (int rr = 0; rr < 4; ++rr) {
            k0[rr] = (d < 28) ? phi(rbf(ak[0][c][rr])) : 0.f; k1[rr] = (d < 28) ? phi(rbf(ak[1][c][rr])) : 0.f;
            v0[rr] = (d == 28) ? 1.f : av[0][c][rr]; v1[rr] = (d == 28) ? 1.f : av[1][c][rr];
        }
        pk[c] = pack8(k0, k1); vv[c] = pack8(v0, v1);
    }
    float* red = (float*)lds;
#pragma unroll
    for (int dc = 0; dc < 2; ++dc)
#pragma unroll
        for (int ec = 0; ec < 2; ++ec) {
            const f32x4 d = mfma32(pk[dc], vv[ec], (f32x4){0,0,0,0});
            *(f32x4*)(red + ((w * 4 + dc * 2 + ec) * 64 + lane) * 4) = d;
        }
    bar();
    {
        const int hh = w & 1, tile = w >> 1;
        f32x4 t4 = {0,0,0,0};
#pragma unroll
        for (int g = 0; g < 4; ++g) t4 += *(const f32x4*)(red + (((g * 2 + hh) * 4 + tile) * 64 + lane) * 4);
        bst4(rs, __builtin_bit_cast(u32x4, t4), (unsigned)(OFF_BKV + (((size_t)rb * 16 + 2 * hp + hh) * 4 + tile) * 1024 + lane * 16), 0);
    }
    WAITVM0();
    bar();
    if (tid == 0) signal(p, FLAG_B1);
}

DEVI void b2_unit(const Params& p, int h, char* lds, bool& dead) {
    const int tid = threadIdx.x, lane = tid & 63, w = __builtin_amdgcn_readfirstlane(tid >> 6);
    const int col = lane & 15, q4 = lane >> 4;
    const rsrc_t rs = mkrsrc(p.ws, 0x10000000u);
    float* kvs = (float*)lds;
    waitflag(p, FLAG_B1, 128, dead);
    if (w < 4) {
        f32x4 t4 = {0,0,0,0};
#pragma unroll
        for (int rb = 0; rb < 16; ++rb) t4 += __builtin_bit_cast(f32x4, bld4(rs, (unsigned)(OFF_BKV + (((size_t)rb * 16 + h) * 4 + w) * 1024 + lane * 16), 0));
        const int dc = w >> 1, ec = w & 1;
#pragma unroll
        for (int rr = 0; rr < 4; ++rr) kvs[(16 * dc + 4 * q4 + rr) * 33 + 16 * ec + col] = t4[rr];
    }
    bar();
    if (tid < 32) bst1(rs, __float_as_uint(kvs[tid * 33 + 28]), (unsigned)(OFF_BZ + (h * 32 + tid) * 4), 0);
    u32x4 ka[2];
#pragma unroll
    for (int bb = 0; bb < 2; ++bb) {
        const int d = 8 * (col >> 2) + 4 * bb + (col & 3);
        float e[8];
#pragma unroll
        for (int j = 0; j < 8; ++j) e[j] = (8 * q4 + j < 28) ? kvs[d * 33 + 8 * q4 + j] : 0.f;
        ka[bb] = (u32x4){pk2(e[0], e[1]), pk2(e[2], e[3]), pk2(e[4], e[5]), pk2(e[6], e[7])};
    }
    for (int ct = w; ct < 28; ct += 8) {
        u16 e[8];
#pragma unroll
        for (int j = 0; j < 8; ++j) e[j] = (8 * q4 + j < 28) ? p.bwo[(size_t)(28 * h + 8 * q4 + j) * DM + 32 * (ct >> 1) + 8 * (col >> 2) + 4 * (ct & 1) + (col & 3)] : (u16)0;
        const u32x4 wf = {e[0] | ((unsigned)e[1] << 16), e[2] | ((unsigned)e[3] << 16), e[4] | ((unsigned)e[5] << 16), e[6] | ((unsigned)e[7] << 16)};
        const f32x4 d0 = mfma32(ka[0], wf, (f32x4){0,0,0,0}), d1 = mfma32(ka[1], wf, (f32x4){0,0,0,0});
        bst4(rs, pack8(d0, d1), (unsigned)(OFF_BM + ((size_t)h * 28 + ct) * FR + lane * 16), 0);
    }
    WAITVM0();
    bar();
    if (tid == 0) signal(p, FLAG_B2);
}

DEVI void b3_unit(const Params& p, int u, char* lds, bool& dead) {
    const int tid = threadIdx.x, lane = tid & 63, w = __builtin_amdgcn_readfirstlane(tid >> 6);
    const int col = lane & 15, q4 = lane >> 4;
    const rsrc_t rs = mkrsrc(p.ws, 0x10000000u);
    waitflag(p, FLAG_B2, 16, dead);
    __syncthreads();
#pragma unroll
    for (int k = 0; k < 4; ++k) {
        const int h = 2 * w + (k >> 1), rt = 2 * u + (k & 1);
        const u32x4 pq = bld4(rs, (unsigned)(OFF_BQ + ((size_t)rt * 16 + h) * FR + lane * 16), 0);
        const f32x4 z0 = __builtin_bit_cast(f32x4, bld4(rs, (unsigned)(OFF_BZ + (h * 32 + 8 * q4) * 4), 0)), z1 = __builtin_bit_cast(f32x4, bld4(rs, (unsigned)(OFF_BZ + (h * 32 + 8 * q4 + 4) * 4), 0));
        float f[8] = {bf_lo(pq.x), bf_hi(pq.x), bf_lo(pq.y), bf_hi(pq.y), bf_lo(pq.z), bf_hi(pq.z), bf_lo(pq.w), bf_hi(pq.w)};
        float den = f[0] * z0.x + f[1] * z0.y + f[2] * z0.z + f[3] * z0.w + f[4] * z1.x + f[5] * z1.y + f[6] * z1.z + f[7] * z1.w;
        { float a, b2; swap16(den, a, b2); den = a + b2; swap32(den, a, b2); den = a + b2; }
        const float inv = 1.0f / (den + 1e-6f);
        const u32x4 pn = {pk2(f[0] * inv, f[1] * inv), pk2(f[2] * inv, f[3] * inv), pk2(f[4] * inv, f[5] * inv), pk2(f[6] * inv, f[7] * inv)};
        *(u32x4*)(lds + ((k & 1) * 16 + h) * 1024 + lane * 16) = pn;
    }
    u32x4 bm[2][16];
    auto bload = [&](int pr) {
#pragma unroll
        for (int a = 0; a < 2; ++a)
#pragma unroll
            for (int h = 0; h < 16; ++h) bm[a][h] = bld4(rs, (unsigned)(OFF_BM + ((size_t)h * 28 + 2 * pr + a) * FR + lane * 16), 0);
    };
    if (w < 7) bload(2 * w);
    __syncthreads();
    if (w < 7) {
#pragma unroll
        for (int jp = 0; jp < 2; ++jp) {
            const int pr = 2 * w + jp;
            f32x4 acc[2][2] = {{{0,0,0,0},{0,0,0,0}},{{0,0,0,0},{0,0,0,0}}};
#pragma unroll
            for (int h = 0; h < 16; ++h) {
                const u32x4 p0 = *(const u32x4*)(lds + h * 1024 + lane * 16), p1 = *(const u32x4*)(lds + (16 + h) * 1024 + lane * 16);
#pragma unroll
                for (int a = 0; a < 2; ++a) { acc[a][0] = mfma32(bm[a][h], p0, acc[a][0]); acc[a][1] = mfma32(bm[a][h], p1, acc[a][1]); }
            }
            if (jp == 0) bload(2 * w + 1);
#pragma unroll
            for (int rt = 0; rt < 2; ++rt)
            {
                *(u32x4*)(p.out + (size_t)(32 * u + 16 * rt + col) * OUTW + 448 + 32 * pr + 8 * q4) = pack8(acc[0][rt], acc[1][rt]);
            }
        }
    }
}

DEVI void worker(const Params& p, int u, char* lds, Tag tg) {
    bool dead = false;
    gemm_worker(p, u, lds, tg);
    b1_unit(p, u >> 3, u & 7, lds, dead);
    if (u < 16) b2_unit(p, u, lds, dead);
    if (u >= 64) b3_unit(p, u - 64, lds, dead);
    _Pragma("nounroll") for (int j = u; ; j += 128) {
        constexpr int NR = NSEG - LASTX;
        int s, k;
        if (j < 2 * FAHD) { s = j >> 1; k = j & 1; }
        else {
            const int q = j - 2 * FAHD;
            if (q < 9 * (NR - FAHD)) { s = q / 9; k = q % 9; if (k < 7) k += 2; else { k -= 7; s += FAHD; } }
            else { const int q2 = q - 9 * (NR - FAHD); s = NR - FAHD + q2 / 7; k = 2 + q2 % 7; }
        }
        int h0 = 8 * k, nh = 8, bg = k - 2, itsel = -1;
        const bool last = j >= 9 * (NSEG - LASTX);
        if (last) {
            if (u < 64 || u >= 64 + 22 * LASTX) break;
            const int e = (u - 64) / 22, v = (u - 64) % 22;
            s = NSEG - 1 - e; k = v < 8 ? 0 : 2; h0 = 2 * v; nh = 2; bg = (v - 8) >> 1; itsel = (v - 8) & 1;
        }
        if (PACEA > 0 && !last) {
            const unsigned t0 = __builtin_amdgcn_readfirstlane(ldflag(p, FLAG_T0));
            if (t0) {
                const unsigned target = t0 + (unsigned)(((PACE0 * 1024 + j * PACEA) >> 10) * (O1T + C2H)) + (unsigned)((((j * PACEA) & 1023) * (O1T + C2H)) >> 10);
                int g = 0;
                _Pragma("nounroll") while ((int)(rt32() - target) < 0 && ++g < 20000) __builtin_amdgcn_s_sleep(16);
            }
        }
        bool instep = false;
        if (k < 2) fin_unit(p, s, h0, nh, lds, dead); else instep = out_unit(p, s, bg, itsel, lds, dead);
        if (last) break;
    }
}

__global__ void __launch_bounds__(512, 2) k_main(Params p) {
    extern __shared__ __attribute__((aligned(16))) char lds[];
    const int b = blockIdx.x;
    const unsigned gen = __builtin_amdgcn_raw_buffer_load_b32(mkrsrc(p.ws, 0x10000000u), (unsigned)p.comm_off + GEN_OFF, 0, 16);
    prep(p, lds, gen);
    const Tag tg = make_tag(gen);
    const int idx = (b >> 3) * 4 + (b & 3);
    if ((b & 7) < 4) {
        if (PBAR) bar();
        const int x = b & 3, k = b >> 3;
        if (k < 28) chain_g(p, 2 * (4 * (k >> 1) + x) + (k & 1), lds, tg); else chain_x(p, 4 * (k - 28) + x, lds, tg);
    } else {
        worker(p, idx, lds, tg);
    }
}

extern "C" void kernel_launch(void* const* d_in, const int* in_sizes, int n_in, void* d_out, int out_size, void* d_ws, size_t ws_size, hipStream_t stream) {
    Params p;
    p.x = (const u16*)d_in[0]; p.mem0 = (const u16*)d_in[1]; p.Wq = (const u16*)d_in[2]; p.Wk = (const u16*)d_in[3];
    p.Wv = (const u16*)d_in[4]; p.Wo = (const u16*)d_in[5]; p.bx = (const u16*)d_in[7]; p.wqkv = (const u16*)d_in[8]; p.bwo = (const u16*)d_in[9];
    p.out = (u16*)d_out; p.ws = (char*)d_ws;
    const uintptr_t base = (uintptr_t)p.ws;
    const uintptr_t al = (base + OFF_COMM_RAW + ((2u << 20) - 1)) & ~(uintptr_t)((2u << 20) - 1);
    p.comm_off = (unsigned long long)(al - base);
    (void)hipFuncSetAttribute((const void*)k_main, hipFuncAttributeMaxDynamicSharedMemorySize, 160000);
    k_main<<<256, 512, 160000, stream>>>(p);
}
```
